```python
import torch

def _softmax(x, axis=-1):
    return torch.softmax(x, dim=axis)
def _gelu(x):
    return torch.nn.functional.gelu(x, approximate="tanh")
def _silu(x):
    return torch.nn.functional.silu(x)
def _sigmoid(x):
    return torch.sigmoid(x)
def _layernorm(x, axis=-1, eps=1e-5):
    m = x.mean(dim=axis, keepdim=True)
    v = x.var(dim=axis, keepdim=True, unbiased=False)
    return (x - m) / torch.sqrt(v + eps)
def _scatter_add(tgt, idx, src, axis=0):
    return tgt.clone().index_add_(axis, idx, src)
def _segment_sum(src, seg, n):
    out = torch.zeros((n,) + src.shape[1:], dtype=src.dtype)
    return out.index_add_(0, seg, src)
def _pad(x, pw):
    flat = tuple(v for pair in reversed(pw) for v in pair)
    return torch.nn.functional.pad(x, flat)

kernel_name = 'interleave_layers__linear_attn_block__glu_gated_reglu__prompt'

def setup_inputs():

    _w = (lambda a: (lambda t: t.to(torch.bfloat16) if t.is_floating_point() else t)(torch.as_tensor(a)))
    def _A_setup():
        B, S, D = 1, 2048, 512
        return {
            "x": _w((lambda *s: torch.randn(*s, dtype=torch.bfloat16) * 0.05)(B, S, D)),
            "wqkv": _w((lambda *s: torch.randn(*s, dtype=torch.bfloat16) * 0.05)(D, 3 * D)),
            "wo": _w((lambda *s: torch.randn(*s, dtype=torch.bfloat16) * 0.05)(D, D)),
        }
    _A_in = _A_setup()

    def _B_setup():
        B, S, D, F = 1, 2048, 512, 1376
        return {
            "x": _w((lambda *s: torch.randn(*s, dtype=torch.bfloat16) * 0.05)(B, S, D)),
            "W_gate": _w((lambda *s: torch.randn(*s, dtype=torch.bfloat16) * 0.4)(D, F) * (2.0 / D) ** 0.5),
            "W_up": _w((lambda *s: torch.randn(*s, dtype=torch.bfloat16) * 0.4)(D, F) * (2.0 / D) ** 0.5),
            "W_down": _w((lambda *s: torch.randn(*s, dtype=torch.bfloat16) * 0.4)(F, D) * (1.0 / F) ** 0.5),
        }
    _B_in = _B_setup()

    return {
        "A_x": _A_in["x"],
        "A_wqkv": _A_in["wqkv"],
        "A_wo": _A_in["wo"],
        "B_x": _B_in["x"],
        "B_W_gate": _B_in["W_gate"],
        "B_W_up": _B_in["W_up"],
        "B_W_down": _B_in["W_down"],
    }

def reference(A_x, A_wqkv, A_wo, B_x, B_W_gate, B_W_up, B_W_down):
    def _A_ref(x, wqkv, wo):
        B, S, D = x.shape
        H = 32; Dh = D // H
        qkv = (lambda x, *d: x.permute(*d))((x @ wqkv).reshape(B, S, 3, H, Dh), 2, 0, 3, 1, 4)
        q, k, v = qkv[0], qkv[1], qkv[2]
        phi = lambda t: torch.where(t > 0, t + 1, torch.exp(t)) + 1e-6
        pq, pk = phi(q), phi(k)
        kv = torch.einsum("bhsd,bhse->bhde", pk, v)
        z = torch.einsum("bhsd->bhd", pk)
        num = torch.einsum("bhqd,bhde->bhqe", pq, kv)
        den = torch.einsum("bhqd,bhd->bhq", pq, z)[..., None] + 1e-6
        o = num / den
        return (lambda x, *d: x.permute(*d))(o, 0, 2, 1, 3).reshape(B, S, D) @ wo

    def _B_ref(x, W_gate, W_up, W_down):
        gate = torch.einsum("bsd,df->bsf", x, W_gate)
        up = torch.einsum("bsd,df->bsf", x, W_up)
        gate = gate * (lambda x, dt: x.to(dt))(gate > 0, gate.dtype)
        h = gate * up
        return torch.einsum("bsf,fd->bsd", h, W_down)

    x = A_x
    x = x + _A_ref(_layernorm(x, axis=-1), A_wqkv, A_wo)
    x = x + _B_ref(_layernorm(x, axis=-1), B_W_gate, B_W_up, B_W_down)
    x = x + _A_ref(_layernorm(x, axis=-1), A_wqkv, A_wo)
    x = x + _B_ref(_layernorm(x, axis=-1), B_W_gate, B_W_up, B_W_down)
    return x
```

```cpp
#include <hip/hip_runtime.h>
#include <hip/hip_bf16.h>
typedef __hip_bfloat16 bf16;
constexpr int S=2048, D=512, H=32, DH=16, F=1376;
__device__ inline float b2f(bf16 x){return __bfloat162float(x);}
__device__ inline bf16 f2b(float x){return __float2bfloat16(x);}

__global__ void k_ln(const bf16* x, bf16* y){
  int row=blockIdx.x; int t=threadIdx.x;
  float v[8]; float s=0;
  for(int j=0;j<8;j++){v[j]=b2f(x[row*D+t*8+j]); s+=v[j];}
  for(int o=32;o;o>>=1) s+=__shfl_xor(s,o);
  float m=s/D; float q=0;
  for(int j=0;j<8;j++){float d=v[j]-m; q+=d*d;}
  for(int o=32;o;o>>=1) q+=__shfl_xor(q,o);
  float r=rsqrtf(q/D+1e-5f);
  for(int j=0;j<8;j++) y[row*D+t*8+j]=f2b((v[j]-m)*r);
}
__global__ void k_gemm(const bf16* A,const bf16* W,bf16* C,const bf16* res,int M,int N,int K,int mode){
  __shared__ float As[16][17], Ws[16][17];
  int tx=threadIdx.x&15, ty=threadIdx.x>>4;
  int row=blockIdx.y*16+ty, col=blockIdx.x*16+tx;
  float acc=0;
  for(int k0=0;k0<K;k0+=16){
    As[ty][tx]=b2f(A[(size_t)row*K+k0+tx]);
    Ws[ty][tx]=b2f(W[(size_t)(k0+ty)*N+col]);
    __syncthreads();
    for(int k=0;k<16;k++) acc+=As[ty][k]*Ws[k][tx];
    __syncthreads();
  }
  if(mode==0) C[(size_t)row*N+col]=f2b(acc);
  else C[(size_t)row*N+col]=f2b(b2f(res[(size_t)row*N+col])+b2f(f2b(acc)));
}
__device__ inline float phi(float t){ float r = t>0 ? t+1.f : __expf(t); return b2f(f2b(r+1e-6f)); }
__global__ void k_kv(const bf16* qkv,float* kv,float* z){
  int h=blockIdx.x; int d=threadIdx.x>>4, e=threadIdx.x&15;
  float acc=0, zz=0;
  for(int s=0;s<S;s++){
    float pk=phi(b2f(qkv[(size_t)s*3*D+D+h*16+d]));
    float v=b2f(qkv[(size_t)s*3*D+2*D+h*16+e]);
    acc+=pk*v; zz+=pk;
  }
  kv[h*256+d*16+e]=b2f(f2b(acc));
  if(e==0) z[h*16+d]=b2f(f2b(zz));
}
__global__ void k_o(const bf16* qkv,const float* kv,const float* z,bf16* o){
  int s=blockIdx.x; int c=threadIdx.x; int h=c>>4,e=c&15;
  float num=0,den=0;
  for(int d=0;d<16;d++){ float pq=phi(b2f(qkv[(size_t)s*3*D+h*16+d])); num+=pq*kv[h*256+d*16+e]; den+=pq*z[h*16+d]; }
  o[s*D+c]=f2b(num/(den+1e-6f));
}
__global__ void k_glu(const bf16* g,const bf16* u,bf16* h,int n){
  int i=blockIdx.x*256+threadIdx.x; if(i<n){ float gg=b2f(g[i]); gg=gg>0?gg:0; h[i]=f2b(gg*b2f(u[i])); }
}
extern "C" void kernel_launch(void* const* d_in, const int* in_sizes, int n_in,
                              void* d_out, int out_size, void* d_ws, size_t ws_size,
                              hipStream_t stream) {
  const bf16* x=(const bf16*)d_in[0]; const bf16* wqkv=(const bf16*)d_in[1]; const bf16* wo=(const bf16*)d_in[2];
  const bf16* wg=(const bf16*)d_in[4]; const bf16* wu=(const bf16*)d_in[5]; const bf16* wd=(const bf16*)d_in[6];
  char* ws=(char*)d_ws;
  bf16* ln=(bf16*)ws; bf16* qkv=(bf16*)(ws+(4<<20)); float* kv=(float*)(ws+(12<<20)); float* z=(float*)(ws+(13<<20));
  bf16* o=(bf16*)(ws+(14<<20)); bf16* x1=(bf16*)(ws+(18<<20)); bf16* g=(bf16*)(ws+(22<<20)); bf16* u=(bf16*)(ws+(30<<20));
  bf16* hh=(bf16*)(ws+(38<<20)); bf16* x2=(bf16*)(ws+(46<<20));
  const bf16* cur=x;
  for(int l=0;l<2;l++){
    k_ln<<<S,64,0,stream>>>(cur,ln);
    k_gemm<<<dim3(3*D/16,S/16),256,0,stream>>>(ln,wqkv,qkv,nullptr,S,3*D,D,0);
    k_kv<<<H,256,0,stream>>>(qkv,kv,z);
    k_o<<<S,512,0,stream>>>(qkv,kv,z,o);
    k_gemm<<<dim3(D/16,S/16),256,0,stream>>>(o,wo,x1,cur,S,D,D,1);
    k_ln<<<S,64,0,stream>>>(x1,ln);
    k_gemm<<<dim3(F/16,S/16),256,0,stream>>>(ln,wg,g,nullptr,S,F,D,0);
    k_gemm<<<dim3(F/16,S/16),256,0,stream>>>(ln,wu,u,nullptr,S,F,D,0);
    k_glu<<<(S*F+255)/256,256,0,stream>>>(g,u,hh,S*F);
    bf16* dst = l==1 ? (bf16*)d_out : x2;
    k_gemm<<<dim3(D/16,S/16),256,0,stream>>>(hh,wd,dst,x1,S,D,F,1);
    cur=x2;
  }
}
```

```cpp
#include <hip/hip_runtime.h>
#include <stdint.h>

#define GAS __attribute__((address_space(1)))
#define LAS __attribute__((address_space(3)))
#define RLX_AGENT __ATOMIC_RELAXED, __HIP_MEMORY_SCOPE_AGENT
typedef unsigned v4u __attribute__((ext_vector_type(4)));
typedef unsigned v2u __attribute__((ext_vector_type(2)));
typedef float f32x4 __attribute__((ext_vector_type(4)));
typedef float f32x16 __attribute__((ext_vector_type(16)));
typedef short s16x8 __attribute__((ext_vector_type(8)));
typedef short s16x4 __attribute__((ext_vector_type(4)));
typedef __bf16 bf16x2 __attribute__((ext_vector_type(2)));
typedef unsigned short u16;
typedef unsigned long long u64;

#define STAMP 0
#define NWF 4
#define AWIN 12

constexpr int KB = 1024;
constexpr int WS_WT1 = 64 * KB;
constexpr int WS_WT2 = WS_WT1 + 16 * 96 * KB;
constexpr int WS_WT3 = WS_WT2 + 8 * 64 * KB;
constexpr int WS_WT4 = WS_WT3 + 43 * 64 * KB;
constexpr int WS_CSP = WS_WT4 + 8 * 172 * KB + 4 * KB;
constexpr int WS_KVP = WS_CSP + 43 * 4 * KB;
constexpr int WS_ZP = WS_KVP + 2 * 32 * 16 * KB;
constexpr int WS_ST = WS_ZP + 2 * 32 * KB;
constexpr int WS_O = WS_ST + 3 * 256 * KB;
constexpr int WS_X = WS_O + 2 * 2048 * KB;
constexpr int WS_H = WS_X + 3 * 2048 * KB;
constexpr int WS_DBG = WS_H + 2 * 64 * 86 * KB;
constexpr int WS_END = WS_DBG + 256 * 2 * KB;
constexpr int CL_E = 0, CL_HELLO = 1, CL_HC = 64, CL_PT2 = 80, CL_PT = 112;

constexpr int SLOT = 16384, NSLOT = 8;
constexpr int NSTAMP = 36;
constexpr int L_FLG = 0;
constexpr int L_ZF = L_FLG + 128;
constexpr int L_RS = L_ZF + 64;
constexpr int L_CS1 = L_RS + 512;
constexpr int L_CS3 = L_CS1 + 768;
constexpr int L_KVF = L_CS3 + 1536;
constexpr int L_STAMP = L_KVF + 1024;
constexpr int L_KVS = L_STAMP + 1152;
constexpr int L_XCH = 16384;
constexpr int L_RING = 32768;
constexpr int L_END = L_RING + NSLOT * SLOT;
static_assert(L_KVS + 4 * 2176 <= L_XCH && L_END == 163840 && (L_KVS & 15) == 0, "lds");
enum { F_FULL = 0, F_FREE = 4, F_FREEH = 8, F_ARR = 10, F_GO = 11, F_KFL = 12, F_KVR = 16, F_XF = 20, F_CS1R = 24, F_CS3R = 26, F_HELLO = 27, F_PTOK = 28 };
constexpr unsigned SPIN_CAP = 1u << 17;

__device__ __forceinline__ int gsplit(int i) { return (43 * i) >> 4; }
__device__ __forceinline__ f32x16 mfma(v4u a, v4u b, f32x16 c) {
  return __builtin_amdgcn_mfma_f32_32x32x16_bf16(__builtin_bit_cast(s16x8, a), __builtin_bit_cast(s16x8, b), c, 0, 0, 0);
}
__device__ __forceinline__ unsigned pack2(float a, float b) {
  bf16x2 v = {(__bf16)a, (__bf16)b};
  return __builtin_bit_cast(unsigned, v);
}
__device__ __forceinline__ float u2f(unsigned u) { return __builtin_bit_cast(float, u); }
__device__ __forceinline__ unsigned f2u(float f) { return __builtin_bit_cast(unsigned, f); }
__device__ __forceinline__ float blo(unsigned u) { return u2f(u << 16); }
__device__ __forceinline__ float bhi(unsigned u) { return u2f(u & 0xffff0000u); }
__device__ __forceinline__ float relu(float x) { return u2f((unsigned)max((int)f2u(x), 0)); }
__device__ __forceinline__ float dot2(unsigned a, unsigned b, float acc) {
  return __builtin_amdgcn_fdot2_f32_bf16(__builtin_bit_cast(bf16x2, a), __builtin_bit_cast(bf16x2, b), acc, false);
}
constexpr unsigned ONES2 = 0x3f803f80u;
__device__ __forceinline__ float red_h(float v) {
  auto s = __builtin_amdgcn_permlane32_swap(f2u(v), f2u(v), false, false);
  return u2f((unsigned)s[0]) + u2f((unsigned)s[1]);
}
__device__ __forceinline__ float red_g(float v) {
  auto r = __builtin_amdgcn_permlane16_swap(f2u(v), f2u(v), false, false);
  return red_h(u2f((unsigned)r[0]) + u2f((unsigned)r[1]));
}
template <int CTRL> __device__ __forceinline__ float dpp(float v) {
  return u2f(__builtin_amdgcn_update_dpp(0, (int)f2u(v), CTRL, 0xf, 0xf, false));
}
__device__ __forceinline__ float phi_s(float a, float r, float rl) {
  const float e = __builtin_amdgcn_fmed3f(__builtin_amdgcn_exp2f(a * rl), 0.f, 1.f);
  return fmaxf(fmaf(a, r, 1.f), e) + 1e-6f;
}
__device__ __forceinline__ void split2(float v, unsigned& hi, unsigned& lo) {
  hi = pack2(v, 0.f) & 0xffffu;
  lo = pack2(v - blo(hi), 0.f) & 0xffffu;
}
#define MEMBAR() asm volatile("" ::: "memory")
#define SB() __builtin_amdgcn_sched_barrier(0)
#define VMWAIT(n) asm volatile("s_waitcnt vmcnt(" #n ")" ::: "memory")
__device__ __forceinline__ v4u tr_frag(LAS char* p) {
  v2u lo = __builtin_bit_cast(v2u, __builtin_amdgcn_ds_read_tr16_b64_v4i16((LAS s16x4*)p));
  v2u hi = __builtin_bit_cast(v2u, __builtin_amdgcn_ds_read_tr16_b64_v4i16((LAS s16x4*)(p + 256)));
  return v4u{lo.x, lo.y, hi.x, hi.y};
}
__device__ __forceinline__ void glds(const void* sbase, unsigned voff, unsigned ldst) {
  asm volatile("s_mov_b32 m0, %2\n\ts_nop 0\n\tglobal_load_lds_dwordx4 %0, %1" ::"v"(voff), "s"(sbase), "s"(ldst) : "memory");
}
__device__ __forceinline__ void glds4(const void* sbase, unsigned voff, unsigned ldst) {
  asm volatile(
      "s_mov_b32 m0, %2\n\ts_nop 0\n\tglobal_load_lds_dwordx4 %0, %1\n\tglobal_load_lds_dwordx4 %0, %1 offset:1024\n\t"
      "global_load_lds_dwordx4 %0, %1 offset:2048\n\tglobal_load_lds_dwordx4 %0, %1 offset:3072" ::"v"(voff), "s"(sbase), "s"(ldst)
      : "memory");
}

struct Ctx {
  LAS char* L;
  __amdgpu_buffer_rsrc_t rs;
  GAS unsigned* ctrl;
  int lane, wave, cl;
  int seq, known;
  int vb0, vb1;
  int nst;
};
__device__ __forceinline__ volatile LAS unsigned* flg(Ctx& c, int i) { return (volatile LAS unsigned*)(c.L + L_FLG) + i; }
__device__ __forceinline__ void stamp0(Ctx& c) {
}
__device__ __forceinline__ void stamp(Ctx& c) { if (STAMP == 1) stamp0(c); }
__device__ __forceinline__ void stamp2(Ctx& c) { if (STAMP == 2) stamp0(c); }
template <int SLP> __device__ __forceinline__ void wait_lds(Ctx& c, int i, unsigned val) {
  for (unsigned sp = 0; sp < SPIN_CAP; ++sp) {
    if ((unsigned)__builtin_amdgcn_readfirstlane(*flg(c, i)) >= val) break;
    if (SLP) __builtin_amdgcn_s_sleep(SLP);
  }
  MEMBAR();
}
template <int SLP> __device__ __forceinline__ void wait_lds2(Ctx& c, int i, unsigned val) {
  for (unsigned sp = 0; sp < SPIN_CAP; ++sp) {
    v2u f = *(volatile LAS v2u*)flg(c, i);
    if ((unsigned)__builtin_amdgcn_readfirstlane(min(f.x, f.y)) >= val) break;
    if (SLP) __builtin_amdgcn_s_sleep(SLP);
  }
  MEMBAR();
}
template <int SLP> __device__ __forceinline__ void wait_lds4(Ctx& c, int i, unsigned val) {
  for (unsigned sp = 0; sp < SPIN_CAP; ++sp) {
    v4u f = *(volatile LAS v4u*)flg(c, i);
    if ((unsigned)__builtin_amdgcn_readfirstlane(min(min(f.x, f.y), min(f.z, f.w))) >= val) break;
    if (SLP) __builtin_amdgcn_s_sleep(SLP);
  }
  MEMBAR();
}
__device__ __forceinline__ void poll_ge(Ctx& c, int line, unsigned val) {
  for (unsigned sp = 0; sp < SPIN_CAP; ++sp) {
    unsigned v = __hip_atomic_load(c.ctrl + line * 64, RLX_AGENT);
    if ((unsigned)__builtin_amdgcn_readfirstlane(v) >= val) break;
  }
  __builtin_amdgcn_fence(__ATOMIC_ACQUIRE, "wavefront");
  MEMBAR();
}
__device__ __forceinline__ int ring_acquire(Ctx& c, int seq) {
  if (c.known <= seq) {
    for (unsigned sp = 0; sp < SPIN_CAP; ++sp) {
      v2u f = *(volatile LAS v2u*)flg(c, F_FULL);
      c.known = __builtin_amdgcn_readfirstlane(min(f.x, f.y));
      if (c.known > seq) break;
    }
  }
  MEMBAR();
  return L_RING + (seq & (NSLOT - 1)) * SLOT;
}
__device__ __forceinline__ v4u ld_ws(Ctx& c, int voff, int soff) { return __builtin_amdgcn_raw_buffer_load_b128(c.rs, voff, soff, 16); }
__device__ __forceinline__ void st_ws(Ctx& c, v4u v, int voff, int soff) {
  __builtin_amdgcn_raw_buffer_store_b128(v, c.rs, voff, soff, 16);
  asm volatile("s_nop 1" ::"v"(v) : "memory");
}
__device__ __forceinline__ int opq(int v) { asm volatile("" : "+s"(v)); return v; }
__device__ __forceinline__ int opqv(int v) { asm volatile("" : "+v"(v)); return v; }
__device__ __forceinline__ v4u ld_fr(Ctx& c, int lv, int sb, int i) { return ld_ws(c, lv + (i & 3) * KB, sb + (i >> 2) * 4 * KB); }

template <int MODE> __device__ __forceinline__ v4u wread(Ctx& c, int f, int lb0, int lb1, bool publish) {
  constexpr int FPS = MODE == 1 ? 8 : 16;
  const int i = f % FPS;
  if (i == 0) { const int sb = ring_acquire(c, c.seq); c.vb0 = lb0 + sb; if (MODE == 1 || MODE == 2) c.vb1 = lb1 + sb; }
  v4u r;
  if (MODE == 0) r = *(LAS v4u*)(c.L + c.vb0 + i * KB);
  else if (MODE == 1) r = *(LAS v4u*)(c.L + ((i & 1) ? c.vb1 : c.vb0) + (i >> 1) * 4 * KB);
  else if (MODE == 2) r = tr_frag(c.L + ((i & 1) ? c.vb1 : c.vb0) + i * KB);
  else r = tr_frag(c.L + c.vb0 + i * KB);
  return r;
}
__device__ __forceinline__ void ring_publish(Ctx& c) { MEMBAR(); *flg(c, F_FREE + c.wave) = (unsigned)c.seq; MEMBAR(); }
template <int MODE> __device__ __forceinline__ void wpre(Ctx& c, v4u (&wf)[NWF], int lb0, int lb1) {
#pragma unroll
  for (int f = 0; f < NWF; ++f) wf[f] = wread<MODE>(c, f, lb0, lb1, false);
  if (MODE == 1 && NWF == 8) c.seq++;
}
struct NoSide { __device__ __forceinline__ void operator()(int) const {} };
template <int NF, int TPS, int MODE, int NEXT, bool XA, int RELF, int EARLY, int AM, class SIDE>
__device__ __forceinline__ void gemm(Ctx& c, v4u (&a)[32], f32x16 (&acc)[2], v4u (&wf)[NWF], int lb0, int lb1, bool chain, SIDE side) {
  constexpr int FPS = MODE == 1 ? 8 : 16;
  static_assert(NEXT < 0 || (NF % NWF == 0 && NF % FPS == 0), "chain");
#pragma unroll
  for (int f = 0; f < NF; ++f) {
    const int s = f / TPS, t = f % TPS;
    const v4u x = a[s & AM], wv = wf[f % NWF];
    acc[t] = XA ? mfma(x, wv, acc[t]) : mfma(wv, x, acc[t]);
    SB();
    const int fn = f + NWF;
    if (fn < NF) {
      wf[f % NWF] = wread<MODE>(c, fn, lb0, lb1, false);
      if (fn % FPS == FPS - 1 || fn == NF - 1) { c.seq++; if (f >= RELF || fn / FPS < EARLY) ring_publish(c); }
      else if (f == RELF && RELF > 0) ring_publish(c);
    } else if (NEXT >= 0) {
      if (chain) wf[f % NWF] = wread<(NEXT < 0 ? 0 : NEXT)>(c, fn - NF, lb0, lb1, false);
    }
    side(f);
    SB();
  }
}
__device__ __forceinline__ f32x16 zero16() { return f32x16{0.f, 0.f, 0.f, 0.f, 0.f, 0.f, 0.f, 0.f, 0.f, 0.f, 0.f, 0.f, 0.f, 0.f, 0.f, 0.f}; }
__device__ __forceinline__ void hop(Ctx& c, int n) {
  VMWAIT(0);
  unsigned old = 0;
  if (c.lane == 0) old = __hip_atomic_fetch_add((LAS unsigned*)flg(c, F_ARR), 1u, __ATOMIC_RELAXED, __HIP_MEMORY_SCOPE_WORKGROUP);
  old = __builtin_amdgcn_readfirstlane(old);
  if ((old & 3u) == 3u) {
    if (c.lane == 0) __hip_atomic_fetch_add(c.ctrl + (CL_HC + c.cl) * 64, 1u, RLX_AGENT);
    poll_ge(c, CL_HC + c.cl, 16u * (n + 1));
    *flg(c, F_GO) = (unsigned)(n + 1);
    MEMBAR();
  } else {
    wait_lds<0>(c, F_GO, (unsigned)(n + 1));
  }
}
__device__ __forceinline__ u64 mix64(u64 z) {
  z += 0x9E3779B97F4A7C15ull;
  z = (z ^ (z >> 30)) * 0xBF58476D1CE4E5B9ull;
  z = (z ^ (z >> 27)) * 0x94D049BB133111EBull;
  return z ^ (z >> 31);
}
__device__ __forceinline__ float csum(v4u f, float s) { return dot2(f.w, ONES2, dot2(f.z, ONES2, dot2(f.y, ONES2, dot2(f.x, ONES2, s)))); }
__device__ __forceinline__ v2u cs_operand(float s) { unsigned hi, lo; split2(s, hi, lo); return v2u{hi | (hi << 16), lo}; }

__global__ __launch_bounds__(512, 2) void fused_fwd(const u16* __restrict__ x, const u16* __restrict__ wqkv, const u16* __restrict__ wo,
                                                    const u16* __restrict__ wg, const u16* __restrict__ wu, const u16* __restrict__ wd,
                                                    u16* __restrict__ out, char* __restrict__ ws) {
  __shared__ __attribute__((aligned(16))) char smem[L_END];
  Ctx c;
  c.L = (LAS char*)smem;
  c.rs = __builtin_amdgcn_make_buffer_rsrc((void*)ws, 0, WS_END, 0x00020000);
  c.ctrl = (GAS unsigned*)ws;
  const int lane = threadIdx.x & 63, wave = __builtin_amdgcn_readfirstlane(threadIdx.x >> 6);
  const int b = blockIdx.x, xc = b & 7, j = b >> 3;
  const int cl = (xc >> 1) * 4 + (j >> 3), m = (xc & 1) * 8 + (j & 7);
  const int rh = (m >> 2) & 1, cg = (m >> 3) * 4 + (m & 3);
  const int g0 = gsplit(m), ng = gsplit(m + 1) - g0;
  c.lane = lane; c.wave = wave; c.cl = cl; c.seq = 0; c.known = 0; c.nst = 0; c.vb0 = 0; c.vb1 = 0;
  const int h = lane >> 5, l31 = lane & 31;
  LAS char* L = c.L;
  if (wave == 0 && lane < 32) *flg(c, lane) = 0u;
  stamp0(c);
  asm volatile("s_waitcnt lgkmcnt(0)\n\ts_barrier" ::: "memory");
  const int trN = (8 * h + ((lane >> 2) & 3)) * 64 + ((lane >> 4) & 1) * 32 + (lane & 3) * 8;
  const int trS = (8 * h + ((lane >> 2) & 3)) * 64 + ((lane >> 4) & 1) * 32 + (((lane & 1) << 1) | ((lane >> 1) & 1)) * 8;

  if (wave < 4) {
    __builtin_amdgcn_s_setprio(2);
    const int w = wave, mh = w >> 1, kh = w & 1;
    v4u a[32], wf[NWF], xr[2];
    f32x16 acc[2];
    const int lv = lane * 16;
    float rstd;
    v4u mfrag;
    auto set_stats = [&](float sum, float ssq, float& var) {
      const float s = red_h(sum) * (1.f / 512), q = red_h(ssq) * (1.f / 512);
      var = fmaxf(q - s * s, 0.f) + 1e-5f;
      unsigned hi, lo;
      split2(-s, hi, lo);
      mfrag = h == 0 ? v4u{hi | (lo << 16), hi, 0u, 0u} : v4u{0u, 0u, 0u, 0u};
    };
    auto cs_frag = [&](int off) { const v2u t = *(LAS v2u*)(L + off + l31 * 8); return v4u{t.x, t.y, 0u, 0u}; };
    {
      const __amdgpu_buffer_rsrc_t rx = __builtin_amdgcn_make_buffer_rsrc((void*)x, 0, 2048 * 1024, 0x00020000);
      const int xv = (lane >> 3) * 1024 + (lane & 7) * 16, xs = (cl * 128 + w * 32) * 1024;
      auto ldx = [&](int i) { a[i] = __builtin_amdgcn_raw_buffer_load_b128(rx, xv + (i >> 2) * 128, xs + (i & 3) * 8192, 0); };
#pragma unroll
      for (int i = 0; i < 16; ++i) ldx(i);
      SB();
      LAS char* sc = L + L_XCH + w * 4096;
      const int r8 = lane >> 3;
      const int wo = r8 * 128 + (((lane & 7) ^ (r8 >> 1)) * 16);
      const int ro = l31 * 128 + ((h ^ ((l31 >> 1) & 7)) * 16);
      float sum = 0.f, ssq = 0.f;
      auto xpose = [&](int kp) {
#pragma unroll
        for (int q = 0; q < 4; ++q) *(LAS v4u*)(sc + ((wo + q * 1024) ^ ((q & 1) * 64))) = a[kp * 4 + q];
        MEMBAR();
#pragma unroll
        for (int q = 0; q < 4; ++q) a[kp * 4 + q] = *(LAS v4u*)(sc + (ro ^ (q * 32)));
        MEMBAR();
      };
      xpose(0);
      wpre<2>(c, wf, trN, trS);
      acc[0] = zero16(); acc[1] = zero16();
      auto side = [&](int f) {
        const int s = f >> 1;
        if ((f & 7) == 0 && s / 4 + 1 < 8) xpose(s / 4 + 1);
        if ((f & 1) == 1 && s + 16 < 32) ldx(s + 16);
        if ((f & 1) == 0) {
#pragma unroll
          for (int e = 0; e < 4; ++e) sum = dot2(a[s][e], ONES2, sum);
        } else {
#pragma unroll
          for (int e = 0; e < 4; ++e) ssq = dot2(a[s][e], a[s][e], ssq);
        }
      };
      gemm<64, 2, 2, 3, true, 0, 0, 31>(c, a, acc, wf, trN, trS, true, side);
      float var;
      set_stats(sum, ssq, var);
      rstd = __builtin_amdgcn_rsqf(var);
      const int xq = l31 * 1024 + h * 16, xqs = (cl * 128 + rh * 64 + mh * 32) * 1024 + cg * 128 + kh * 64;
      xr[0] = __builtin_amdgcn_raw_buffer_load_b128(rx, xq, xqs, 0);
      xr[1] = __builtin_amdgcn_raw_buffer_load_b128(rx, xq + 32, xqs, 0);
      if (h == 0) *(LAS float*)(L + L_RS + w * 128 + l31 * 4) = rstd;
      wait_lds2<0>(c, F_CS1R, 1u);
    }
#pragma unroll 1
    for (int layer = 0; layer < 2; ++layer) {
      stamp(c);
      {
        acc[0] = mfma(mfrag, cs_frag(L_CS1), acc[0]);
        acc[1] = mfma(mfrag, cs_frag(L_CS1 + 256), acc[1]);
        v4u pk[2], vv[2];
#pragma unroll
        for (int q = 0; q < 4; ++q) {
          const f32x4 r = *(LAS f32x4*)(L + L_RS + w * 128 + (8 * q + 4 * h) * 4);
          float p[4], v[4];
#pragma unroll
          for (int i = 0; i < 4; ++i) { p[i] = phi_s(acc[0][4 * q + i], r[i], r[i] * 1.44269504f); v[i] = acc[1][4 * q + i] * r[i]; }
          pk[q >> 1][(q & 1) * 2] = pack2(p[0], p[1]); pk[q >> 1][(q & 1) * 2 + 1] = pack2(p[2], p[3]);
          vv[q >> 1][(q & 1) * 2] = pack2(v[0], v[1]); vv[q >> 1][(q & 1) * 2 + 1] = pack2(v[2], v[3]);
        }
        f32x16 kv = mfma(pk[0], vv[0], zero16());
        kv = mfma(pk[1], vv[1], kv);
        const float z = red_h(csum(pk[1], csum(pk[0], 0.f)));
        LAS char* ks = L + L_KVS + w * 2176;
        if (h == 0) *(LAS float*)(ks + 2048 + l31 * 4) = z;
        if (lane & 16) {
          *(LAS f32x4*)(ks + lv) = f32x4{kv[8], kv[9], kv[10], kv[11]};
          *(LAS f32x4*)(ks + 1024 + lv) = f32x4{kv[12], kv[13], kv[14], kv[15]};
          MEMBAR();
        } else {
          MEMBAR();
          *(LAS f32x4*)(ks + lv) = f32x4{kv[0], kv[1], kv[2], kv[3]};
          *(LAS f32x4*)(ks + 1024 + lv) = f32x4{kv[4], kv[5], kv[6], kv[7]};
        }
        MEMBAR();
        *flg(c, F_KFL + w) = (unsigned)(layer + 1);
        MEMBAR();
      }
      stamp(c);
      acc[0] = zero16();
      if (layer == 0) gemm<32, 1, 3, -1, false, 0, 0, 31>(c, a, acc, wf, trN, trN, false, NoSide());
      else gemm<32, 1, 0, -1, false, 0, 0, 31>(c, a, acc, wf, lv, lv, false, NoSide());
      const int kb0 = lv + kh * 3072, kb1 = lv + kh * 2048 + (kh ^ 1) * 1024;
      {
        if (layer == 0) wait_lds2<0>(c, F_CS1R, 2u);
        acc[0] = mfma(cs_frag(L_CS1 + 512), mfrag, acc[0]);
        v4u pq[2];
        const float rl = rstd * 1.44269504f;
#pragma unroll
        for (int i = 0; i < 8; ++i) pq[i >> 2][i & 3] = pack2(phi_s(acc[0][2 * i], rstd, rl), phi_s(acc[0][2 * i + 1], rstd, rl));
        asm volatile("" : "+v"(pq[0]), "+v"(pq[1]));
        stamp(c);
        wait_lds2<0>(c, F_KVR, (unsigned)(layer + 1));
        stamp(c);
        const v4u kvf = *(LAS v4u*)(L + L_KVF + lv);
        v2u zf[4];
#pragma unroll
        for (int q = 0; q < 4; ++q) zf[q] = *(LAS v2u*)(L + L_ZF + q * 16 + h * 8);
        const bool up = (lane & 16) != 0;
        const v4u zz = {0u, 0u, 0u, 0u};
        f32x16 num = mfma(up ? zz : kvf, pq[0], zero16());
        num = mfma(up ? kvf : zz, pq[1], num);
        float r[2];
#pragma unroll
        for (int hd = 0; hd < 2; ++hd) {
          const float d = dot2(pq[hd].w, zf[2 * hd + 1].y, dot2(pq[hd].z, zf[2 * hd + 1].x, dot2(pq[hd].y, zf[2 * hd].y, dot2(pq[hd].x, zf[2 * hd].x, 0.f))));
          r[hd] = __builtin_amdgcn_rcpf(red_h(d) + 1e-6f);
        }
        const int ob = opq(WS_O + layer * 2048 * KB + ((cl * 4 + w) * 32 + 2 * m) * KB);
#pragma unroll
        for (int hd = 0; hd < 2; ++hd) {
          v4u o;
#pragma unroll
          for (int i = 0; i < 4; ++i) o[i] = pack2(num[8 * hd + 2 * i] * r[hd], num[8 * hd + 2 * i + 1] * r[hd]);
          st_ws(c, o, lv + hd * KB, ob);
        }
      }
      wpre<1>(c, wf, kb0, kb1);
      auto res_init = [&]() {
#pragma unroll
        for (int i = 0; i < 8; ++i) { acc[0][2 * i] = blo(xr[i >> 2][i & 3]); acc[0][2 * i + 1] = bhi(xr[i >> 2][i & 3]); }
        acc[1] = zero16();
        asm volatile("" : "+v"(acc[0]), "+v"(acc[1]));
      };
      res_init();
      stamp(c);
      hop(c, layer * 4 + 0);
      stamp(c);
      const int rtA = opq(cl * 4 + rh * 2 + mh);
      auto res_epilogue = [&](int xi, bool final_out, unsigned xseq) {
        LAS char* xs = L + L_XCH;
#pragma unroll
        for (int q = 0; q < 4; ++q) *(LAS f32x4*)(xs + w * 4096 + q * 1024 + lv) = f32x4{acc[1][4 * q], acc[1][4 * q + 1], acc[1][4 * q + 2], acc[1][4 * q + 3]};
        MEMBAR();
        *flg(c, F_XF + w) = xseq;
        wait_lds<0>(c, F_XF + (w ^ 1), xseq);
#pragma unroll
        for (int q = 0; q < 4; ++q) {
          const f32x4 o = *(LAS f32x4*)(xs + (w ^ 1) * 4096 + q * 1024 + lv);
          xr[q >> 1][(q & 1) * 2] = pack2(acc[0][4 * q] + o[0], acc[0][4 * q + 1] + o[1]);
          xr[q >> 1][(q & 1) * 2 + 1] = pack2(acc[0][4 * q + 2] + o[2], acc[0][4 * q + 3] + o[3]);
        }
        if (final_out) {
          GAS char* ob = (GAS char*)out + (size_t)(rtA * 32 + l31) * 1024 + cg * 128 + kh * 64 + h * 16;
          *(GAS v4u*)ob = xr[0];
          *(GAS v4u*)(ob + 32) = xr[1];
        } else {
          const int xb = WS_X + xi * 2048 * KB + (rtA * 32 + cg * 4 + kh * 2) * KB;
          st_ws(c, xr[0], lv, xb);
          st_ws(c, xr[1], lv + KB, xb);
          float s = 0.f, q = 0.f;
#pragma unroll
          for (int i = 0; i < 8; ++i) { s = dot2(xr[i >> 2][i & 3], ONES2, s); q = dot2(xr[i >> 2][i & 3], xr[i >> 2][i & 3], q); }
          s = red_h(s); q = red_h(q);
          if (h == 0) __builtin_amdgcn_raw_buffer_store_b64(v2u{f2u(s), f2u(q)}, c.rs, l31 * 16, WS_ST + (xi * 64 + rtA) * 4 * KB + cg * 512 + kh * 8, 16);
        }
      };
      {
        const int ab = WS_O + layer * 2048 * KB + (rtA * 32 + kh) * KB;
#pragma unroll
        for (int i = 0; i < AWIN; ++i) a[i] = ld_fr(c, lv, ab, 2 * i);
        SB();
        auto side = [&](int f) { if ((f & 1) && (f >> 1) + AWIN < 16) a[(f >> 1) + AWIN] = ld_fr(c, lv, ab, 2 * ((f >> 1) + AWIN)); };
        gemm<32, 2, 1, -1, false, 2 * (16 - AWIN), 0, 31>(c, a, acc, wf, kb0, kb1, false, side);
        stamp(c);
        res_epilogue(layer * 2, false, (unsigned)(layer * 2 + 1));
      }
      wpre<0>(c, wf, lv, lv);
      stamp(c);
      hop(c, layer * 4 + 1);
      stamp(c);
      const int rtB = opq(cl * 4 + w);
      v4u st[4];
      auto ln_loads = [&](int xi) {
#pragma unroll
        for (int i = 0; i < 4; ++i) a[i] = ld_fr(c, lv, WS_X + xi * 2048 * KB + rtB * 32 * KB, i);
#pragma unroll
        for (int q = 0; q < 4; ++q) st[q] = ld_fr(c, lv, WS_ST + (xi * 64 + rtB) * 4 * KB, q);
#pragma unroll
        for (int i = 4; i < AWIN; ++i) a[i] = ld_fr(c, lv, WS_X + xi * 2048 * KB + rtB * 32 * KB, i);
        SB();
      };
      auto ln_stats = [&](float& var) {
        float s = 0.f, q = 0.f;
#pragma unroll
        for (int i = 0; i < 4; ++i) { s += u2f(st[i].x) + u2f(st[i].z); q += u2f(st[i].y) + u2f(st[i].w); }
        set_stats(s, q, var);
      };
      {
        stamp2(c);
        ln_loads(layer * 2);
        float r2;
        const int xb = WS_X + layer * 2 * 2048 * KB + rtB * 32 * KB;
        const int hb = WS_H + layer * 64 * 86 * KB + (rtB * 86 + g0 * 2) * KB;
        auto side0 = [&](int f) {
          if ((f & 1) && (f >> 1) + AWIN < 32) a[(f >> 1) + AWIN] = ld_fr(c, lv, xb, (f >> 1) + AWIN);
          if (f == 44) { float var; ln_stats(var); r2 = __builtin_amdgcn_rcpf(var); }
        };
        auto finish = [&](int gi) {
          acc[0] = mfma(cs_frag(L_CS3 + gi * 512), mfrag, acc[0]);
          acc[1] = mfma(cs_frag(L_CS3 + gi * 512 + 256), mfrag, acc[1]);
#pragma unroll
          for (int s = 0; s < 2; ++s) {
            v4u o;
#pragma unroll
            for (int i = 0; i < 4; ++i) {
              const int e = 8 * s + 2 * i;
              o[i] = pack2(relu(acc[0][e]) * r2 * acc[1][e], relu(acc[0][e + 1]) * r2 * acc[1][e + 1]);
            }
            st_ws(c, o, lv + s * KB, hb + gi * 2 * KB);
          }
        };
        if (layer == 0) wait_lds<0>(c, F_CS3R, 1u);
        stamp2(c);
        acc[0] = zero16(); acc[1] = zero16();
        gemm<64, 2, 0, 0, false, 2 * (32 - AWIN), 0, 31>(c, a, acc, wf, lv, lv, true, side0);
        stamp2(c);
        finish(0);
#pragma unroll 1
        for (int gi = 1; gi < ng; ++gi) {
          stamp2(c);
          acc[0] = zero16(); acc[1] = zero16();
          gemm<64, 2, 0, 0, false, 0, 0, 31>(c, a, acc, wf, lv, lv, gi + 1 < ng, NoSide());
          stamp2(c);
          finish(gi);
        }
      }
      wpre<1>(c, wf, kb0, kb1);
      res_init();
      stamp(c); stamp2(c);
      hop(c, layer * 4 + 2);
      stamp(c); stamp2(c);
      {
        const int ab = WS_H + layer * 64 * 86 * KB + (rtA * 86 + kh) * KB;
#pragma unroll
        for (int i = 0; i < AWIN; ++i) a[i] = ld_fr(c, lv, ab, 2 * i);
        SB();
        stamp2(c);
        auto side = [&](int f) { if ((f & 1) && (f >> 1) + AWIN < 43) a[((f >> 1) + AWIN) & 15] = ld_fr(c, lv, ab, 2 * ((f >> 1) + AWIN)); };
        gemm<86, 2, 1, -1, false, 2 * (43 - AWIN), 3, 15>(c, a, acc, wf, kb0, kb1, false, side);
        stamp(c); stamp2(c);
        res_epilogue(1, layer == 1, (unsigned)(layer * 2 + 2));
      }
      if (layer == 1) break;
      wpre<0>(c, wf, lv, lv);
      stamp(c);
      hop(c, 3);
      stamp(c);
      {
        ln_loads(1);
        const int xb = WS_X + 2048 * KB + rtB * 32 * KB;
        auto side = [&](int f) {
          if ((f & 1) && (f >> 1) + AWIN < 32) a[(f >> 1) + AWIN] = ld_fr(c, lv, xb, (f >> 1) + AWIN);
          if (f == 44) {
            float var; ln_stats(var); rstd = __builtin_amdgcn_rsqf(var);
            if (h == 0) *(LAS float*)(L + L_RS + w * 128 + l31 * 4) = rstd;
          }
        };
        acc[0] = zero16(); acc[1] = zero16();
        gemm<64, 2, 0, 0, true, 2 * (32 - AWIN), 0, 31>(c, a, acc, wf, lv, lv, true, side);
      }
    }
  } else if (wave < 6) {
    const int lw = wave - 4;
    const int lv = lane * 16;
    const int k0 = gsplit(cl), nk = gsplit(cl + 1) - k0;
    const int pts = L_RING + (6 + lw) * SLOT;
    volatile LAS unsigned* full = flg(c, F_FULL + lw);
    {
      const unsigned r = lane >> 2, sw = (lane & 3) * 16;
      const unsigned vo1 = r * 3072 + sw, vo3 = r * 2752 + sw, vo2 = r * 1024 + sw;
      const char* kvb = (const char*)wqkv + (1 + lw) * 1024 + m * 64;
#pragma unroll 1
      for (int s = 0; s < 32; ++s) glds(kvb + (size_t)(s * 16) * 3072, vo1, L_RING + s * 2048 + lw * 1024);
      const char* qb = (const char*)wqkv + m * 64 + (size_t)(lw * 16) * 3072;
      VMWAIT(24); *full = 1u;
#pragma unroll 1
      for (int s = 0; s < 8; ++s) glds(qb + (size_t)(s * 32) * 3072, vo1, L_RING + 65536 + s * 2048 + lw * 1024);
      VMWAIT(24); *full = 2u;
#pragma unroll 1
      for (int s = 8; s < 16; ++s) glds(qb + (size_t)(s * 32) * 3072, vo1, L_RING + 65536 + s * 2048 + lw * 1024);
      VMWAIT(24); *full = 3u;
      const int colb = cg * 128 + rh * 64;
      const char* j3 = lw ? (const char*)wd + (size_t)((k0 + 1) * 32) * 1024 + colb : (const char*)wo + (size_t)(cl * 32) * 1024 + colb;
      const char* j4 = (const char*)wd + (size_t)((lw ? k0 + nk - 1 : k0) * 32) * 1024 + colb;
      glds(j3, vo2, pts + 3 * 2048); glds(j3 + 16 * 1024, vo2, pts + 3 * 2048 + 1024);
      glds(j4, vo2, pts + 4 * 2048); glds(j4 + 16 * 1024, vo2, pts + 4 * 2048 + 1024);
      const char* w3 = (const char*)(lw ? wu : wg) + (size_t)(cl * 32) * 2752;
#pragma unroll
      for (int i = 0; i < 3; ++i) {
        const int aa = g0 + min(i, ng - 1);
        glds(w3 + aa * 64, vo3, pts + i * 2048);
        glds(w3 + aa * 64 + 16 * 2752, vo3, pts + i * 2048 + 1024);
      }
    }
    VMWAIT(26); *full = 4u; VMWAIT(18); *full = 5u; VMWAIT(10); *full = 6u;
    VMWAIT(0);
    stamp(c);
    {
#pragma unroll
      for (int s = 0; s < 2; ++s) {
        const int d3 = lw ? WS_WT4 + cg * 172 * KB + (((k0 + 1) * 2 + s) * 2 + rh) * KB : WS_WT2 + cg * 64 * KB + ((cl * 2 + s) * 2 + rh) * KB;
        st_ws(c, tr_frag(L + pts + 3 * 2048 + s * 1024 + trS), lv, d3);
        st_ws(c, tr_frag(L + pts + 4 * 2048 + s * 1024 + trS), lv, WS_WT4 + cg * 172 * KB + (((lw ? k0 + nk - 1 : k0) * 2 + s) * 2 + rh) * KB);
      }
#pragma unroll
      for (int i = 0; i < 3; ++i) {
        const int aa = g0 + min(i, ng - 1);
        const v4u f0 = tr_frag(L + pts + i * 2048 + trS), f1 = tr_frag(L + pts + i * 2048 + 1024 + trS);
        st_ws(c, f0, lv, WS_WT3 + aa * 64 * KB + (cl * 4 + lw) * KB);
        st_ws(c, f1, lv, WS_WT3 + aa * 64 * KB + (cl * 4 + 2 + lw) * KB);
        const float s = red_h(csum(f1, csum(f0, 0.f)));
        if (h == 0) __builtin_amdgcn_raw_buffer_store_b32(f2u(s), c.rs, l31 * 4, WS_CSP + aa * 4 * KB + cl * 256 + lw * 128, 16);
      }
    }
    wait_lds<1>(c, F_HELLO, 1u);
    VMWAIT(0);
    if (lane == 0) __hip_atomic_fetch_add(c.ctrl + (CL_PT + m) * 64, 1u, RLX_AGENT);
    stamp(c);
    poll_ge(c, CL_PT + (lw ? m ^ 4 : m), 32u);
    *flg(c, F_PTOK + lw) = 1u;
    wait_lds2<1>(c, F_PTOK, 1u);
    __builtin_amdgcn_fence(__ATOMIC_ACQUIRE, "agent");
    stamp(c);
    int seq = 6, pub = 6;
#pragma unroll 1
    for (int seg = 1; seg < 8; ++seg) {
      const int ph = seg & 3;
      int src, n;
      if (ph == 0) { src = WS_WT1 + m * 96 * KB; n = 6; }
      else if (ph == 1) { src = WS_WT2 + cg * 64 * KB; n = 4; }
      else if (ph == 2) { src = WS_WT3 + g0 * 64 * KB; n = 4 * ng; }
      else { src = WS_WT4 + cg * 172 * KB; n = 11; }
      if (seg == 3) {
        VMWAIT(0);
        if (pub < seq) { pub = seq; *full = (unsigned)pub; }
        poll_ge(c, CL_PT2 + m, 32u);
        __builtin_amdgcn_fence(__ATOMIC_ACQUIRE, "agent");
      }
      src += lw * 8192;
#pragma unroll 1
      for (int i = 0; i < n; ++i) {
        if (seq >= NSLOT) {
          const unsigned need = (unsigned)(seq - NSLOT + 1);
          bool first = true;
          for (unsigned sp = 0; sp < SPIN_CAP; ++sp) {
            v4u f = *(volatile LAS v4u*)flg(c, F_FREE);
            v2u hh = *(volatile LAS v2u*)flg(c, F_FREEH);
            if ((unsigned)__builtin_amdgcn_readfirstlane(min(min(min(f.x, f.y), min(f.z, f.w)), min(hh.x, hh.y))) >= need) break;
            if (first && pub < seq) { VMWAIT(0); pub = seq; *full = (unsigned)pub; }
            first = false;
            __builtin_amdgcn_s_sleep(1);
          }
          MEMBAR();
        }
        const unsigned dst = L_RING + (seq & (NSLOT - 1)) * SLOT + lw * 8192;
        glds4(ws + src, lv, dst);
        glds4(ws + src + 4096, lv, dst + 4096);
        src += SLOT;
        ++seq;
        if (seq - pub > 3) { VMWAIT(24); pub = seq - 3; *full = (unsigned)pub; }
      }
      stamp(c);
    }
    VMWAIT(0);
    *full = (unsigned)seq;
  } else {
    const int hh = wave - 6;
    const int lv = lane * 16;
    const u64 tag = mix64(__hip_atomic_load((GAS u64*)(c.ctrl + CL_E * 64), RLX_AGENT));
    if (b == 0 && hh == 1) {
      __hip_atomic_store(c.ctrl + (CL_HC + lane) * 64, 0u, RLX_AGENT);
      VMWAIT(0);
      if (lane < 32) __hip_atomic_store((GAS u64*)(c.ctrl + (CL_HELLO + lane) * 64), tag, RLX_AGENT);
    }
    stamp(c);
    {
      const int tb = (hh ? trS : trN) + hh * 1024;
      float s0 = 0.f, s1 = 0.f;
#pragma unroll 1
      for (int sl = 0; sl < 4; ++sl) {
        const int sb = ring_acquire(c, sl);
        v4u f[8];
#pragma unroll
        for (int i = 0; i < 8; ++i) f[i] = tr_frag(L + sb + tb + i * 2048);
#pragma unroll
        for (int i = 0; i < 8; i += 2) { s0 = csum(f[i], s0); s1 = csum(f[i + 1], s1); }
      }
      const v2u o = cs_operand(red_h(s0 + s1));
      if (h == 0) *(LAS v2u*)(L + L_CS1 + hh * 256 + l31 * 8) = o;
      MEMBAR();
      *flg(c, F_CS1R + hh) = 1u;
      MEMBAR();
      if (hh == 0) {
        s0 = 0.f; s1 = 0.f;
#pragma unroll 1
        for (int sl = 0; sl < 4; ++sl) {
          const int sb = ring_acquire(c, 4 + (sl >> 1)) + (sl & 1) * 8192;
          v4u f[8];
#pragma unroll
          for (int i = 0; i < 8; ++i) f[i] = tr_frag(L + sb + trN + i * 1024);
#pragma unroll
          for (int i = 0; i < 8; i += 2) { s0 = csum(f[i], s0); s1 = csum(f[i + 1], s1); }
        }
        const v2u oq = cs_operand(red_h(s0 + s1));
        if (h == 0) *(LAS v2u*)(L + L_CS1 + 512 + l31 * 8) = oq;
      } else {
        for (unsigned sp = 0; sp < SPIN_CAP; ++sp) {
          const u64 v = __hip_atomic_load((GAS u64*)(c.ctrl + (CL_HELLO + (b & 31)) * 64), RLX_AGENT);
          if (__builtin_amdgcn_readfirstlane((int)(v == tag))) break;
          __builtin_amdgcn_s_sleep(4);
        }
        MEMBAR();
        *flg(c, F_HELLO) = 1u;
        ring_acquire(c, 5);
#pragma unroll
        for (int s = 0; s < 2; ++s) {
          const int ks = cl * 2 + s;
          st_ws(c, tr_frag(L + L_RING + ks * 2048 + trN), lv, WS_WT1 + m * 96 * KB + ks * 2 * KB);
          st_ws(c, tr_frag(L + L_RING + ks * 2048 + 1024 + trS), lv, WS_WT1 + m * 96 * KB + (ks * 2 + 1) * KB);
          st_ws(c, tr_frag(L + L_RING + 65536 + ks * 1024 + trN), lv, WS_WT1 + m * 96 * KB + (64 + ks) * KB);
        }
      }
      MEMBAR();
      *flg(c, F_CS1R + hh) = 2u;
      *flg(c, F_FREEH + hh) = 0x7fffffffu;
      MEMBAR();
    }
    stamp(c);
    __builtin_amdgcn_s_setprio(3);
#pragma unroll 1
    for (int layer = 0; layer < 2; ++layer) {
      const unsigned tg = (unsigned)(tag >> (layer * 32));
      const v4u T = {tg & 0xffu, (tg >> 8) & 0xffu, (tg >> 16) & 0xffu, tg >> 24};
      wait_lds4<1>(c, F_KFL, (unsigned)(layer + 1));
      stamp(c);
      f32x4 kv = {0.f, 0.f, 0.f, 0.f}, zz = {0.f, 0.f, 0.f, 0.f};
#pragma unroll
      for (int ww = 0; ww < 4; ++ww) {
        kv += *(LAS f32x4*)(L + L_KVS + ww * 2176 + hh * 1024 + lv);
        zz += *(LAS f32x4*)(L + L_KVS + ww * 2176 + 2048 + hh * 64 + (lane & 3) * 16);
      }
      const int kvb = WS_KVP + ((layer * 16 + m) * 2 + hh) * 16 * KB, zb = WS_ZP + ((layer * 16 + m) * 2 + hh) * KB;
      st_ws(c, (__builtin_bit_cast(v4u, kv) & 0xffffff00u) | T, lv, kvb + cl * KB);
      if (lane < 4) st_ws(c, (__builtin_bit_cast(v4u, zz) & 0xffffff00u) | T, lv, zb + cl * 64);
      stamp(c);
      f32x4 s, zs;
      for (unsigned sp = 0; sp < SPIN_CAP; ++sp) {
        v4u p[17];
        p[16] = ld_ws(c, lv, zb);
#pragma unroll
        for (int i = 0; i < 16; ++i) p[i] = ld_fr(c, lv, kvb, i);
        SB();
        p[16] ^= T;
        v4u chk = p[16];
        zs = __builtin_bit_cast(f32x4, p[16]);
#pragma unroll
        for (int i = 0; i < 16; ++i) {
          p[i] ^= T; chk |= p[i];
          if (i == 0) s = __builtin_bit_cast(f32x4, p[0]); else s += __builtin_bit_cast(f32x4, p[i]);
        }
        if (__all(((chk.x | chk.y | chk.z | chk.w) & 0xffu) == 0u)) break;
        MEMBAR();
      }
      stamp(c);
      *(LAS v2u*)(L + L_KVF + lv + hh * 8) = v2u{pack2(s[0], s[1]), pack2(s[2], s[3])};
#pragma unroll
      for (int i = 0; i < 4; ++i) { float v = zs[i]; v += dpp<0x128>(v); v += dpp<0x124>(v); zs[i] = red_g(v); }
      if (lane < 4) *(LAS v2u*)(L + L_ZF + hh * 32 + lane * 8) = v2u{pack2(zs[0], zs[1]), pack2(zs[2], zs[3])};
      if (layer == 0) wait_lds<1>(c, F_HELLO, 1u);
      MEMBAR();
      *flg(c, F_KVR + hh) = (unsigned)(layer + 1);
      MEMBAR();
      stamp(c);
      if (layer == 0) {
        VMWAIT(0);
        if (lane == 0) __hip_atomic_fetch_add(c.ctrl + (CL_PT2 + m) * 64, 1u, RLX_AGENT);
        if (hh == 0) {
          wait_lds2<2>(c, F_PTOK, 1u);
#pragma unroll
          for (int i = 0; i < 3; ++i) {
            const int aa = g0 + min(i, ng - 1);
            f32x4 t = {0.f, 0.f, 0.f, 0.f};
#pragma unroll
            for (int q = 0; q < 4; ++q) t += __builtin_bit_cast(f32x4, ld_fr(c, lv, WS_CSP + aa * 4 * KB, q));
#pragma unroll
            for (int e = 0; e < 4; ++e) {
              const v2u o = cs_operand(red_g(t[e]));
              if (lane < 16) *(LAS v2u*)(L + L_CS3 + i * 512 + (lane * 4 + e) * 8) = o;
            }
          }
          MEMBAR();
          *flg(c, F_CS3R) = 1u;
          MEMBAR();
        } else if (b == 0) {
          wait_lds<4>(c, F_GO, 1u);
          if (lane == 0) __hip_atomic_store((GAS u64*)(c.ctrl + CL_E * 64), tag, RLX_AGENT);
        }
      }
    }
  }
}

extern "C" void kernel_launch(void* const* d_in, const int* in_sizes, int n_in, void* d_out, int out_size, void* d_ws, size_t ws_size,
                              hipStream_t stream) {
  fused_fwd<<<256, 512, 0, stream>>>((const u16*)d_in[0], (const u16*)d_in[1], (const u16*)d_in[2], (const u16*)d_in[4], (const u16*)d_in[5],
                                     (const u16*)d_in[6], (u16*)d_out, (char*)d_ws);
}
extern "C" int dbg_offset() { return WS_DBG; }
extern "C" int ws_off(int i) { const int t[] = {WS_WT1, WS_WT2, WS_WT3, WS_WT4, WS_CSP, WS_KVP, WS_ZP, WS_ST, WS_O, WS_X, WS_H}; return t[i]; }
```

```cpp
#include <hip/hip_runtime.h>
#include <stdint.h>
#include <utility>

#define GAS __attribute__((address_space(1)))
#define LAS __attribute__((address_space(3)))
#define RLX_AGENT __ATOMIC_RELAXED, __HIP_MEMORY_SCOPE_AGENT
typedef unsigned v4u __attribute__((ext_vector_type(4)));
typedef unsigned v2u __attribute__((ext_vector_type(2)));
typedef float f32x4 __attribute__((ext_vector_type(4)));
typedef float f32x16 __attribute__((ext_vector_type(16)));
typedef short s16x8 __attribute__((ext_vector_type(8)));
typedef short s16x4 __attribute__((ext_vector_type(4)));
typedef __bf16 bf16x2 __attribute__((ext_vector_type(2)));
typedef unsigned short u16;
typedef unsigned long long u64;

#define STAMP 0
#define NWF 4
#define LDEP 2
#define ROT 1
#define RMAP 1
#define CPACE 4
#define NOPROBE 1
#define XDELAY 8
#define PSLEEP 0
#define KVMODE 1
#define KVDELAY 0
#define KVWARM 0
#define R2AT 14u
#define PRETOUCH 0
#define CODEPF 45056
#define NSPEC 0
#define JOBDELAY 3
#define AWIN 16

constexpr int KB = 1024;
constexpr int WS_WT1 = 256 * KB;
constexpr int WS_WT3 = WS_WT1 + 16 * 96 * KB;
constexpr int WS_CSP = WS_WT3 + 43 * 64 * KB;
constexpr int WS_S0P = WS_CSP + 43 * 4 * KB;
constexpr int WS_C1P = WS_S0P + 32 * KB;
constexpr int WS_KVP = WS_C1P + 16 * 8 * KB;
constexpr int WS_ZP = WS_KVP + 2 * 32 * 16 * KB;
constexpr int WS_ST = WS_ZP + 2 * 32 * KB;
constexpr int WS_O = WS_ST + 3 * 256 * KB;
constexpr int WS_X = WS_O + 2 * 2048 * KB;
constexpr int WS_H = WS_X + 3 * 2048 * KB;
constexpr int WS_DBG = WS_H + 2 * 64 * 86 * KB;
constexpr int WS_END = WS_DBG + 256 * 2 * KB;
constexpr int WS_E = 0, WS_F1 = 4 * KB, WS_F2 = WS_F1 + 256 * 4 * 64;
constexpr unsigned SENT = 0xffffffffu;

constexpr int SLOT = 16384, NSLOT = 8;
constexpr int NSTAMP = 36;
constexpr int L_FLG = 0;
constexpr int L_ZF = L_FLG + 128;
constexpr int L_RS = L_ZF + 64;
constexpr int L_CS1 = L_RS + 512;
constexpr int L_CS3 = L_CS1 + 768;
constexpr int L_KVF = L_CS3 + 1536;
constexpr int L_STAMP = L_KVF + 1024;
constexpr int L_KVS = L_STAMP + 1152;
constexpr int L_XCH = 16384;
constexpr int L_RING = 32768;
constexpr int L_END = L_RING + NSLOT * SLOT;
static_assert(L_KVS + 4 * 2176 <= L_XCH && L_END == 163840 && (L_KVS & 15) == 0, "lds");
enum { F_FULL = 0, F_TAG = 2, F_FREE = 4, F_JOB = 8, F_QGO = 10, F_KFL = 12, F_KVR = 16, F_PTOK = 18, F_XF = 20, F_CS1R = 28, F_CS3R = 30, F_CLR = 31 };
constexpr unsigned SPIN_CAP = 1u << 17;

template <int V> struct IC { static constexpr int v = V; __device__ constexpr operator int() const { return V; } };
template <class F, int... I> __device__ __forceinline__ void sfor_(F&& fn, std::integer_sequence<int, I...>) { (fn(IC<I>{}), ...); }
template <int N, class F> __device__ __forceinline__ void sfor(F&& fn) { sfor_(fn, std::make_integer_sequence<int, N>{}); }
#define INL __attribute__((always_inline))
__device__ __forceinline__ unsigned umin(unsigned a, unsigned b) { return a < b ? a : b; }
__device__ __forceinline__ int gsplit(int i) { return (43 * i) >> 4; }
__device__ __forceinline__ f32x16 mfma(v4u a, v4u b, f32x16 c) {
  return __builtin_amdgcn_mfma_f32_32x32x16_bf16(__builtin_bit_cast(s16x8, a), __builtin_bit_cast(s16x8, b), c, 0, 0, 0);
}
__device__ __forceinline__ unsigned pack2(float a, float b) {
  bf16x2 v = {(__bf16)a, (__bf16)b};
  return __builtin_bit_cast(unsigned, v);
}
__device__ __forceinline__ float u2f(unsigned u) { return __builtin_bit_cast(float, u); }
__device__ __forceinline__ unsigned f2u(float f) { return __builtin_bit_cast(unsigned, f); }
__device__ __forceinline__ float blo(unsigned u) { return u2f(u << 16); }
__device__ __forceinline__ float bhi(unsigned u) { return u2f(u & 0xffff0000u); }
__device__ __forceinline__ float relu(float x) { return u2f((unsigned)max((int)f2u(x), 0)); }
__device__ __forceinline__ float dot2(unsigned a, unsigned b, float acc) {
  return __builtin_amdgcn_fdot2_f32_bf16(__builtin_bit_cast(bf16x2, a), __builtin_bit_cast(bf16x2, b), acc, false);
}
constexpr unsigned ONES2 = 0x3f803f80u;
__device__ __forceinline__ float red_h(float v) {
  auto s = __builtin_amdgcn_permlane32_swap(f2u(v), f2u(v), false, false);
  return u2f((unsigned)s[0]) + u2f((unsigned)s[1]);
}
__device__ __forceinline__ float red_g(float v) {
  auto r = __builtin_amdgcn_permlane16_swap(f2u(v), f2u(v), false, false);
  return red_h(u2f((unsigned)r[0]) + u2f((unsigned)r[1]));
}
template <int CTRL> __device__ __forceinline__ float dpp(float v) {
  return u2f(__builtin_amdgcn_update_dpp(0, (int)f2u(v), CTRL, 0xf, 0xf, false));
}
__device__ __forceinline__ float phi_s(float a, float r, float rl) {
  const float e = __builtin_amdgcn_fmed3f(__builtin_amdgcn_exp2f(a * rl), 0.f, 1.f);
  return fmaxf(fmaf(a, r, 1.f), e) + 1e-6f;
}
__device__ __forceinline__ void split2(float v, unsigned& hi, unsigned& lo) {
  hi = pack2(v, 0.f) & 0xffffu;
  lo = pack2(v - blo(hi), 0.f) & 0xffffu;
}
#define MEMBAR() asm volatile("" ::: "memory")
#define PREDELAY() VMWAIT(0)
#define SB() __builtin_amdgcn_sched_barrier(0)
#define VMWAIT(n) asm volatile("s_waitcnt vmcnt(" #n ")" ::: "memory")
template <int D = 256> __device__ __forceinline__ v4u tr_frag(LAS char* p) {
  v2u lo = __builtin_bit_cast(v2u, __builtin_amdgcn_ds_read_tr16_b64_v4i16((LAS s16x4*)p));
  v2u hi = __builtin_bit_cast(v2u, __builtin_amdgcn_ds_read_tr16_b64_v4i16((LAS s16x4*)(p + D)));
  return v4u{lo.x, lo.y, hi.x, hi.y};
}
__device__ __forceinline__ void glds(const void* sbase, unsigned voff, unsigned ldst) {
  asm volatile("s_mov_b32 m0, %2\n\ts_nop 0\n\tglobal_load_lds_dwordx4 %0, %1" ::"v"(voff), "s"(sbase), "s"(ldst) : "memory");
}
__device__ __forceinline__ void glds4(const void* sbase, unsigned voff, unsigned ldst) {
  asm volatile(
      "s_mov_b32 m0, %2\n\ts_nop 0\n\tglobal_load_lds_dwordx4 %0, %1 sc1\n\tglobal_load_lds_dwordx4 %0, %1 offset:1024 sc1\n\t"
      "global_load_lds_dwordx4 %0, %1 offset:2048 sc1\n\tglobal_load_lds_dwordx4 %0, %1 offset:3072 sc1" ::"v"(voff), "s"(sbase), "s"(ldst)
      : "memory");
}

struct Ctx {
  LAS char* L;
  __amdgpu_buffer_rsrc_t rs;
  int lane, wave, cl, rot;
  int seq, known;
  int vb0, vb1;
  v2u fpre;
  int nst;
};
__device__ __forceinline__ volatile LAS unsigned* flg(Ctx& c, int i) { return (volatile LAS unsigned*)(c.L + L_FLG) + i; }
__device__ __forceinline__ void stamp0(Ctx& c) {
}
__device__ __forceinline__ void stamp(Ctx& c) { if (STAMP == 1) stamp0(c); }
__device__ __forceinline__ void stamp2(Ctx& c) { if (STAMP == 2) stamp0(c); }
__device__ __forceinline__ void stamp3(Ctx& c) { if (STAMP == 3) stamp0(c); }
__device__ __forceinline__ void stamp4(Ctx& c) { if (STAMP == 4) stamp0(c); }
__device__ __forceinline__ void stamp5(Ctx& c) { if (STAMP == 5) stamp0(c); }
template <int SLP> __device__ __forceinline__ void wait_lds(Ctx& c, int i, unsigned val) {
#pragma nounroll
  for (unsigned sp = 0; sp < SPIN_CAP; ++sp) {
    if ((unsigned)__builtin_amdgcn_readfirstlane(*flg(c, i)) >= val) break;
    if (SLP) __builtin_amdgcn_s_sleep(SLP);
  }
  MEMBAR();
}
template <int SLP> __device__ __forceinline__ void wait_lds2(Ctx& c, int i, unsigned val) {
#pragma nounroll
  for (unsigned sp = 0; sp < SPIN_CAP; ++sp) {
    v2u f = *(volatile LAS v2u*)flg(c, i);
    if ((unsigned)__builtin_amdgcn_readfirstlane(min(f.x, f.y)) >= val) break;
    if (SLP) __builtin_amdgcn_s_sleep(SLP);
  }
  MEMBAR();
}
template <int SLP> __device__ __forceinline__ void wait_lds4(Ctx& c, int i, unsigned val) {
#pragma nounroll
  for (unsigned sp = 0; sp < SPIN_CAP; ++sp) {
    v4u f = *(volatile LAS v4u*)flg(c, i);
    if ((unsigned)__builtin_amdgcn_readfirstlane(min(min(f.x, f.y), min(f.z, f.w))) >= val) break;
    if (SLP) __builtin_amdgcn_s_sleep(SLP);
  }
  MEMBAR();
}
template <int N> __device__ __forceinline__ void poll_tags(Ctx& c, int base, v2u tag) {
#pragma nounroll
  for (unsigned sp = 0; sp < SPIN_CAP; ++sp) {
    const v2u v = __builtin_amdgcn_raw_buffer_load_b64(c.rs, (c.lane & (N - 1)) * 64, base, 16);
    if (__all(v.x == tag.x && v.y == tag.y)) break;
    __builtin_amdgcn_s_sleep(4);
    MEMBAR();
  }
  MEMBAR();
}
__device__ __forceinline__ int ring_acquire(Ctx& c, int seq) {
  if (c.known <= seq) {
#pragma nounroll
    for (unsigned sp = 0; sp < SPIN_CAP; ++sp) {
      v2u f = *(volatile LAS v2u*)flg(c, F_FULL);
      c.known = __builtin_amdgcn_readfirstlane(min(f.x, f.y));
      if (c.known > seq) break;
    }
  }
  MEMBAR();
  return L_RING + (seq & (NSLOT - 1)) * SLOT;
}
__device__ __forceinline__ v4u ld_ws(Ctx& c, int voff, int soff) { return __builtin_amdgcn_raw_buffer_load_b128(c.rs, voff, soff, 16); }
__device__ __forceinline__ void st_ws(Ctx& c, v4u v, int voff, int soff) {
  __builtin_amdgcn_raw_buffer_store_b128(v, c.rs, voff, soff, 16);
  asm volatile("s_nop 1" ::"v"(v) : "memory");
}
__device__ __forceinline__ int opq(int v) { asm volatile("" : "+s"(v)); return v; }
__device__ __forceinline__ int opqv(int v) { asm volatile("" : "+v"(v)); return v; }
__device__ __forceinline__ v4u ld_fr(Ctx& c, int lv, int sb, int i) { return ld_ws(c, lv + (i & 3) * KB, sb + (i >> 2) * 4 * KB); }

template <bool R> __device__ __forceinline__ v4u ld_act(Ctx& c, int lv, int sb, int j) {
  if (!R || j >= 64 || !ROT) return ld_fr(c, lv, sb, j);
  return ld_ws(c, lv + (j & 3) * KB, sb + (j >> 5) * 32 * KB + (((j >> 2) + c.rot) & 7) * 4 * KB);
}
template <int MODE> __device__ __forceinline__ v4u wread(Ctx& c, int f, int lb0, int lb1, bool ahead) {
  constexpr int FPS = MODE == 1 ? 8 : 16;
  const int i = f % FPS;
  if (i == 0) {
    if (ahead) c.known = max(c.known, (int)__builtin_amdgcn_readfirstlane(min(c.fpre.x, c.fpre.y)));
    const int sb = ring_acquire(c, c.seq); c.vb0 = lb0 + sb; if (MODE == 1 || MODE == 2) c.vb1 = lb1 + sb;
  }
  v4u r;
  if (MODE == 0) r = *(LAS v4u*)(c.L + c.vb0 + i * KB);
  else if (MODE == 1) r = tr_frag<512>(c.L + ((i & 1) ? c.vb1 : c.vb0) + (i >> 1) * 4 * KB);
  else if (MODE == 2) r = tr_frag(c.L + ((i & 1) ? c.vb1 : c.vb0) + i * KB);
  else r = tr_frag(c.L + c.vb0 + i * KB);
  if (i == FPS - 4) c.fpre = *(volatile LAS v2u*)flg(c, F_FULL);
  return r;
}
__device__ __forceinline__ void ring_publish(Ctx& c) { MEMBAR(); *flg(c, F_FREE + c.wave) = (unsigned)c.seq; MEMBAR(); }
template <int MODE> __device__ __forceinline__ void wpre(Ctx& c, v4u (&wf)[NWF], int lb0, int lb1) {
#pragma unroll
  for (int f = 0; f < NWF; ++f) wf[f] = wread<MODE>(c, f, lb0, lb1, false);
  if (MODE == 1 && NWF == 8) c.seq++;
}
struct NoSide { __device__ __forceinline__ void operator()(int) const {} };
template <int NF, int TPS, int MODE, int NEXT, bool XA, int RELF, int EARLY, int AM, class SIDE>
__device__ __forceinline__ void gemm(Ctx& c, v4u (&a)[32], f32x16 (&acc)[2], v4u (&wf)[NWF], int lb0, int lb1, bool chain, SIDE side) {
  constexpr int FPS = MODE == 1 ? 8 : 16;
  static_assert(NEXT < 0 || (NF % NWF == 0 && NF % FPS == 0), "chain");
  sfor<NF>([&](auto F) INL {
    constexpr int f = F.v, s = f / TPS, t = f % TPS, fn = f + NWF;
    const v4u x = a[s & AM], wv = wf[f % NWF];
    acc[t] = XA ? mfma(x, wv, acc[t]) : mfma(wv, x, acc[t]);
    SB();
    if constexpr (fn < NF) {
      wf[f % NWF] = wread<MODE>(c, fn, lb0, lb1, true);
      if constexpr (fn % FPS == FPS - 1 || fn == NF - 1) { c.seq++; if constexpr (f >= RELF || fn / FPS < EARLY) ring_publish(c); }
      else if constexpr (f == RELF && RELF > 0) ring_publish(c);
    } else if constexpr (NEXT >= 0) {
      if (chain) wf[f % NWF] = wread<(NEXT < 0 ? 0 : NEXT)>(c, fn - NF, lb0, lb1, true);
    }
    side(F);
    SB();
  });
}
__device__ __forceinline__ f32x16 zero16() { return f32x16{0.f, 0.f, 0.f, 0.f, 0.f, 0.f, 0.f, 0.f, 0.f, 0.f, 0.f, 0.f, 0.f, 0.f, 0.f, 0.f}; }
__device__ __forceinline__ v4u nosent(v4u v) { return v4u{umin(v.x, SENT - 1), umin(v.y, SENT - 1), umin(v.z, SENT - 1), umin(v.w, SENT - 1)}; }
__device__ __forceinline__ unsigned umax3(unsigned a, unsigned b, unsigned d) { unsigned r; asm("v_max3_u32 %0, %1, %2, %3" : "=v"(r) : "v"(a), "v"(b), "v"(d)); return r; }
template <int N, int M, int NA> __device__ __forceinline__ bool incomplete(v4u (&a)[NA], int i0) {
  unsigned m0 = a[i0 & M].x, m1 = a[i0 & M].z;
  m0 = max(m0, a[i0 & M].y); m1 = umax3(m1, a[i0 & M].w, m0);
#pragma unroll
  for (int i = 1; i < N; ++i) { const v4u v = a[(i0 + i) & M]; m1 = umax3(m1, v.x, v.y); m1 = umax3(m1, v.z, v.w); }
  return __any(m1 == SENT);
}
template <int N, int STR, int M, int NA> __device__ __forceinline__ void check(Ctx& c, v4u (&a)[NA], int lv, int sb, int i0) {
  if (__builtin_expect(incomplete<N, M>(a, i0), 0)) {
    unsigned sp = 0;
#pragma nounroll
    do {
      MEMBAR();
#pragma unroll
      for (int i = 0; i < N; ++i) a[(i0 + i) & M] = ld_act<M == 31>(c, lv, sb, STR * (i0 + i));
    } while (incomplete<N, M>(a, i0) && ++sp < SPIN_CAP);
  }
}
template <int STR> __device__ __forceinline__ unsigned probe(Ctx& c, int sb, int n) {
  return __builtin_amdgcn_raw_buffer_load_b32(c.rs, min(c.lane, n - 1) * (STR * KB) + 1020, sb, 16);
}
template <int STR> __device__ __forceinline__ void open_phase(Ctx& c, v4u (&a)[32], int lv, int sb, int n) {
  stamp5(c);
  if (XDELAY) __builtin_amdgcn_s_sleep(XDELAY);
#pragma nounroll
  for (unsigned sp = 0; sp < SPIN_CAP && !NOPROBE; ++sp) {
    const unsigned p = probe<STR>(c, sb, n);
#pragma unroll
    for (int i = 0; i < NSPEC; ++i) a[i] = ld_act<true>(c, lv, sb, STR * i);
    SB();
    if (!__any(p == SENT)) break;
    if (PSLEEP) __builtin_amdgcn_s_sleep(PSLEEP);
    MEMBAR();
  }
  stamp5(c);
#pragma unroll
  for (int i = NSPEC; i < 8; ++i) a[i] = ld_act<true>(c, lv, sb, STR * i);
  SB();
  stamp5(c);
  check<4, STR, 31>(c, a, lv, sb, 0);
  stamp5(c);
}
constexpr int ramp(int f, int n) {
  const int i = f < 2 * (AWIN - 8) ? 8 + f : ((f & 1) ? (f >> 1) + AWIN : -1);
  return i < n ? i : -1;
}
__device__ __forceinline__ u64 mix64(u64 z) {
  z += 0x9E3779B97F4A7C15ull;
  z = (z ^ (z >> 30)) * 0xBF58476D1CE4E5B9ull;
  z = (z ^ (z >> 27)) * 0x94D049BB133111EBull;
  return z ^ (z >> 31);
}
__device__ __forceinline__ float csum(v4u f, float s) { return dot2(f.w, ONES2, dot2(f.z, ONES2, dot2(f.y, ONES2, dot2(f.x, ONES2, s)))); }
__device__ __forceinline__ v2u cs_operand(float s) { unsigned hi, lo; split2(s, hi, lo); return v2u{hi | (hi << 16), lo}; }

__global__ __launch_bounds__(512, 2) void fused_fwd(const u16* __restrict__ x, const u16* __restrict__ wqkv, const u16* __restrict__ wo,
                                                    const u16* __restrict__ wg, const u16* __restrict__ wu, const u16* __restrict__ wd,
                                                    u16* __restrict__ out, char* __restrict__ ws) {
  __shared__ __attribute__((aligned(16))) char smem[L_END];
  Ctx c;
  c.L = (LAS char*)smem;
  c.rs = __builtin_amdgcn_make_buffer_rsrc((void*)ws, 0, WS_END, 0x00020000);
  const int lane = threadIdx.x & 63, wave = __builtin_amdgcn_readfirstlane(threadIdx.x >> 6);
  const int b = blockIdx.x, xc = b & 7, j = b >> 3;
  const int cl = (xc >> 1) * 4 + (j >> 3), m = (xc & 1) * 8 + (j & 7);
  const int rh = RMAP ? m >> 3 : (m >> 2) & 1, cg = RMAP ? m & 7 : (m >> 3) * 4 + (m & 3);
  const int g0 = gsplit(m), ng = gsplit(m + 1) - g0;
  c.lane = lane; c.wave = wave; c.cl = cl; c.seq = 0; c.known = 0; c.nst = 0; c.vb0 = 0; c.vb1 = 0; c.fpre = v2u{0u, 0u};
  const int rot = (2 * cl + m) & 7;
  c.rot = rot;
  const int h = lane >> 5, l31 = lane & 31;
  LAS char* L = c.L;
  if (wave == 0 && lane < 32) *flg(c, lane) = 0u;
  unsigned warm = 0;
  if (PRETOUCH && wave == 7) {
    const char* p = ws + (size_t)(lane & 15) * (2u << 20);
    if (lane >= 16) {
      const int q = (lane >> 1) & 7, e = lane & 1;
      p = (const char*)(q == 0 ? x : q == 1 ? wqkv : q == 2 ? wo : q == 3 ? wg : q == 4 ? wu : q == 5 ? wd : (const u16*)out);
      p += e * (q == 2 ? 512 * KB - 4 : q == 1 ? 1536 * KB - 4 : (q == 0 || q > 5) ? 2048 * KB - 4 : 1376 * KB - 4);
    }
    if (lane < 32) warm = *(const GAS unsigned*)p;
  }
  if (CODEPF && wave == 5) {
    u64 pc;
    asm volatile("s_getpc_b64 %0" : "=s"(pc));
    const GAS char* cp = (const GAS char*)(pc & ~(u64)255) + j * (CODEPF / 32) + lane * 32;
    if (lane * 32 < CODEPF / 32) warm = *(const GAS unsigned*)cp;
  }
  stamp0(c);
  asm volatile("s_waitcnt lgkmcnt(0)\n\ts_barrier" ::: "memory");
  const int trN = (8 * h + ((lane >> 2) & 3)) * 64 + ((lane >> 4) & 1) * 32 + (lane & 3) * 8;
  const int trS = (8 * h + ((lane >> 2) & 3)) * 64 + ((lane >> 4) & 1) * 32 + (((lane & 1) << 1) | ((lane >> 1) & 1)) * 8;

  auto load_slot0 = [&](int sl) INL {
    const unsigned vo1 = (lane >> 2) * 3072 + (lane & 3) * 16;
    const char* wb = (const char*)wqkv + m * 64;
#pragma unroll 1
    for (int p = 0; p < 16; ++p) {
      const int s = sl < 4 ? sl * 8 + (p >> 1) : (sl - 4) * 16 + p;
      glds(wb + (sl < 4 ? (1 + (p & 1)) * 1024 : 0) + (size_t)(((s + 4 * rot) & 31) * 16) * 3072, vo1, L_RING + sl * SLOT + p * 1024);
    }
  };
  if (wave < 4) {
    __builtin_amdgcn_s_setprio(2);
    const int w = wave, mh = w >> 1, kh = w & 1;
    v4u a[32], wf[NWF], xr[2];
    f32x16 acc[2];
    const int lv = lane * 16;
    float rstd;
    v2u tag2;
    v4u mfrag;
    auto set_stats = [&](float sum, float ssq, float& var) INL {
      const float s = sum * (1.f / 512), q = ssq * (1.f / 512);
      var = fmaxf(q - s * s, 0.f) + 1e-5f;
      unsigned hi, lo;
      split2(-s, hi, lo);
      mfrag = h == 0 ? v4u{hi | (lo << 16), hi, 0u, 0u} : v4u{0u, 0u, 0u, 0u};
    };
    auto cs_frag = [&](int off) INL { const v2u t = *(LAS v2u*)(L + off + l31 * 8); return v4u{t.x, t.y, 0u, 0u}; };
    {
      const __amdgpu_buffer_rsrc_t rx = __builtin_amdgcn_make_buffer_rsrc((void*)x, 0, 2048 * 1024, 0x00020000);
      const int xv = (lane >> 3) * 1024 + (lane & 7) * 16, xs = (cl * 128 + w * 32) * 1024;
      auto ldx = [&](int i) INL { a[i] = __builtin_amdgcn_raw_buffer_load_b128(rx, xv, xs + (i & 3) * 8192 + (((i >> 2) + rot) & 7) * 128, 0); };
#pragma unroll
      for (int i = 0; i < 8; ++i) ldx(i);
      SB();
      stamp3(c);
      LAS char* sc = L + L_XCH + w * 4096;
      const int r8 = lane >> 3;
      const int wo = r8 * 128 + (((lane & 7) ^ (r8 >> 1)) * 16);
      const int ro = l31 * 128 + ((h ^ ((l31 >> 1) & 7)) * 16);
      auto xpose = [&](int kp) INL {
#pragma unroll
        for (int q = 0; q < 4; ++q) *(LAS v4u*)(sc + ((wo + q * 1024) ^ ((q & 1) * 64))) = a[kp * 4 + q];
        MEMBAR();
#pragma unroll
        for (int q = 0; q < 4; ++q) a[kp * 4 + q] = *(LAS v4u*)(sc + (ro ^ (q * 32)));
        MEMBAR();
      };
      xpose(0);
#pragma unroll
      for (int i = 8; i < 16; ++i) ldx(i);
      xpose(1);
      stamp3(c);
      wpre<2>(c, wf, trN, trS);
      stamp3(c);
      acc[0] = zero16(); acc[1] = zero16();
      v4u s0;
      const int s0b = WS_S0P + (cl * 128 + w * 32) * 16;
      auto side = [&](int f) INL {
        if (f < 16) ldx(16 + f);
        if ((f & 7) == 2 && (f >> 3) + 2 < 8) xpose((f >> 3) + 2);
        if (f == 40) s0 = ld_ws(c, l31 * 16, s0b);
        if ((f & 15) == 15) stamp3(c);
      };
      gemm<64, 2, 2, -1, true, 0, 0, 31>(c, a, acc, wf, trN, trS, false, side);
      wait_lds2<0>(c, F_CS1R, 1u);
      tag2 = *(volatile LAS v2u*)flg(c, F_TAG);
      if (__any(s0.y != tag2.x || s0.w != tag2.y)) {
        unsigned sp = 0;
#pragma nounroll
        do { MEMBAR(); s0 = ld_ws(c, l31 * 16, s0b); } while (__any(s0.y != tag2.x || s0.w != tag2.y) && ++sp < SPIN_CAP);
      }
      float var;
      set_stats(u2f(s0.x), u2f(s0.z), var);
      rstd = __builtin_amdgcn_rsqf(var);
      if (h == 0) *(LAS float*)(L + L_RS + w * 128 + l31 * 4) = rstd;
      const int xq = l31 * 1024 + h * 16, xqs = (cl * 128 + rh * 64 + mh * 32) * 1024 + cg * 128 + kh * 64;
      xr[0] = __builtin_amdgcn_raw_buffer_load_b128(rx, xq, xqs, 0);
      xr[1] = __builtin_amdgcn_raw_buffer_load_b128(rx, xq + 32, xqs, 0);
      stamp3(c);
      acc[0] = mfma(mfrag, cs_frag(L_CS1), acc[0]);
      acc[1] = mfma(mfrag, cs_frag(L_CS1 + 256), acc[1]);
    }
#pragma unroll 1
    for (int layer = 0; layer < 2; ++layer) {
      stamp(c);
      {
        v4u pk[2], vv[2];
#pragma unroll
        for (int q = 0; q < 4; ++q) {
          const f32x4 r = *(LAS f32x4*)(L + L_RS + w * 128 + (8 * q + 4 * h) * 4);
          float p[4], v[4];
#pragma unroll
          for (int i = 0; i < 4; ++i) { p[i] = phi_s(acc[0][4 * q + i], r[i], r[i] * 1.44269504f); v[i] = acc[1][4 * q + i] * r[i]; }
          pk[q >> 1][(q & 1) * 2] = pack2(p[0], p[1]); pk[q >> 1][(q & 1) * 2 + 1] = pack2(p[2], p[3]);
          vv[q >> 1][(q & 1) * 2] = pack2(v[0], v[1]); vv[q >> 1][(q & 1) * 2 + 1] = pack2(v[2], v[3]);
        }
        f32x16 kv = mfma(pk[0], vv[0], zero16());
        kv = mfma(pk[1], vv[1], kv);
        const float z = red_h(csum(pk[1], csum(pk[0], 0.f)));
        LAS char* ks = L + L_KVS + w * 2176;
        if (h == 0) *(LAS float*)(ks + 2048 + l31 * 4) = z;
        if (lane & 16) {
          *(LAS f32x4*)(ks + lv) = f32x4{kv[8], kv[9], kv[10], kv[11]};
          *(LAS f32x4*)(ks + 1024 + lv) = f32x4{kv[12], kv[13], kv[14], kv[15]};
          MEMBAR();
        } else {
          MEMBAR();
          *(LAS f32x4*)(ks + lv) = f32x4{kv[0], kv[1], kv[2], kv[3]};
          *(LAS f32x4*)(ks + 1024 + lv) = f32x4{kv[4], kv[5], kv[6], kv[7]};
        }
        MEMBAR();
        unsigned old = 0;
        if (lane == 0) old = __hip_atomic_fetch_add((LAS unsigned*)flg(c, F_KFL), 1u, __ATOMIC_RELAXED, __HIP_MEMORY_SCOPE_WORKGROUP);
        if ((__builtin_amdgcn_readfirstlane(old) & 3u) == 3u) {
          MEMBAR();
          f32x4 k0 = {0.f, 0.f, 0.f, 0.f}, k1 = k0, zz = k0;
#pragma unroll
          for (int ww = 0; ww < 4; ++ww) {
            k0 += *(LAS f32x4*)(L + L_KVS + ww * 2176 + lv);
            k1 += *(LAS f32x4*)(L + L_KVS + ww * 2176 + 1024 + lv);
            zz += *(LAS f32x4*)(L + L_KVS + ww * 2176 + 2048 + (lane & 7) * 16);
          }
          const unsigned tg = layer ? tag2.y : tag2.x;
          const v4u T = {tg & 0xffu, (tg >> 8) & 0xffu, (tg >> 16) & 0xffu, tg >> 24};
          const int kvb = WS_KVP + ((layer * 16 + m) * 32 + cl) * KB;
          st_ws(c, (__builtin_bit_cast(v4u, k0) & 0xffffff00u) | T, lv, kvb);
          st_ws(c, (__builtin_bit_cast(v4u, k1) & 0xffffff00u) | T, lv, kvb + 16 * KB);
          if (lane < 8) st_ws(c, (__builtin_bit_cast(v4u, zz) & 0xffffff00u) | T, lv, WS_ZP + (layer * 16 + m) * 2 * KB + cl * 128);
        }
        MEMBAR();
      }
      stamp(c);
      acc[0] = mfma(cs_frag(L_CS1 + 512), mfrag, zero16());
      if (layer == 0) { wpre<3>(c, wf, trN, trN); gemm<32, 1, 3, -1, false, 0, 0, 31>(c, a, acc, wf, trN, trN, false, NoSide()); }
      else { wpre<0>(c, wf, lv, lv); gemm<32, 1, 0, -1, false, 0, 0, 31>(c, a, acc, wf, lv, lv, false, NoSide()); }
      const int kbb = (8 * h + ((lane >> 2) & 3)) * 128 + ((lane >> 4) & 1) * 32 + (((lane & 1) << 1) | ((lane >> 1) & 1)) * 8 + kh * 2048;
      const int kb0 = kbb + ((kh ^ (lane >> 3)) & 1) * 64, kb1 = kb0 ^ 64;
      auto res_init = [&]() INL {
#pragma unroll
        for (int i = 0; i < 8; ++i) { acc[0][2 * i] = blo(xr[i >> 2][i & 3]); acc[0][2 * i + 1] = bhi(xr[i >> 2][i & 3]); }
        acc[1] = zero16();
        asm volatile("" : "+v"(acc[0]), "+v"(acc[1]));
      };
      {
        v4u pq[2];
        const float rl = rstd * 1.44269504f;
#pragma unroll
        for (int i = 0; i < 8; ++i) pq[i >> 2][i & 3] = pack2(phi_s(acc[0][2 * i], rstd, rl), phi_s(acc[0][2 * i + 1], rstd, rl));
        asm volatile("" : "+v"(pq[0]), "+v"(pq[1]));
        res_init();
        wait_lds<0>(c, F_CLR, layer ? 3u : 1u);
        stamp(c);
        wait_lds2<0>(c, F_KVR, (unsigned)(layer + 1));
        stamp(c);
        const v4u kvf = *(LAS v4u*)(L + L_KVF + lv);
        v2u zf[4];
#pragma unroll
        for (int q = 0; q < 4; ++q) zf[q] = *(LAS v2u*)(L + L_ZF + q * 16 + h * 8);
        const bool up = (lane & 16) != 0;
        const v4u zz = {0u, 0u, 0u, 0u};
        f32x16 num = mfma(up ? zz : kvf, pq[0], zero16());
        num = mfma(up ? kvf : zz, pq[1], num);
        float r[2];
#pragma unroll
        for (int hd = 0; hd < 2; ++hd) {
          const float d = dot2(pq[hd].w, zf[2 * hd + 1].y, dot2(pq[hd].z, zf[2 * hd + 1].x, dot2(pq[hd].y, zf[2 * hd].y, dot2(pq[hd].x, zf[2 * hd].x, 0.f))));
          r[hd] = __builtin_amdgcn_rcpf(red_h(d) + 1e-6f);
        }
        stamp5(c);
        const int ob = opq(WS_O + layer * 2048 * KB + ((cl * 4 + w) * 32 + 2 * m) * KB);
#pragma unroll
        for (int hd = 0; hd < 2; ++hd) {
          v4u o;
#pragma unroll
          for (int i = 0; i < 4; ++i) o[i] = pack2(num[8 * hd + 2 * i] * r[hd], num[8 * hd + 2 * i + 1] * r[hd]);
          st_ws(c, nosent(o), lv + hd * KB, ob);
        }
      }
      stamp(c);
      const int rtA = opq(cl * 4 + rh * 2 + mh);
      auto res_epilogue = [&](int xi, bool final_out, unsigned xseq) INL {
        LAS char* xs = L + L_XCH;
#pragma unroll
        for (int q = 0; q < 4; ++q) *(LAS f32x4*)(xs + w * 4096 + q * 1024 + lv) = f32x4{acc[1][4 * q], acc[1][4 * q + 1], acc[1][4 * q + 2], acc[1][4 * q + 3]};
        MEMBAR();
        *flg(c, F_XF + w) = xseq;
        f32x4 o[4];
#pragma nounroll
        for (unsigned sp = 0; sp < SPIN_CAP; ++sp) {
          const unsigned fl = *flg(c, F_XF + (w ^ 1));
          MEMBAR();
#pragma unroll
          for (int q = 0; q < 4; ++q) o[q] = *(LAS f32x4*)(xs + (w ^ 1) * 4096 + q * 1024 + lv);
          MEMBAR();
          if ((unsigned)__builtin_amdgcn_readfirstlane(fl) >= xseq) break;
        }
#pragma unroll
        for (int q = 0; q < 4; ++q) {
          xr[q >> 1][(q & 1) * 2] = pack2(acc[0][4 * q] + o[q][0], acc[0][4 * q + 1] + o[q][1]);
          xr[q >> 1][(q & 1) * 2 + 1] = pack2(acc[0][4 * q + 2] + o[q][2], acc[0][4 * q + 3] + o[q][3]);
        }
        if (final_out) {
          GAS char* ob = (GAS char*)out + (size_t)(rtA * 32 + l31) * 1024 + cg * 128 + kh * 64 + h * 16;
          *(GAS v4u*)ob = xr[0];
          *(GAS v4u*)(ob + 32) = xr[1];
        } else {
          const int xb = WS_X + xi * 2048 * KB + (rtA * 32 + cg * 4 + kh * 2) * KB;
          stamp5(c);
          st_ws(c, nosent(xr[0]), lv, xb);
          st_ws(c, nosent(xr[1]), lv + KB, xb);
          float s = 0.f, q = 0.f;
#pragma unroll
          for (int i = 0; i < 8; ++i) { s = dot2(xr[i >> 2][i & 3], ONES2, s); q = dot2(xr[i >> 2][i & 3], xr[i >> 2][i & 3], q); }
          s = red_h(s); q = red_h(q);
          if (h == 0) __builtin_amdgcn_raw_buffer_store_b64(v2u{umin(f2u(s), SENT - 1), umin(f2u(q), SENT - 1)}, c.rs, l31 * 16, WS_ST + (xi * 64 + rtA) * 4 * KB + cg * 512 + kh * 8, 16);
        }
      };
      {
        const int ab = WS_O + layer * 2048 * KB + (rtA * 32 + kh) * KB;
        wpre<1>(c, wf, kb0, kb1);
        stamp5(c);
        PREDELAY();
        open_phase<2>(c, a, lv, ab, 16);
        stamp(c);
        auto side = [&](int f) INL {
          if (ramp(f, 16) >= 0) a[ramp(f, 16)] = ld_act<true>(c, lv, ab, 2 * ramp(f, 16));
          if ((f & 7) == 4 && f < 24) check<4, 2, 31>(c, a, lv, ab, (f >> 1) + 2);
        };
        gemm<32, 2, 1, -1, false, 8, 0, 31>(c, a, acc, wf, kb0, kb1, false, side);
        stamp(c);
        res_epilogue(layer * 2, false, (unsigned)(layer * 2 + 1));
      }
      wpre<0>(c, wf, lv, lv);
      stamp(c);
      const int rtB = opq(cl * 4 + w);
      v4u st[4];
      auto ln_side = [&](int f, int xi, float& var) INL -> bool {
        const int xb = WS_X + xi * 2048 * KB + rtB * 32 * KB, sb = WS_ST + (xi * 64 + rtB) * 4 * KB;
        if (ramp(f, 32) >= 0) a[ramp(f, 32)] = ld_act<true>(c, lv, xb, ramp(f, 32));
        if ((f & 7) == 4 && f < 56) check<4, 1, 31>(c, a, lv, xb, (f >> 1) + 2);
        if (f >= 2 * (32 - AWIN) && f < 2 * (32 - AWIN) + 4) st[f - 2 * (32 - AWIN)] = ld_fr(c, lv, sb, f - 2 * (32 - AWIN));
        if (f != 54) return false;
        check<4, 1, 3>(c, st, lv, sb, 0);
        float s = 0.f, q = 0.f;
#pragma unroll
        for (int i = 0; i < 4; ++i) { s += u2f(st[i].x) + u2f(st[i].z); q += u2f(st[i].y) + u2f(st[i].w); }
        set_stats(red_h(s), red_h(q), var);
        return true;
      };
      const int ab4 = WS_H + layer * 64 * 86 * KB + (rtA * 86 + kh) * KB;
      {
        float r2;
        const int hb = WS_H + layer * 64 * 86 * KB + rtB * 86 * KB;
        auto corr = [&](int gi) INL {
          acc[0] = mfma(cs_frag(L_CS3 + gi * 512), mfrag, acc[0]);
          acc[1] = mfma(cs_frag(L_CS3 + gi * 512 + 256), mfrag, acc[1]);
        };
        auto side0 = [&](int f) INL {
          float var;
          if (ln_side(f, layer * 2, var)) { r2 = __builtin_amdgcn_rcpf(var); asm volatile("" : "+v"(r2), "+v"(mfrag)); }
          if (f == 58 && layer == 0) { wait_lds<0>(c, F_CS3R, 1u); wait_lds<0>(c, F_CLR, 2u); }
          if (f == 59) corr(0);
        };
        auto finish = [&](int gi) INL {
          const int pos = gi * 16 + (gi < 2 ? m : g0 - 2 * m);
#pragma unroll
          for (int s = 0; s < 2; ++s) {
            v4u o;
#pragma unroll
            for (int i = 0; i < 4; ++i) {
              const int e = 8 * s + 2 * i;
              o[i] = pack2(relu(acc[0][e]) * r2 * acc[1][e], relu(acc[0][e + 1]) * r2 * acc[1][e + 1]);
            }
            st_ws(c, nosent(o), lv + s * KB, hb + pos * 2 * KB);
          }
        };
        acc[0] = zero16(); acc[1] = zero16();
        stamp5(c);
        PREDELAY();
        open_phase<1>(c, a, lv, WS_X + layer * 2 * 2048 * KB + rtB * 32 * KB, 32);
        stamp(c); stamp2(c);
        gemm<64, 2, 0, 0, false, 2 * (32 - AWIN), 0, 31>(c, a, acc, wf, lv, lv, true, side0);
        stamp2(c);
        finish(0);
#pragma unroll 1
        for (int gi = 1; gi < ng; ++gi) {
          stamp2(c);
          acc[0] = zero16(); acc[1] = zero16();
          auto side = [&](int f) INL { if (f == 5) corr(gi); };
          gemm<64, 2, 0, 0, false, 0, 0, 31>(c, a, acc, wf, lv, lv, gi + 1 < ng, side);
          stamp2(c);
          if (gi + 1 == ng) {
#pragma unroll
            for (int i = 0; i < 8; ++i) a[i] = ld_act<true>(c, lv, ab4, 2 * i);
            SB();
          }
          if (gi + 1 == ng) stamp4(c);
          finish(gi);
        }
      }
      stamp4(c);
      {
        unsigned pr = probe<2>(c, ab4 + 32 * KB, 16);
        wpre<1>(c, wf, kb0, kb1);
        res_init();
        stamp4(c);
        check<4, 2, 31>(c, a, lv, ab4, 0);
        stamp4(c);
        stamp(c); stamp2(c);
        auto gate = [&](int sb, int n) INL {
          if (__builtin_expect(__any(pr == SENT), 0)) {
            unsigned sp = 0;
#pragma nounroll
            do { MEMBAR(); pr = probe<2>(c, sb, n); } while (__any(pr == SENT) && ++sp < SPIN_CAP);
          }
        };
        auto side = [&](int f) INL {
          const int i = ramp(f, 43);
          if (i >= 0) {
            if (i == 16) gate(ab4 + 32 * KB, 16);
            if (i == 24) pr = probe<2>(c, ab4 + 64 * KB, 11);
            if (i == 32) gate(ab4 + 64 * KB, 11);
            a[i & 31] = ld_act<true>(c, lv, ab4, 2 * i);
          }
          if ((f & 7) == 4 && f < 76) check<4, 2, 31>(c, a, lv, ab4, (f >> 1) + 2);
          if (f == 76) check<3, 2, 31>(c, a, lv, ab4, 40);
          if ((f & 7) == 7 || f == 85) stamp4(c);
        };
        gemm<86, 2, 1, -1, false, 2 * (43 - AWIN), 3, 31>(c, a, acc, wf, kb0, kb1, false, side);
        stamp(c); stamp2(c);
        res_epilogue(1, layer == 1, (unsigned)(layer * 2 + 2));
      }
      if (layer == 1) break;
      wpre<0>(c, wf, lv, lv);
      stamp(c);
      {
        auto side = [&](int f) INL {
          float var;
          if (ln_side(f, 1, var)) {
            rstd = __builtin_amdgcn_rsqf(var);
            if (h == 0) *(LAS float*)(L + L_RS + w * 128 + l31 * 4) = rstd;
            asm volatile("" : "+v"(mfrag));
          }
          if (f == 59) { acc[0] = mfma(mfrag, cs_frag(L_CS1), acc[0]); acc[1] = mfma(mfrag, cs_frag(L_CS1 + 256), acc[1]); }
        };
        acc[0] = zero16(); acc[1] = zero16();
        stamp5(c);
        PREDELAY();
        open_phase<1>(c, a, lv, WS_X + 2048 * KB + rtB * 32 * KB, 32);
        stamp(c);
        gemm<64, 2, 0, -1, true, 2 * (32 - AWIN), 0, 31>(c, a, acc, wf, lv, lv, false, side);
      }
    }
  } else if (wave < 6) {
    const int lw = wave - 4;
    const int lv = lane * 16;
    volatile LAS unsigned* full = flg(c, F_FULL + lw);
    if (lw == 0) __builtin_amdgcn_s_setprio(3); else { __builtin_amdgcn_s_setprio(2); *full = 1u; }
    load_slot0(lw);
    VMWAIT(0);
    *full = (unsigned)(2 + lw);
    stamp3(c);
    __builtin_amdgcn_s_setprio(1);
    wait_lds<2>(c, F_QGO + lw, 1u);
    load_slot0(4 + lw);
    VMWAIT(0);
    wait_lds<1>(c, F_FULL + lw, (unsigned)(4 + lw));
    *full = 6u;
    stamp(c); stamp3(c);
    {
      wait_lds4<1>(c, F_FREE, 3u);
      const int pts = L_XCH + lw * 6144;
      const unsigned vo3 = (lane >> 2) * 2752 + (lane & 3) * 16;
      const char* w3 = (const char*)(lw ? wu : wg) + (size_t)(cl * 32) * 2752;
#pragma unroll
      for (int i = 0; i < 3; ++i) {
        const int aa = g0 + min(i, ng - 1);
        glds(w3 + aa * 64, vo3, pts + i * 2048);
        glds(w3 + aa * 64 + 16 * 2752, vo3, pts + i * 2048 + 1024);
      }
      VMWAIT(0);
#pragma unroll
      for (int i = 0; i < 3; ++i) {
        const int aa = g0 + min(i, ng - 1);
        const v4u g0f = tr_frag(L + pts + i * 2048 + trS), g1f = tr_frag(L + pts + i * 2048 + 1024 + trS);
        st_ws(c, g0f, lv, WS_WT3 + aa * 64 * KB + (cl * 4 + lw) * KB);
        st_ws(c, g1f, lv, WS_WT3 + aa * 64 * KB + (cl * 4 + 2 + lw) * KB);
        const float s = red_h(csum(g1f, csum(g0f, 0.f)));
        if (h == 0) __builtin_amdgcn_raw_buffer_store_b32(f2u(s), c.rs, l31 * 4, WS_CSP + aa * 4 * KB + cl * 256 + lw * 128, 16);
      }
    }
    stamp3(c);
    wait_lds2<1>(c, F_JOB, 1u);
    int seq = 6, pub = 6;
    const unsigned vo2 = (lane >> 3) * 1024 + (((lane & 7) ^ (((lane >> 4) & 1) << 2)) * 16);
#pragma unroll 1
    for (int seg = 1; seg < 8; ++seg) {
      const int ph = seg & 3;
      int src = 0, n;
      if (ph == 0) { src = WS_WT1 + m * 96 * KB; n = 6; }
      else if (ph == 1) n = 4;
      else if (ph == 2) { src = WS_WT3 + g0 * 64 * KB; n = 4 * ng; }
      else n = 11;
      if (seg == 2) {
        VMWAIT(0);
        if (pub < seq) { pub = seq; *full = (unsigned)pub; }
        const v2u tag2 = *(volatile LAS v2u*)flg(c, F_TAG);
        if (lane == 0) __builtin_amdgcn_raw_buffer_store_b64(tag2, c.rs, 0, WS_F1 + ((m * 16 + cl) * 4 + lw) * 64, 16);
        stamp3(c);
        if (lw == 0) { poll_tags<64>(c, WS_F1 + m * 4096, tag2); *flg(c, F_PTOK) = 1u; }
        else wait_lds<1>(c, F_PTOK, 1u);
        stamp3(c);
      }
#pragma unroll 1
      for (int i = 0; i < n; ++i) {
        if (seq >= NSLOT) {
          const unsigned need = (unsigned)(seq - NSLOT + 1);
          bool first = true;
#pragma nounroll
          for (unsigned sp = 0; sp < SPIN_CAP; ++sp) {
            v4u f = *(volatile LAS v4u*)flg(c, F_FREE);
            if ((unsigned)__builtin_amdgcn_readfirstlane(min(min(f.x, f.y), min(f.z, f.w))) >= need) break;
            if (first && pub < seq) { VMWAIT(0); pub = seq; *full = (unsigned)pub; }
            first = false;
            __builtin_amdgcn_s_sleep(1);
          }
          MEMBAR();
        }
        const unsigned dst = L_RING + (seq & (NSLOT - 1)) * SLOT + lw * 8192;
        const int hs = i * 2 + lw, hr = ROT ? (hs & ~7) | ((hs + rot) & 7) : hs;
        if (ph & 1) {
#pragma unroll
          for (int q = 0; q < 2; ++q) {
            const int p = min((hs < 16 ? hr : hs) * 2 + q, 42);
            const int m3 = min(p - 32 + 1 + ((p - 32) >> 1), 15);
            const int aa = ph == 1 ? p : (p < 32 ? gsplit(p & 15) + (p >> 4) : gsplit(m3) + 2);
            const char* sp = (const char*)(ph == 1 ? wo : wd) + (size_t)aa * 32768 + cg * 128;
#pragma unroll
            for (int e = 0; e < 4; ++e) glds(sp + e * 8192, vo2, dst + q * 4096 + e * 1024);
          }
        } else if (ph == 0 && i >= 4) {
          const int q0 = (hs - 8) * 2;
          glds4(ws + src + 65536 + ((q0 + (ROT ? rot : 0)) & 7) * 4096, lv, dst);
          glds4(ws + src + 65536 + ((q0 + 1 + (ROT ? rot : 0)) & 7) * 4096, lv, dst + 4096);
        } else {
          glds4(ws + src + hr * 8192, lv, dst);
          glds4(ws + src + hr * 8192 + 4096, lv, dst + 4096);
        }
        ++seq;
        if (seq - pub > LDEP) {
          if (LDEP == 1) VMWAIT(8); else if (LDEP == 2) VMWAIT(16); else VMWAIT(24);
          pub = seq - LDEP; *full = (unsigned)pub;
        }
      }
      stamp(c);
    }
    VMWAIT(0);
    *full = (unsigned)seq;
  } else {
    const int hh = wave - 6;
    const int lv = lane * 16;
    const int pts = L_RING + (6 + hh) * SLOT;
    const unsigned vr = lane >> 2, vsw = (lane & 3) * 16;
    __builtin_amdgcn_s_setprio(3);
    {
      const unsigned vo1 = vr * 3072 + vsw;
      const char* j0 = (const char*)wqkv + (size_t)(cl * 32) * 3072 + m * 64;
      glds(j0 + (1 + hh) * 1024, vo1, pts); glds(j0 + (1 + hh) * 1024 + 16 * 3072, vo1, pts + 1024);
      glds(j0 + hh * 16 * 3072, vo1, pts + 2048);
    }
    const int srow = cl * 128 + m * 8 + hh * 4 + (lane >> 4);
    v4u xs[4];
#pragma unroll
    for (int i = 0; i < 4; ++i) xs[i] = *(const GAS v4u*)((const GAS char*)x + (size_t)srow * 1024 + i * 256 + (lane & 15) * 16);
    const u64 tag = mix64(__hip_atomic_load((GAS u64*)(ws + WS_E), RLX_AGENT));
    VMWAIT(0);
    asm volatile("" : "+v"(xs[0]), "+v"(xs[1]), "+v"(xs[2]), "+v"(xs[3]));
    const v2u tag2 = {(unsigned)tag, (unsigned)(tag >> 32)};
    if (hh == 0) *(volatile LAS v2u*)flg(c, F_TAG) = tag2;
    stamp(c); stamp3(c);
    const unsigned tc = (unsigned)(mix64(tag) >> 32);
    {
      const unsigned tb = (tc >> ((lane & 3) * 8)) & 0xffu;
      const v4u f0 = tr_frag(L + pts + (hh ? trS : trN)), f1 = tr_frag(L + pts + 1024 + (hh ? trS : trN)), f2 = tr_frag(L + pts + 2048 + trN);
      const float s0 = red_h(csum(f1, csum(f0, 0.f))), s2 = red_h(csum(f2, 0.f));
      __builtin_amdgcn_raw_buffer_store_b32((f2u(h ? s2 : s0) & 0xffffff00u) | tb, c.rs, l31 * 4 + h * 4096, WS_C1P + m * 8 * KB + cl * 256 + hh * 128, 16);
      float sa = 0.f, sb = 0.f, qa = 0.f, qb = 0.f;
#pragma unroll
      for (int i = 0; i < 4; ++i) {
        sa = dot2(xs[i].y, ONES2, dot2(xs[i].x, ONES2, sa)); sb = dot2(xs[i].w, ONES2, dot2(xs[i].z, ONES2, sb));
        qa = dot2(xs[i].y, xs[i].y, dot2(xs[i].x, xs[i].x, qa)); qb = dot2(xs[i].w, xs[i].w, dot2(xs[i].z, xs[i].z, qb));
      }
      sa += sb; qa += qb;
      sa += dpp<0x128>(sa); qa += dpp<0x128>(qa); sa += dpp<0x124>(sa); qa += dpp<0x124>(qa);
      sa += dpp<0x122>(sa); qa += dpp<0x122>(qa); sa += dpp<0x121>(sa); qa += dpp<0x121>(qa);
      if ((lane & 15) == 0) st_ws(c, v4u{f2u(sa), tag2.x, f2u(qa), tag2.y}, srow * 16, WS_S0P);
      __builtin_amdgcn_s_setprio(2);
      MEMBAR();
      *flg(c, F_JOB + hh) = 1u;
      load_slot0(2 + hh);
      *flg(c, F_QGO + hh) = 1u;
      VMWAIT(0);
      wait_lds<0>(c, F_FULL + hh, (unsigned)(2 + hh));
      *flg(c, F_FULL + hh) = (unsigned)(4 + hh);
      stamp3(c);
      {
        float t;
#pragma nounroll
        for (unsigned sp = 0; sp < SPIN_CAP; ++sp) {
          unsigned p[16];
#pragma unroll
          for (int i = 0; i < 16; ++i) p[i] = __builtin_amdgcn_raw_buffer_load_b32(c.rs, lane * 4 + i * 256, WS_C1P + m * 8 * KB + hh * 4096, 16);
          SB();
          unsigned chk = 0;
          t = 0.f;
#pragma unroll
          for (int i = 0; i < 16; ++i) { p[i] ^= tb; chk |= p[i]; t += u2f(p[i]); }
          if (!__any((chk & 0xffu) != 0u)) break;
          MEMBAR();
        }
        if (hh) t = red_h(t);
        if (hh == 0 || h == 0) *(LAS v2u*)(L + L_CS1 + hh * 512 + lane * 8) = cs_operand(t);
        MEMBAR();
        *flg(c, F_CS1R + hh) = 1u;
      }
      stamp3(c);
      __builtin_amdgcn_s_setprio(1);
      st_ws(c, f0, lv, WS_WT1 + m * 96 * KB + (cl * 4 + hh) * KB);
      st_ws(c, f1, lv, WS_WT1 + m * 96 * KB + (cl * 4 + 2 + hh) * KB);
      st_ws(c, f2, lv, WS_WT1 + m * 96 * KB + (64 + cl * 2 + hh) * KB);
    }
    auto clear_round = [&](int r) INL {
      const v4u S = {SENT, SENT, SENT, SENT};
      auto st_clr = [&](int vo, int so) INL { st_ws(c, S, vo, so); if (CPACE && r) __builtin_amdgcn_s_sleep(CPACE); };
      const int ly = r == 2, xi = r;
      const int rt = cl * 4 + rh * 2 + hh;
      const int xb = WS_X + xi * 2048 * KB + (rt * 32 + cg * 4) * KB;
#pragma unroll
      for (int e = 0; e < 4; ++e) st_clr( lv + e * KB, xb);
      if (h == 0) st_ws(c, S, lv, WS_ST + (xi * 64 + rt) * 4 * KB + cg * 512);
#pragma unroll 1
      for (int ww = 2 * hh; ww < 2 * hh + 2; ++ww) {
        if (r != 1) {
          const int ob = WS_O + ly * 2048 * KB + ((cl * 4 + ww) * 32 + 2 * m) * KB;
          st_clr( lv, ob); st_clr( lv + KB, ob);
        }
        if (r != 0) {
#pragma unroll 1
          for (int gi = 0; gi < ng; ++gi) {
            const int hb = WS_H + ly * 64 * 86 * KB + ((cl * 4 + ww) * 86 + (gi * 16 + (gi < 2 ? m : g0 - 2 * m)) * 2) * KB;
            st_clr( lv, hb); st_clr( lv + KB, hb);
          }
        }
      }
    };
    auto clear_flag = [&](int r) INL {
      if (lane == 0) __builtin_amdgcn_raw_buffer_store_b64(tag2, c.rs, 0, WS_F2 + (((r * 16 + cl) * 16 + m) * 2 + hh) * 64, 16);
    };
    auto clear_poll = [&](int r) INL { poll_tags<32>(c, WS_F2 + (r * 16 + cl) * 2048, tag2); *flg(c, F_CLR) = (unsigned)(r + 1); };
    clear_round(0);
    stamp3(c);
    VMWAIT(0);
    clear_flag(0);
    if (lane == 0) __builtin_amdgcn_raw_buffer_store_b64(tag2, c.rs, 0, WS_F1 + ((m * 16 + cl) * 4 + 2 + hh) * 64, 16);
    stamp3(c);
    if (hh == 1) clear_poll(0);
    stamp(c); stamp3(c);
#pragma unroll 1
    for (int layer = 0; layer < 2; ++layer) {
      const unsigned tg = layer ? tag2.y : tag2.x;
      const v4u T = {tg & 0xffu, (tg >> 8) & 0xffu, (tg >> 16) & 0xffu, tg >> 24};
      const int kvb = WS_KVP + ((layer * 16 + m) * 2 + hh) * 16 * KB, zb = WS_ZP + (layer * 16 + m) * 2 * KB + hh * 64;
      const int zv = (lane >> 2) * 128 + (lane & 3) * 16;
      wait_lds<2>(c, F_KFL, 4u * (unsigned)(layer + 1));
      __builtin_amdgcn_s_setprio(3);
      stamp(c);
      if (KVDELAY) __builtin_amdgcn_s_sleep(KVDELAY);
      v4u p[17];
#define AF(i) __builtin_bit_cast(f32x4, p[i])
      if (KVMODE == 0) {
        unsigned need = 0x1ffffu;
#pragma nounroll
        for (unsigned sp = 0; sp < SPIN_CAP && need; ++sp) {
          sfor<17>([&](auto I) INL { if (need & (1u << I.v)) p[I.v] = I.v == 16 ? ld_ws(c, zv, zb) : ld_fr(c, lv, kvb, I.v); });
          sfor<17>([&](auto I) INL {
            if (need & (1u << I.v)) {
              p[I.v] ^= T;
              if (!__any(((p[I.v].x | p[I.v].y | p[I.v].z | p[I.v].w) & 0xffu) != 0u)) need &= ~(1u << I.v);
            }
          });
          MEMBAR();
        }
      } else {
#pragma nounroll
        for (unsigned sp = 0; sp < SPIN_CAP; ++sp) {
          p[16] = ld_ws(c, zv, zb) ^ T;
          if (!__any(((p[16].x | p[16].y | p[16].z | p[16].w) & 0xffu) != 0u)) break;
          MEMBAR();
        }
#pragma nounroll
        for (unsigned sp = 0; sp < SPIN_CAP; ++sp) {
#pragma unroll
          for (int i = 0; i < 16; ++i) p[i] = ld_fr(c, lv, kvb, i);
          SB();
          unsigned chk = 0;
#pragma unroll
          for (int i = 0; i < 16; ++i) { p[i] ^= T; chk |= (p[i].x | p[i].y) | (p[i].z | p[i].w); }
          if (!__any((chk & 0xffu) != 0u)) break;
          MEMBAR();
        }
      }
      stamp(c);
      const f32x4 s = (((AF(0) + AF(1)) + (AF(2) + AF(3))) + ((AF(4) + AF(5)) + (AF(6) + AF(7)))) +
                      (((AF(8) + AF(9)) + (AF(10) + AF(11))) + ((AF(12) + AF(13)) + (AF(14) + AF(15))));
      f32x4 zs = AF(16);
#undef AF
      *(LAS v2u*)(L + L_KVF + lv + hh * 8) = v2u{pack2(s[0], s[1]), pack2(s[2], s[3])};
#pragma unroll
      for (int i = 0; i < 4; ++i) { float v = zs[i]; v += dpp<0x128>(v); v += dpp<0x124>(v); zs[i] = red_g(v); }
      if (lane < 4) *(LAS v2u*)(L + L_ZF + hh * 32 + lane * 8) = v2u{pack2(zs[0], zs[1]), pack2(zs[2], zs[3])};
      MEMBAR();
      *flg(c, F_KVR + hh) = (unsigned)(layer + 1);
      MEMBAR();
      __builtin_amdgcn_s_setprio(0);
      stamp(c);
      if (layer == 0) {
        clear_round(1);
        VMWAIT(0);
        clear_flag(1);
        if (hh == 1) {
          wait_lds<2>(c, F_PTOK, 1u);
#pragma unroll
          for (int i = 0; i < 3; ++i) {
            const int aa = g0 + min(i, ng - 1);
            f32x4 t = {0.f, 0.f, 0.f, 0.f};
#pragma unroll
            for (int q = 0; q < 4; ++q) t += __builtin_bit_cast(f32x4, ld_fr(c, lv, WS_CSP + aa * 4 * KB, q));
#pragma unroll
            for (int e = 0; e < 4; ++e) {
              const v2u o = cs_operand(red_g(t[e]));
              if (lane < 16) *(LAS v2u*)(L + L_CS3 + i * 512 + (lane * 4 + e) * 8) = o;
            }
          }
          MEMBAR();
          *flg(c, F_CS3R) = 1u;
          MEMBAR();
        } else {
          clear_poll(1);
        }
        stamp(c);
        wait_lds<8>(c, F_FREE, R2AT);
        clear_round(2);
        VMWAIT(0);
        clear_flag(2);
        if (hh == 0) {
          clear_poll(2);
          if (b == 0 && lane == 0) __hip_atomic_store((GAS u64*)(ws + WS_E), tag, RLX_AGENT);
        }
        stamp(c);
      }
    }
  }
  asm volatile("" ::"v"(warm));
}

extern "C" void kernel_launch(void* const* d_in, const int* in_sizes, int n_in, void* d_out, int out_size, void* d_ws, size_t ws_size,
                              hipStream_t stream) {
  fused_fwd<<<256, 512, 0, stream>>>((const u16*)d_in[0], (const u16*)d_in[1], (const u16*)d_in[2], (const u16*)d_in[4], (const u16*)d_in[5],
                                     (const u16*)d_in[6], (u16*)d_out, (char*)d_ws);
}
extern "C" int dbg_offset() { return WS_DBG; }
extern "C" int ws_off(int i) { const int t[] = {WS_WT1, 0, WS_WT3, 0, WS_CSP, WS_KVP, WS_ZP, WS_ST, WS_O, WS_X, WS_H}; return t[i]; }
```

```cpp
#include <hip/hip_runtime.h>
#include <stdint.h>
#include <utility>

#define GAS __attribute__((address_space(1)))
#define LAS __attribute__((address_space(3)))
#define RLX_AGENT __ATOMIC_RELAXED, __HIP_MEMORY_SCOPE_AGENT
typedef unsigned v4u __attribute__((ext_vector_type(4)));
typedef unsigned v2u __attribute__((ext_vector_type(2)));
typedef float f32x4 __attribute__((ext_vector_type(4)));
typedef float f32x16 __attribute__((ext_vector_type(16)));
typedef short s16x8 __attribute__((ext_vector_type(8)));
typedef short s16x4 __attribute__((ext_vector_type(4)));
typedef __bf16 bf16x2 __attribute__((ext_vector_type(2)));
typedef unsigned short u16;
typedef unsigned long long u64;

#define STAMP 0
#define LDEP 2
#define XDELAY 12
#define TOUCH 0
#define WPACE 2
#define S2H 46
#define BACKOFF 4
#define PTOUCH 1
#define P4CAP 7
#define CODEPF 49152

constexpr int KB = 1024;
constexpr int WS_E = 0;
constexpr int WS_F1 = 4 * KB;
constexpr int WS_WT1 = 128 * KB;
constexpr int WS_WT3 = WS_WT1 + 16 * 96 * KB;
constexpr int WS_CSP = WS_WT3 + 43 * 64 * KB;
constexpr int WS_S0P = WS_CSP + 43 * 4 * KB;
constexpr int WS_C1P = WS_S0P + 32 * KB;
constexpr int WS_KVP = WS_C1P + 16 * 8 * KB;
constexpr int WS_ZP = WS_KVP + 2 * 32 * 16 * KB;
constexpr int REC = 2560;
constexpr int WS_ACT = 6 * KB * KB, ACT_CL = 2 * KB * KB;
constexpr int WS_O = WS_ACT;
constexpr int WS_X = WS_O + 2 * 4 * 16 * REC;
constexpr int WS_H = WS_X + 3 * 4 * 16 * REC;
static_assert(WS_ZP + 2 * 32 * KB <= WS_ACT && WS_H + (2 * 4 * 43 + 1) * REC <= WS_ACT + ACT_CL, "ws");
constexpr int WS_DBG = WS_ACT + 16 * ACT_CL;
constexpr int WS_END = WS_DBG + 256 * 2 * KB;

constexpr int SLOT = 16384, NSLOT = 8;
constexpr int NSTAMP = 36;
constexpr int L_FLG = 0;
constexpr int L_ZF = L_FLG + 128;
constexpr int L_CS = L_ZF + 64;
constexpr int L_KVF = L_CS + 9 * 512;
constexpr int L_STAMP = L_KVF + 1024;
constexpr int L_KVS = L_STAMP + 1152;
constexpr int L_XCH = 16384;
constexpr int L_RING = 32768;
constexpr int L_END = L_RING + NSLOT * SLOT;
static_assert(L_KVS + 4 * 2304 <= L_XCH && L_END == 163840 && (L_KVS & 15) == 0 && (L_CS & 15) == 0, "lds");
enum { F_FULL = 0, F_TAG = 2, F_FREE = 4, F_JOB = 8, F_KFL = 12, F_QGO = 24, F_CS3R = 15, F_KVR = 16, F_PTOK = 18, F_XF = 20, F_CS1R = 28 };
constexpr unsigned SPIN_CAP = 1u << 17;

template <int V> struct IC { static constexpr int v = V; __device__ constexpr operator int() const { return V; } };
template <class F, int... I> __device__ __forceinline__ void sfor_(F&& fn, std::integer_sequence<int, I...>) { (fn(IC<I>{}), ...); }
template <int N, class F> __device__ __forceinline__ void sfor(F&& fn) { sfor_(fn, std::make_integer_sequence<int, N>{}); }
#define INL __attribute__((always_inline))
#define MEMBAR() asm volatile("" ::: "memory")
#define SB() __builtin_amdgcn_sched_barrier(0)
#define VMWAIT(n) asm volatile("s_waitcnt vmcnt(" #n ")" ::: "memory")
#define PIN(x) asm volatile("" : "+v"(x))

__device__ __forceinline__ unsigned umin(unsigned a, unsigned b) { return a < b ? a : b; }
__device__ __forceinline__ unsigned umax(unsigned a, unsigned b) { return a > b ? a : b; }
__device__ __forceinline__ int gsplit(int i) { return (43 * i) >> 4; }
__device__ __forceinline__ f32x16 mfma(v4u a, v4u b, f32x16 c) {
  return __builtin_amdgcn_mfma_f32_32x32x16_bf16(__builtin_bit_cast(s16x8, a), __builtin_bit_cast(s16x8, b), c, 0, 0, 0);
}
__device__ __forceinline__ f32x16 zero16() { return f32x16{0.f, 0.f, 0.f, 0.f, 0.f, 0.f, 0.f, 0.f, 0.f, 0.f, 0.f, 0.f, 0.f, 0.f, 0.f, 0.f}; }
__device__ __forceinline__ unsigned pack2(float a, float b) {
  bf16x2 v = {(__bf16)a, (__bf16)b};
  return __builtin_bit_cast(unsigned, v);
}
__device__ __forceinline__ float u2f(unsigned u) { return __builtin_bit_cast(float, u); }
__device__ __forceinline__ unsigned f2u(float f) { return __builtin_bit_cast(unsigned, f); }
__device__ __forceinline__ float blo(unsigned u) { return u2f(u << 16); }
__device__ __forceinline__ float bhi(unsigned u) { return u2f(u & 0xffff0000u); }
__device__ __forceinline__ float relu(float x) { return u2f((unsigned)max((int)f2u(x), 0)); }
__device__ __forceinline__ float dot2(unsigned a, unsigned b, float acc) {
  return __builtin_amdgcn_fdot2_f32_bf16(__builtin_bit_cast(bf16x2, a), __builtin_bit_cast(bf16x2, b), acc, false);
}
constexpr unsigned ONES2 = 0x3f803f80u;
constexpr float LOG2E = 1.44269504f;
__device__ __forceinline__ float red_h(float v) {
  auto s = __builtin_amdgcn_permlane32_swap(f2u(v), f2u(v), false, false);
  return u2f((unsigned)s[0]) + u2f((unsigned)s[1]);
}
__device__ __forceinline__ float red_g(float v) {
  auto r = __builtin_amdgcn_permlane16_swap(f2u(v), f2u(v), false, false);
  return red_h(u2f((unsigned)r[0]) + u2f((unsigned)r[1]));
}
template <int CTRL> __device__ __forceinline__ float dpp(float v) { return u2f(__builtin_amdgcn_update_dpp(0, (int)f2u(v), CTRL, 0xf, 0xf, false)); }
__device__ __forceinline__ float exp2c(float x) { return __builtin_amdgcn_fmed3f(__builtin_amdgcn_exp2f(x), 0.f, 1.f); }
__device__ __forceinline__ void split2(float v, unsigned& hi, unsigned& lo) {
  hi = pack2(v, 0.f) & 0xffffu;
  lo = pack2(v - blo(hi), 0.f) & 0xffffu;
}
template <int D = 256> __device__ __forceinline__ v4u tr_frag(LAS char* p) {
  v2u lo = __builtin_bit_cast(v2u, __builtin_amdgcn_ds_read_tr16_b64_v4i16((LAS s16x4*)p));
  v2u hi = __builtin_bit_cast(v2u, __builtin_amdgcn_ds_read_tr16_b64_v4i16((LAS s16x4*)(p + D)));
  return v4u{lo.x, lo.y, hi.x, hi.y};
}
__device__ __forceinline__ void glds(const void* sbase, unsigned voff, unsigned ldst) {
  asm volatile("s_mov_b32 m0, %2\n\ts_nop 0\n\tglobal_load_lds_dwordx4 %0, %1" ::"v"(voff), "s"(sbase), "s"(ldst) : "memory");
}
__device__ __forceinline__ void glds4(const void* sbase, unsigned voff, unsigned ldst) {
  asm volatile(
      "s_mov_b32 m0, %2\n\ts_nop 0\n\tglobal_load_lds_dwordx4 %0, %1 sc1\n\tglobal_load_lds_dwordx4 %0, %1 offset:1024 sc1\n\t"
      "global_load_lds_dwordx4 %0, %1 offset:2048 sc1\n\tglobal_load_lds_dwordx4 %0, %1 offset:3072 sc1" ::"v"(voff), "s"(sbase), "s"(ldst)
      : "memory");
}

struct Ctx {
  LAS char* L;
  __amdgpu_buffer_rsrc_t rs;
  int lane, wave;
  int seq, known;
  int vb0, vb1;
  v2u fpre;
  v4u w0, w1, w2, w3;
  int nst;
  template <int I> __device__ __forceinline__ v4u& wf() { if constexpr (I == 0) return w0; else if constexpr (I == 1) return w1; else if constexpr (I == 2) return w2; else return w3; }
};
__device__ __forceinline__ volatile LAS unsigned* flg(Ctx& c, int i) { return (volatile LAS unsigned*)(c.L + L_FLG) + i; }
__device__ __forceinline__ void stamp0(Ctx& c) {
}
template <int K> __device__ __forceinline__ void stamp(Ctx& c) { if (STAMP == K) stamp0(c); }
template <int N, int SLP> __device__ __forceinline__ void wait_lds(Ctx& c, int i, unsigned val) {
#pragma nounroll
  for (unsigned sp = 0; sp < SPIN_CAP; ++sp) {
    unsigned v;
    if (N == 1) v = *flg(c, i);
    else if (N == 2) { v2u f = *(volatile LAS v2u*)flg(c, i); v = umin(f.x, f.y); }
    else { v4u f = *(volatile LAS v4u*)flg(c, i); v = umin(umin(f.x, f.y), umin(f.z, f.w)); }
    if ((unsigned)__builtin_amdgcn_readfirstlane(v) >= val) break;
    if (SLP) __builtin_amdgcn_s_sleep(SLP);
  }
  MEMBAR();
}
template <int N> __device__ __forceinline__ void poll_tags(Ctx& c, int base, v2u tag) {
#pragma nounroll
  for (unsigned sp = 0; sp < SPIN_CAP; ++sp) {
    const v2u v = __builtin_amdgcn_raw_buffer_load_b64(c.rs, (c.lane & (N - 1)) * 64, base, 16);
    if (__all(v.x == tag.x && v.y == tag.y)) break;
    __builtin_amdgcn_s_sleep(4);
    MEMBAR();
  }
  MEMBAR();
}
__device__ __forceinline__ v4u ld_ws(Ctx& c, int voff, int soff) { return __builtin_amdgcn_raw_buffer_load_b128(c.rs, voff, soff, 16); }
__device__ __forceinline__ void st_ws(Ctx& c, v4u v, int voff, int soff) {
  __builtin_amdgcn_raw_buffer_store_b128(v, c.rs, voff, soff, 16);
  asm volatile("s_nop 1" ::"v"(v) : "memory");
}
__device__ __forceinline__ int opq(int v) { asm volatile("" : "+s"(v)); return v; }
__device__ __forceinline__ int opqv(int v) { asm volatile("" : "+v"(v)); return v; }
__device__ __forceinline__ v4u ld_fr(Ctx& c, int lv, int sb, int i) { return ld_ws(c, lv + (i & 3) * KB, sb + (i >> 2) * 4 * KB); }

template <int MODE, int F, bool AHEAD> __device__ __forceinline__ v4u wread(Ctx& c, int lb0, int lb1) {
  constexpr int FPS = MODE == 1 ? 8 : 16, i = F % FPS;
  if constexpr (i == 0) {
    if (AHEAD) c.known = max(c.known, (int)__builtin_amdgcn_readfirstlane(umin(c.fpre.x, c.fpre.y)));
    if (__builtin_expect(c.known <= c.seq, 0)) {
#pragma nounroll
      for (unsigned sp = 0; sp < SPIN_CAP; ++sp) {
        v2u f = *(volatile LAS v2u*)flg(c, F_FULL);
        c.known = __builtin_amdgcn_readfirstlane(umin(f.x, f.y));
        if (c.known > c.seq) break;
      }
    }
    MEMBAR();
    const int sb = L_RING + (c.seq & (NSLOT - 1)) * SLOT;
    c.vb0 = lb0 + sb;
    if (MODE == 1 || MODE == 2) c.vb1 = lb1 + sb;
  }
  v4u r;
  if (MODE == 0) r = *(LAS v4u*)(c.L + c.vb0 + i * KB);
  else if (MODE == 1) r = tr_frag<512>(c.L + ((i & 1) ? c.vb1 : c.vb0) + ((i >> 1) & 1) * 2 * KB + (i >> 2) * 8 * KB);
  else if (MODE == 2) r = tr_frag(c.L + ((i & 1) ? c.vb1 : c.vb0) + i * KB);
  else r = tr_frag(c.L + c.vb0 + i * KB);
  if (i == FPS - 4) c.fpre = *(volatile LAS v2u*)flg(c, F_FULL);
  return r;
}
__device__ __forceinline__ void ring_publish(Ctx& c) { MEMBAR(); *flg(c, F_FREE + c.wave) = (unsigned)c.seq; MEMBAR(); }
template <int MODE> __device__ __forceinline__ void wpre(Ctx& c, int lb0, int lb1) {
  sfor<4>([&](auto F) INL { c.wf<F.v>() = wread<MODE, F.v, false>(c, lb0, lb1); });
}
struct NoSide { template <class T> __device__ __forceinline__ void operator()(T) const {} };
template <int NF, int TPS, int MODE, int NEXT, bool XA, int ZERO, int RELF, int EARLY, int CAP, int F0 = 0, int F1 = NF, class SIDE>
__device__ __forceinline__ void gemm(Ctx& c, v4u (&a)[32], f32x16& c0, f32x16& c1, int lb0, int lb1, int nb0, int nb1, SIDE side) {
  constexpr int FPS = MODE == 1 ? 8 : 16;
  static_assert(NEXT < 0 || (NF % 4 == 0 && NF % FPS == 0), "chain");
  sfor<F1 - F0>([&](auto FI) INL {
    constexpr IC<F0 + FI.v> F{};
    constexpr int f = F.v, s = f / TPS, t = f % TPS, fn = f + 4;
    const v4u x = a[s & 31], wv = c.wf<f % 4>();
    f32x16& acc = t ? c1 : c0;
    const f32x16 cin = (s == 0 && ((ZERO >> t) & 1)) ? zero16() : acc;
    acc = XA ? mfma(x, wv, cin) : mfma(wv, x, cin);
    SB();
    if constexpr (fn < NF) {
      c.wf<f % 4>() = wread<MODE, fn, true>(c, lb0, lb1);
      if constexpr (fn % FPS == FPS - 1 || fn == NF - 1) { c.seq++; if constexpr ((f >= RELF || fn / FPS < EARLY) && fn / FPS < CAP) ring_publish(c); }
      else if constexpr (f == RELF && RELF > 0) ring_publish(c);
    } else if constexpr (NEXT >= 0) {
      c.wf<f % 4>() = wread<(NEXT < 0 ? 0 : NEXT), fn - NF, true>(c, nb0, nb1);
    }
    side(F);
    SB();
  });
}
__device__ __forceinline__ unsigned sum4(unsigned m, v4u v) { return ((m + v.x + v.y) + v.z + v.w); }
template <bool EX> __device__ __forceinline__ void ld_chk(Ctx& c, v2u& k, int sb) {
  if (EX) k = __builtin_amdgcn_raw_buffer_load_b64(c.rs, c.lane * 8 + 2 * KB, sb, 16);
  else k.x = __builtin_amdgcn_raw_buffer_load_b32(c.rs, c.lane * 4 + 2 * KB, sb, 16);
}
template <int STR, bool EX> __device__ __forceinline__ void ld_rec(Ctx& c, v4u (&a)[32], v2u (&k)[8], int lv, int sb, int p, int e) {
  if (e < 2) a[(2 * p + e) & 31] = ld_ws(c, lv + e * KB, sb + STR * p * REC);
  else ld_chk<EX>(c, k[p & 7], sb + STR * p * REC);
}
template <bool EX> __device__ __forceinline__ unsigned rec_bad(v4u (&a)[32], v2u (&k)[8], int p, unsigned tag) {
  return sum4(sum4(EX ? k[p & 7].x + k[p & 7].y : k[p & 7].x, a[(2 * p) & 31]), a[(2 * p + 1) & 31]) ^ tag;
}
template <int N, int STR, bool EX> __device__ __forceinline__ void settle(Ctx& c, unsigned bad, v4u (&a)[32], v2u (&k)[8], int lv, int sb, int p0, unsigned tag) {
  if (__builtin_expect(__any(bad != 0u), 0)) {
    unsigned sp = 0;
#pragma nounroll
    do {
      if (BACKOFF) __builtin_amdgcn_s_sleep(BACKOFF);
      MEMBAR();
      bad = 0;
#pragma unroll
      for (int i = 0; i < 3 * N; ++i) ld_rec<STR, EX>(c, a, k, lv, sb, p0 + i / 3, i % 3);
#pragma unroll
      for (int i = 0; i < N; ++i) bad |= rec_bad<EX>(a, k, p0 + i, tag);
    } while (__any(bad != 0u) && ++sp < SPIN_CAP);
  }
}
template <int F, int NP, int STR, bool EX>
__device__ __forceinline__ void check_step(Ctx& c, unsigned& m0, unsigned& m1, v4u (&a)[32], v2u (&k)[8], int lv, int sb, unsigned tag) {
  constexpr int p = 2 * ((F + 8) / 8), r = (F + 8) % 8;
  if constexpr (p < NP) {
    if constexpr (r == 3) m0 = sum4(EX ? k[p & 7].x + k[p & 7].y : k[p & 7].x, a[(2 * p) & 31]);
    if constexpr (r == 4) m0 = sum4(m0, a[(2 * p + 1) & 31]) ^ tag;
    if constexpr (r == 5) m1 = sum4(EX ? k[(p + 1) & 7].x + k[(p + 1) & 7].y : k[(p + 1) & 7].x, a[(2 * p + 2) & 31]);
    if constexpr (r == 6) settle<2, STR, EX>(c, m0 | (sum4(m1, a[(2 * p + 3) & 31]) ^ tag), a, k, lv, sb, p, tag);
  }
}
template <int STR, bool EX> __device__ __forceinline__ void open_phase(Ctx& c, v4u (&a)[32], v2u (&k)[8], int lv, int sb, unsigned tag) {
  VMWAIT(0);
  if (XDELAY) __builtin_amdgcn_s_sleep(XDELAY);
#pragma unroll
  for (int i = 0; i < 12; ++i) ld_rec<STR, EX>(c, a, k, lv, sb, i / 3, i % 3);
  SB();
  settle<2, STR, EX>(c, rec_bad<EX>(a, k, 0, tag) | rec_bad<EX>(a, k, 1, tag), a, k, lv, sb, 0, tag);
}
template <int F, int NP, int STR, bool EX> __device__ __forceinline__ void ramp_step(Ctx& c, v4u (&a)[32], v2u (&k)[8], int lv, int sb) {
  if constexpr (F < 12) {
    constexpr int p = 4 + F / 2;
    if constexpr (p < NP) { ld_rec<STR, EX>(c, a, k, lv, sb, p, F & 1); if constexpr (F & 1) ld_rec<STR, EX>(c, a, k, lv, sb, p, 2); }
  } else {
    constexpr int p = 10 + (F - 12) / 4, e = (F - 12) % 4;
    if constexpr (p < NP && e < 3) ld_rec<STR, EX>(c, a, k, lv, sb, p, e);
  }
}
template <bool EX> __device__ __forceinline__ void st_chk(Ctx& c, unsigned sum, unsigned ex, int sb, unsigned tag) {
  if (EX) __builtin_amdgcn_raw_buffer_store_b64(v2u{tag - sum - ex, ex}, c.rs, c.lane * 8 + 2 * KB, sb, 16);
  else __builtin_amdgcn_raw_buffer_store_b32(tag - sum, c.rs, c.lane * 4 + 2 * KB, sb, 16);
}
__device__ __forceinline__ u64 mix64(u64 z) {
  z += 0x9E3779B97F4A7C15ull;
  z = (z ^ (z >> 30)) * 0xBF58476D1CE4E5B9ull;
  z = (z ^ (z >> 27)) * 0x94D049BB133111EBull;
  return z ^ (z >> 31);
}
__device__ __forceinline__ float csum(v4u f, float s) { return dot2(f.w, ONES2, dot2(f.z, ONES2, dot2(f.y, ONES2, dot2(f.x, ONES2, s)))); }
__device__ __forceinline__ v4u cs_operand(float s) { unsigned hi, lo; split2(s, hi, lo); return v4u{hi | (hi << 16), lo, 0u, 0u}; }

__global__ __launch_bounds__(512, 2) void fused_fwd(const u16* __restrict__ x, const u16* __restrict__ wqkv, const u16* __restrict__ wo,
                                                    const u16* __restrict__ wg, const u16* __restrict__ wu, const u16* __restrict__ wd,
                                                    u16* __restrict__ out, char* __restrict__ ws) {
  __shared__ __attribute__((aligned(16))) char smem[L_END];
  u64 pc0;
  asm volatile("s_getpc_b64 %0" : "=s"(pc0));
  Ctx c;
  c.L = (LAS char*)smem;
  c.rs = __builtin_amdgcn_make_buffer_rsrc((void*)ws, 0, WS_END, 0x00020000);
  const int lane = threadIdx.x & 63, wave = __builtin_amdgcn_readfirstlane(threadIdx.x >> 6);
  const int b = blockIdx.x, xc = b & 7, j = b >> 3;
  const int cl = (xc >> 1) * 4 + (j >> 3), m = (xc & 1) * 8 + (j & 7);
  const int rh = m >> 3, cg = m & 7;
  const int g0 = gsplit(m), ng = gsplit(m + 1) - g0;
  c.lane = lane; c.wave = wave; c.seq = 0; c.known = 0; c.nst = 0; c.vb0 = 0; c.vb1 = 0; c.fpre = v2u{0u, 0u};
  const int rot = (2 * cl + m) & 7;
  const int h = lane >> 5, l31 = lane & 31;
  LAS char* L = c.L;
  if (wave == 0 && lane < 32) *flg(c, lane) = 0u;
  stamp0(c);
  asm volatile("s_waitcnt lgkmcnt(0)\n\ts_barrier" ::: "memory");
  const int trN = (8 * h + ((lane >> 2) & 3)) * 64 + ((lane >> 4) & 1) * 32 + (lane & 3) * 8;
  const int trS = (8 * h + ((lane >> 2) & 3)) * 64 + ((lane >> 4) & 1) * 32 + (((lane & 1) << 1) | ((lane >> 1) & 1)) * 8;
  auto load_slot0 = [&](int sl) INL {
    const unsigned vo1 = (lane >> 2) * 3072 + (lane & 3) * 16;
    const char* wb = (const char*)wqkv + m * 64;
#pragma unroll 1
    for (int p = 0; p < 16; ++p) {
      const int s = sl < 4 ? sl * 8 + (p >> 1) : (sl - 4) * 16 + p;
      glds(wb + (sl < 4 ? (1 + (p & 1)) * 1024 : 0) + (size_t)(((s + 4 * rot) & 31) * 16) * 3072, vo1, L_RING + sl * SLOT + p * 1024);
    }
  };
  const int pp = wave & 1;
  auto cache_load = [&](auto& wc, auto NK) INL {
    const __amdgpu_buffer_rsrc_t rd = __builtin_amdgcn_make_buffer_rsrc((void*)wd, 0, 1376 * 1024, 0x00020000);
    const int vo2 = (lane >> 3) * 1024 + (((lane & 7) ^ (((lane >> 4) & 1) << 2)) * 16);
#pragma unroll
    for (int k = 0; k < NK.v; ++k) {
#pragma unroll
      for (int q = 0; q < 2; ++q) {
        const int p = min(((NK.v == 4 ? 2 * k + 3 : k < 3 ? k : 2 * k - 2) * 2 + pp) * 2 + q, 42);
        const int m3 = min(p - 32 + 1 + ((p - 32) >> 1), 15);
        const int aa = p < 32 ? gsplit(p & 15) + (p >> 4) : gsplit(m3) + 2;
#pragma unroll
        for (int e = 0; e < 4; ++e) wc[k * 8 + q * 4 + e] = __builtin_amdgcn_raw_buffer_load_b128(rd, vo2, aa * 32768 + cg * 128 + e * 8192, 0);
        SB();
        if (WPACE && NK.v == 4) __builtin_amdgcn_s_sleep(WPACE);
      }
    }
  };
  auto cache_put = [&](auto& wc, auto NK, int base) INL {
#pragma unroll
    for (int k = 0; k < NK.v; ++k) {
      const int sq = base + (NK.v == 4 ? 2 * k + 3 : k < 3 ? k : 2 * k - 2);
      wait_lds<4, 1>(c, F_FREE, (unsigned)(sq - NSLOT + 1));
      LAS char* d = L + L_RING + (sq & (NSLOT - 1)) * SLOT + pp * 8192 + lane * 16;
#pragma unroll
      for (int e = 0; e < 8; ++e) *(LAS v4u*)(d + e * KB) = wc[k * 8 + e];
      wait_lds<1, 1>(c, F_FULL + pp, (unsigned)sq);
      *flg(c, F_FULL + pp) = (unsigned)(sq + 1);
      MEMBAR();
    }
  };
  auto hpos = [&](int gi) INL { return gi * 16 + (gi < 2 ? m : g0 - 2 * m); };

  if (wave < 4) {
    __builtin_amdgcn_s_setprio(2);
    const int w = wave, mh = w >> 1, kh = w & 1;
    v4u a[32], xr0, xr1;
    v2u k[8];
    f32x16 A0, A1, B0, B1;
    const int lv = lane * 16;
    float rstd;
    unsigned tagx, tagy, tagc;
    v4u mfrag;
    auto set_stats = [&](float sum, float ssq) INL -> float {
      const float s = sum * (1.f / 512), q = ssq * (1.f / 512);
      unsigned hi, lo;
      split2(-s, hi, lo);
      mfrag = h == 0 ? v4u{hi | (lo << 16), hi, 0u, 0u} : v4u{0u, 0u, 0u, 0u};
      return fmaxf(q - s * s, 0.f) + 1e-5f;
    };
    auto cs_frag = [&](int tile) INL { return *(LAS v4u*)(L + L_CS + tile * 512 + l31 * 16); };
    auto rs_put = [&]() INL { *(LAS float*)(L + L_KVS + w * 2304 + lane * 4) = h ? rstd * LOG2E : rstd; MEMBAR(); };
    auto rs_get = [&](int q) INL {
      const f32x4 r = *(LAS f32x4*)(L + L_KVS + w * 2304 + (8 * q + 4 * h) * 4), rl = *(LAS f32x4*)(L + L_KVS + w * 2304 + 128 + (8 * q + 4 * h) * 4);
#pragma unroll
      for (int i = 0; i < 4; ++i) { B0[4 * q + i] = r[i]; B1[4 * q + i] = rl[i]; }
    };
    {
      const __amdgpu_buffer_rsrc_t rx = __builtin_amdgcn_make_buffer_rsrc((void*)x, 0, 2048 * 1024, 0x00020000);
      const int xv = (lane >> 3) * 1024 + (lane & 7) * 16, xs = (cl * 128 + w * 32) * 1024;
      auto ldx = [&](int i) INL { a[i] = __builtin_amdgcn_raw_buffer_load_b128(rx, xv, xs + (i & 3) * 8192 + (((i >> 2) + rot) & 7) * 128, 0); };
#pragma unroll
      for (int i = 0; i < 8; ++i) ldx(i);
      SB();
      stamp<3>(c);
      LAS char* sc = L + L_XCH + w * 4096;
      const int r8 = lane >> 3;
      const int wo_ = r8 * 128 + (((lane & 7) ^ (r8 >> 1)) * 16);
      const int ro = l31 * 128 + ((h ^ ((l31 >> 1) & 7)) * 16);
      auto xpose = [&](int kp) INL {
#pragma unroll
        for (int q = 0; q < 4; ++q) *(LAS v4u*)(sc + ((wo_ + q * 1024) ^ ((q & 1) * 64))) = a[kp * 4 + q];
        MEMBAR();
#pragma unroll
        for (int q = 0; q < 4; ++q) a[kp * 4 + q] = *(LAS v4u*)(sc + (ro ^ (q * 32)));
        MEMBAR();
      };
      xpose(0);
#pragma unroll
      for (int i = 8; i < 16; ++i) ldx(i);
      xpose(1);
      stamp<3>(c);
      wpre<2>(c, trN, trS);
      stamp<3>(c);
      v4u s0;
      const int s0b = WS_S0P + (cl * 128 + w * 32) * 16;
      gemm<64, 2, 2, -1, true, 3, 0, 0, 99>(c, a, A0, A1, trN, trS, 0, 0, [&](auto F) INL {
        constexpr int f = F.v;
        if (f < 16) ldx(16 + f);
        if ((f & 7) == 2 && (f >> 3) + 2 < 8) xpose((f >> 3) + 2);
        if (f == 36) s0 = ld_ws(c, l31 * 16, s0b);
        if (f == 52) {
          wait_lds<2, 0>(c, F_CS1R, 1u);
          const v2u tg = *(volatile LAS v2u*)flg(c, F_TAG);
          tagx = __builtin_amdgcn_readfirstlane(tg.x); tagy = __builtin_amdgcn_readfirstlane(tg.y); tagc = tagx * 0x9E3779B1u + tagy;
          if (__any(s0.y != tagx || s0.w != tagy)) {
            unsigned sp = 0;
#pragma nounroll
            do { MEMBAR(); s0 = ld_ws(c, l31 * 16, s0b); } while (__any(s0.y != tagx || s0.w != tagy) && ++sp < SPIN_CAP);
          }
          rstd = __builtin_amdgcn_rsqf(set_stats(u2f(s0.x), u2f(s0.z)));
          rs_put();
          xr0 = cs_frag(0); xr1 = cs_frag(1);
        }
        if (f == 57) { A0 = mfma(mfrag, xr0, A0); A1 = mfma(mfrag, xr1, A1); }
        if (f >= 58 && f < 62) rs_get(f - 58);
        if ((f & 15) == 15) stamp<3>(c);
      });
      const int xq = l31 * 1024 + h * 16, xqs = (cl * 128 + rh * 64 + mh * 32) * 1024 + cg * 128 + kh * 64;
      xr0 = __builtin_amdgcn_raw_buffer_load_b128(rx, xq, xqs, 0);
      xr1 = __builtin_amdgcn_raw_buffer_load_b128(rx, xq + 32, xqs, 0);
    }
#pragma unroll 1
    for (int layer = 0; layer < 2; ++layer) {
      stamp<1>(c);
      {
#pragma unroll
        for (int i = 0; i < 16; ++i) B1[i] *= A0[i];
        SB();
#pragma unroll
        for (int i = 0; i < 16; ++i) { B1[i] = exp2c(B1[i]); A0[i] = fmaf(A0[i], B0[i], 1.f); A1[i] *= B0[i]; }
        SB();
        v4u pk0, pk1, vv0, vv1;
#pragma unroll
        for (int i = 0; i < 16; ++i) A0[i] = fmaxf(A0[i], B1[i]) + 1e-6f;
#pragma unroll
        for (int i = 0; i < 4; ++i) {
          pk0[i] = pack2(A0[2 * i], A0[2 * i + 1]); vv0[i] = pack2(A1[2 * i], A1[2 * i + 1]);
          pk1[i] = pack2(A0[8 + 2 * i], A0[9 + 2 * i]); vv1[i] = pack2(A1[8 + 2 * i], A1[9 + 2 * i]);
        }
        f32x16 kv = mfma(pk0, vv0, zero16());
        kv = mfma(pk1, vv1, kv);
        const float z = red_h(csum(pk0, 0.f) + csum(pk1, 0.f));
        LAS char* ks = L + L_KVS + w * 2304;
        if (h == 0) *(LAS float*)(ks + 2048 + l31 * 4) = z;
        if (lane & 16) {
          *(LAS f32x4*)(ks + lv) = f32x4{kv[8], kv[9], kv[10], kv[11]};
          *(LAS f32x4*)(ks + 1024 + lv) = f32x4{kv[12], kv[13], kv[14], kv[15]};
          MEMBAR();
        } else {
          MEMBAR();
          *(LAS f32x4*)(ks + lv) = f32x4{kv[0], kv[1], kv[2], kv[3]};
          *(LAS f32x4*)(ks + 1024 + lv) = f32x4{kv[4], kv[5], kv[6], kv[7]};
        }
        MEMBAR();
        unsigned old = 0;
        if (lane == 0) old = __hip_atomic_fetch_add((LAS unsigned*)flg(c, F_KFL), 1u, __ATOMIC_RELAXED, __HIP_MEMORY_SCOPE_WORKGROUP);
        if ((__builtin_amdgcn_readfirstlane(old) & 3u) == 3u) {
          MEMBAR();
          f32x4 k0 = {0.f, 0.f, 0.f, 0.f}, k1 = k0, zz = k0;
#pragma unroll
          for (int ww = 0; ww < 4; ++ww) {
            k0 += *(LAS f32x4*)(L + L_KVS + ww * 2304 + lv);
            k1 += *(LAS f32x4*)(L + L_KVS + ww * 2304 + 1024 + lv);
            zz += *(LAS f32x4*)(L + L_KVS + ww * 2304 + 2048 + (lane & 7) * 16);
          }
          const unsigned tg = layer ? tagy : tagx;
          const v4u T = {tg & 0xffu, (tg >> 8) & 0xffu, (tg >> 16) & 0xffu, tg >> 24};
          const int kvb = WS_KVP + ((layer * 16 + m) * 32 + cl) * KB;
          st_ws(c, (__builtin_bit_cast(v4u, k0) & 0xffffff00u) | T, lv, kvb);
          st_ws(c, (__builtin_bit_cast(v4u, k1) & 0xffffff00u) | T, lv, kvb + 16 * KB);
          if (lane < 8) st_ws(c, (__builtin_bit_cast(v4u, zz) & 0xffffff00u) | T, lv, WS_ZP + (layer * 16 + m) * 2 * KB + cl * 128);
        }
        MEMBAR();
      }
      stamp<1>(c);
      B0 = mfma(cs_frag(2), mfrag, zero16());
      if (layer == 0) { wpre<3>(c, trN, trN); gemm<32, 1, 3, -1, false, 0, 0, 0, 99>(c, a, B0, B0, trN, trN, 0, 0, NoSide()); }
      else { wpre<0>(c, lv, lv); gemm<32, 1, 0, -1, false, 0, 0, 0, 99>(c, a, B0, B0, lv, lv, 0, 0, NoSide()); }
      const int lq = opqv(lane);
      const int kb0 = (8 * (lq >> 5) + ((lq >> 2) & 3)) * 128 + ((lq >> 4) & 1) * 32 + (((lq & 1) << 1) | ((lq >> 1) & 1)) * 8 + kh * 4096 + ((kh ^ (lq >> 3)) & 1) * 64;
      const int kb1 = kb0 ^ 64;
      auto res_init = [&](int i) INL { const unsigned u = i < 4 ? xr0[i & 3] : xr1[i & 3]; A0[2 * i] = blo(u); A0[2 * i + 1] = bhi(u); };
      {
        v4u pq0, pq1;
        const float rl = rstd * LOG2E;
#pragma unroll
        for (int i = 0; i < 16; ++i) B1[i] = exp2c(B0[i] * rl);
#pragma unroll
        for (int i = 0; i < 16; ++i) B0[i] = fmaxf(fmaf(B0[i], rstd, 1.f), B1[i]) + 1e-6f;
#pragma unroll
        for (int i = 0; i < 4; ++i) { pq0[i] = pack2(B0[2 * i], B0[2 * i + 1]); pq1[i] = pack2(B0[8 + 2 * i], B0[9 + 2 * i]); }
#pragma unroll
        for (int i = 0; i < 8; ++i) res_init(i);
        PIN(pq0); PIN(pq1); PIN(A0);
        wpre<1>(c, kb0, kb1);
        const int ob = opq(WS_O + cl * ACT_CL + ((layer * 4 + w) * 16 + m) * REC);
        stamp<1>(c);
        v4u kvf;
        v2u z0, z1, z2, z3;
#pragma nounroll
        for (unsigned sp = 0; sp < SPIN_CAP; ++sp) {
          const v2u fl = *(volatile LAS v2u*)flg(c, F_KVR);
          MEMBAR();
          kvf = *(LAS v4u*)(L + L_KVF + lv);
          z0 = *(LAS v2u*)(L + L_ZF + h * 8); z1 = *(LAS v2u*)(L + L_ZF + 16 + h * 8);
          z2 = *(LAS v2u*)(L + L_ZF + 32 + h * 8); z3 = *(LAS v2u*)(L + L_ZF + 48 + h * 8);
          MEMBAR();
          if ((unsigned)__builtin_amdgcn_readfirstlane(umin(fl.x, fl.y)) > (unsigned)layer) break;
        }
        stamp<1>(c);
        const bool up = (lane & 16) != 0;
        const v4u zz = {0u, 0u, 0u, 0u};
        B0 = mfma(up ? zz : kvf, pq0, zero16());
        B0 = mfma(up ? kvf : zz, pq1, B0);
        const float d0 = dot2(pq0.y, z0.y, dot2(pq0.x, z0.x, 0.f)) + dot2(pq0.w, z1.y, dot2(pq0.z, z1.x, 0.f));
        const float d1 = dot2(pq1.y, z2.y, dot2(pq1.x, z2.x, 0.f)) + dot2(pq1.w, z3.y, dot2(pq1.z, z3.x, 0.f));
        const float r0 = __builtin_amdgcn_rcpf(red_h(d0) + 1e-6f), r1 = __builtin_amdgcn_rcpf(red_h(d1) + 1e-6f);
        v4u o0, o1;
#pragma unroll
        for (int i = 0; i < 4; ++i) { o0[i] = pack2(B0[2 * i] * r0, B0[2 * i + 1] * r0); o1[i] = pack2(B0[8 + 2 * i] * r1, B0[9 + 2 * i] * r1); }
        st_ws(c, o0, lv, ob);
        st_ws(c, o1, lv + KB, ob);
        st_chk<false>(c, sum4(sum4(0u, o0), o1), 0u, ob, tagc);
      }
      stamp<1>(c);
      const int rtA = opq(rh * 2 + mh), clb = opq(cl * ACT_CL);
      auto res_epilogue = [&](int xi, bool final_out, unsigned xseq) INL {
        LAS char* xs = L + L_XCH;
#pragma unroll
        for (int q = 0; q < 4; ++q) *(LAS f32x4*)(xs + w * 4096 + q * 1024 + lv) = f32x4{A1[4 * q], A1[4 * q + 1], A1[4 * q + 2], A1[4 * q + 3]};
        MEMBAR();
        *flg(c, F_XF + w) = xseq;
        f32x4 o0, o1, o2, o3;
#pragma nounroll
        for (unsigned sp = 0; sp < SPIN_CAP; ++sp) {
          const unsigned fl = *flg(c, F_XF + (w ^ 1));
          MEMBAR();
          o0 = *(LAS f32x4*)(xs + (w ^ 1) * 4096 + lv); o1 = *(LAS f32x4*)(xs + (w ^ 1) * 4096 + 1024 + lv);
          o2 = *(LAS f32x4*)(xs + (w ^ 1) * 4096 + 2048 + lv); o3 = *(LAS f32x4*)(xs + (w ^ 1) * 4096 + 3072 + lv);
          MEMBAR();
          if ((unsigned)__builtin_amdgcn_readfirstlane(fl) >= xseq) break;
        }
        xr0 = v4u{pack2(A0[0] + o0[0], A0[1] + o0[1]), pack2(A0[2] + o0[2], A0[3] + o0[3]), pack2(A0[4] + o1[0], A0[5] + o1[1]), pack2(A0[6] + o1[2], A0[7] + o1[3])};
        xr1 = v4u{pack2(A0[8] + o2[0], A0[9] + o2[1]), pack2(A0[10] + o2[2], A0[11] + o2[3]), pack2(A0[12] + o3[0], A0[13] + o3[1]), pack2(A0[14] + o3[2], A0[15] + o3[3])};
        if (final_out) {
          const int lo = opqv(lane);
          GAS char* ob = (GAS char*)out + (size_t)((cl * 4 + rtA) * 32 + (lo & 31)) * 1024 + cg * 128 + kh * 64 + (lo >> 5) * 16;
          *(GAS v4u*)ob = xr0;
          *(GAS v4u*)(ob + 32) = xr1;
        } else {
          const int xb = WS_X + clb + ((xi * 4 + rtA) * 16 + cg * 2 + kh) * REC;
          st_ws(c, xr0, lv, xb);
          st_ws(c, xr1, lv + KB, xb);
          const float s = csum(xr0, 0.f) + csum(xr1, 0.f);
          float q0 = 0.f, q1 = 0.f;
#pragma unroll
          for (int i = 0; i < 4; ++i) { q0 = dot2(xr0[i], xr0[i], q0); q1 = dot2(xr1[i], xr1[i], q1); }
          const auto sw = __builtin_amdgcn_permlane32_swap(f2u(s), f2u(q0 + q1), false, false);
          st_chk<true>(c, sum4(sum4(0u, xr0), xr1), f2u(u2f((unsigned)sw[0]) + u2f((unsigned)sw[1])), xb, tagc);
        }
      };
      unsigned m0, m1;
      {
        const int ab = WS_O + clb + ((layer * 4 + rtA) * 16 + kh) * REC;
        open_phase<2, false>(c, a, k, lv, ab, tagc);
        stamp<1>(c);
        gemm<32, 2, 1, -1, false, 2, 8, 0, 99>(c, a, A0, A1, kb0, kb1, 0, 0, [&](auto F) INL {
          ramp_step<F.v, 8, 2, false>(c, a, k, lv, ab);
          check_step<F.v, 8, 2, false>(c, m0, m1, a, k, lv, ab, tagc);
        });
        stamp<1>(c);
        res_epilogue(layer * 2, false, (unsigned)(layer * 2 + 1));
      }
      wpre<0>(c, lv, lv);
      stamp<1>(c);
      const int rtB = w;
      v4u cs0, cs1;
      float sacc;
      auto ln_side = [&](auto F, int xb, float& var) INL -> bool {
        constexpr int f = F.v;
        if constexpr (f == 0) { sacc = u2f(k[0].y) + u2f(k[1].y); PIN(sacc); }
        ramp_step<f, 16, 1, true>(c, a, k, lv, xb);
        check_step<f, 16, 1, true>(c, m0, m1, a, k, lv, xb, tagc);
        if constexpr ((f & 7) == 7 && f < 56) { sacc += u2f(k[(f / 4 + 1) & 7].y) + u2f(k[(f / 4 + 2) & 7].y); PIN(sacc); }
        if constexpr (f == 56) {
          const auto sw = __builtin_amdgcn_permlane32_swap(f2u(sacc), f2u(sacc), false, false);
          var = set_stats(u2f((unsigned)sw[0]), u2f((unsigned)sw[1]));
          PIN(mfrag);
          return true;
        }
        return false;
      };
      const int ab4 = WS_H + clb + ((layer * 4 + rtA) * 43 + kh) * REC;
      const int hb = WS_H + clb + (layer * 4 + rtB) * 43 * REC;
      float r2;
      v4u ho;
      unsigned hs;
      auto hside = [&](auto F, auto R, f32x16& G, f32x16& U, int gi) INL {
        constexpr int f = F.v, r = R.v, sl = f / r - 1;
        if constexpr (f % r == 0 && sl >= 0 && sl < 8) {
          ho[sl & 3] = pack2(relu(G[2 * sl]) * r2 * U[2 * sl], relu(G[2 * sl + 1]) * r2 * U[2 * sl + 1]);
          PIN(ho);
        }
        if constexpr (f == 5 * r - 1 + (r == 1) * 0 && r > 1 || f == 9 * r - 1 && r > 1 || (r == 1 && (f == 4 || f == 8))) {
          constexpr bool second = sl >= 4;
          st_ws(c, ho, lv + second * KB, hb + hpos(gi) * REC);
          hs = sum4(second ? hs : 0u, ho);
          PIN(hs);
          if constexpr (second) {
            st_chk<false>(c, hs, 0u, hb + hpos(gi) * REC, tagc);
            if (lane == 0) __builtin_amdgcn_raw_buffer_store_b32(tagc, c.rs, 2304, hb + hpos(gi) * REC, 16);
          }
        }
      };
        auto hgate = [&](int p0, int n) INL {
#pragma nounroll
          for (unsigned sp = 0; sp < SPIN_CAP; ++sp) {
            const unsigned p = __builtin_amdgcn_raw_buffer_load_b32(c.rs, min(lane, n - 1) * (2 * REC) + 2304, ab4 + 2 * p0 * REC, 16);
            if (!__any(p != tagc)) break;
            __builtin_amdgcn_s_sleep(4);
            MEMBAR();
          }
        };
      auto corr = [&](auto F, int at, f32x16& G, f32x16& U, int gi) INL {
        if (F.v == at - 4) { cs0 = cs_frag(3 + 2 * gi); cs1 = cs_frag(4 + 2 * gi); }
        if (F.v == at) { G = mfma(cs0, mfrag, G); U = mfma(cs1, mfrag, U); }
      };
      const int xb3 = WS_X + clb + (layer * 2 * 4 + rtB) * 16 * REC;
      open_phase<1, true>(c, a, k, lv, xb3, tagc);
      stamp<1>(c); stamp<2>(c);
      gemm<64, 2, 0, 0, false, 3, 34, 0, 99>(c, a, A0, A1, lv, lv, lv, lv, [&](auto F) INL {
        float var;
        if (ln_side(F, xb3, var)) { r2 = __builtin_amdgcn_rcpf(var); PIN(r2); }
        if (F.v == 54 && layer == 0) wait_lds<1, 0>(c, F_CS3R, 1u);
        corr(F, 61, A0, A1, 0);
      });
      stamp<2>(c);
      if (ng == 3) {
        gemm<64, 2, 0, 0, false, 3, 0, 0, 99>(c, a, B0, B1, lv, lv, lv, lv, [&](auto F) INL {
          hside(F, IC<2>{}, A0, A1, 0);
          corr(F, 25, B0, B1, 1);
        });
        A0 = B0; A1 = B1;
        PIN(A0); PIN(A1);
      }
      stamp<2>(c);
      gemm<64, 2, 0, 1, false, 3, 0, 0, 99>(c, a, B0, B1, lv, lv, kb0, kb1, [&](auto F) INL {
        constexpr int f = F.v;
        hside(F, IC<2>{}, A0, A1, ng - 2);
        corr(F, 25, B0, B1, ng - 1);
        if constexpr (f >= 20 && f < 28) { res_init(f - 20); PIN(A0); }
        if (f == 31 && ng == 2) hgate(0, 8);
        if constexpr (f >= 32 && (f & 3) < 3) ld_rec<2, false>(c, a, k, lv, ab4, (f - 32) >> 2, f & 3);
        if constexpr (f >= 56) check_step<f - 64, 22, 2, false>(c, m0, m1, a, k, lv, ab4, tagc);
      });
      stamp<1>(c); stamp<2>(c); stamp<4>(c);
      {
        if (ng == 2) {
          sfor<9>([&](auto F) INL { hside(F, IC<1>{}, B0, B1, 1); });
          hgate(8, 8);
        }
        auto side4 = [&](auto F) INL {
          constexpr int f = F.v;
          hside(F, IC<1>{}, B0, B1, ng - 1);
          if constexpr (f < 32 && (f & 3) < 3) ld_rec<2, false>(c, a, k, lv, ab4, 8 + (f >> 2), f & 3);
          if (f == S2H && ng == 2) hgate(16, 6 - kh);
          if constexpr (f >= S2H && f < S2H + 12) {
            ld_rec<2, false>(c, a, k, lv, ab4, 16 + (f - S2H) / 2, (f - S2H) & 1);
            if constexpr ((f - S2H) & 1) ld_rec<2, false>(c, a, k, lv, ab4, 16 + (f - S2H) / 2, 2);
          }
          if constexpr (f < 72) check_step<f, 22, 2, false>(c, m0, m1, a, k, lv, ab4, tagc);
          if constexpr (f == 76) settle<1, 2, false>(c, rec_bad<false>(a, k, 20, tagc), a, k, lv, ab4, 20, tagc);
          if constexpr (f == 80) { if (kh == 0) settle<1, 2, false>(c, rec_bad<false>(a, k, 21, tagc), a, k, lv, ab4, 21, tagc); }
          if ((f & 7) == 7) stamp<4>(c);
        };
        gemm<88, 2, 1, -1, false, 2, 32, 3, P4CAP, 0, 84>(c, a, A0, A1, kb0, kb1, 0, 0, side4);
        if (kh == 0) gemm<88, 2, 1, -1, false, 2, 32, 3, P4CAP, 84, 88>(c, a, A0, A1, kb0, kb1, 0, 0, NoSide());
        stamp<1>(c); stamp<2>(c);
        res_epilogue(1, layer == 1, (unsigned)(layer * 2 + 2));
      }
      if (layer == 1) break;
      wpre<0>(c, lv, lv);
      stamp<1>(c);
      const int xb1 = WS_X + clb + (4 + rtB) * 16 * REC;
      open_phase<1, true>(c, a, k, lv, xb1, tagc);
      stamp<1>(c);
      gemm<64, 2, 0, -1, true, 3, 34, 0, 99>(c, a, A0, A1, lv, lv, 0, 0, [&](auto F) INL {
        constexpr int f = F.v;
        float var;
        if (ln_side(F, xb1, var)) { rstd = __builtin_amdgcn_rsqf(var); rs_put(); }
        if (f == 53) { cs0 = cs_frag(0); cs1 = cs_frag(1); }
        if (f == 59) { A0 = mfma(mfrag, cs0, A0); A1 = mfma(mfrag, cs1, A1); }
        if (f >= 58 && f < 62) rs_get(f - 58);
      });
    }
  } else if (wave < 6) {
    const int lw = wave - 4;
    const int lv = lane * 16;
    volatile LAS unsigned* full = flg(c, F_FULL + lw);
    if (lw == 0) __builtin_amdgcn_s_setprio(3); else { __builtin_amdgcn_s_setprio(2); *full = 1u; }
    unsigned warm = 0;
    if (CODEPF && lw == 1) {
      const GAS char* cp = (const GAS char*)(pc0 & ~(u64)255) + j * (CODEPF / 32) + lane * 32;
      if (lane * 32 < CODEPF / 32) warm = *(const GAS unsigned*)cp;
    }
    load_slot0(lw);
    const int pts = L_RING + (6 + lw) * SLOT + 4096;
    {
      const unsigned vo3 = (lane >> 2) * 2752 + (lane & 3) * 16;
      const char* w3 = (const char*)(lw ? wu : wg) + (size_t)(cl * 32) * 2752;
#pragma unroll
      for (int i = 0; i < 3; ++i) {
        const int aa = g0 + min(i, ng - 1);
        glds(w3 + aa * 64, vo3, pts + i * 2048);
        glds(w3 + aa * 64 + 16 * 2752, vo3, pts + i * 2048 + 1024);
      }
      if (TOUCH) {
        if (lw == 0) glds((const char*)wd + b * 5504, min(lane, 42) * 128, pts + 6144);
        else glds((const char*)wo + b * 2048, (lane & 15) * 128, pts + 6144);
      }
    }
    if (TOUCH) VMWAIT(7); else VMWAIT(6);
    *full = (unsigned)(2 + lw);
    stamp<3>(c);
    __builtin_amdgcn_s_setprio(1);
    wait_lds<1, 2>(c, F_QGO + lw, 1u);
    load_slot0(4 + lw);
    VMWAIT(16);
#pragma unroll
    for (int i = 0; i < 3; ++i) {
      const int aa = g0 + min(i, ng - 1);
      const v4u g0f = tr_frag(L + pts + i * 2048 + trS), g1f = tr_frag(L + pts + i * 2048 + 1024 + trS);
      st_ws(c, g0f, lv, WS_WT3 + aa * 64 * KB + (cl * 4 + lw) * KB);
      st_ws(c, g1f, lv, WS_WT3 + aa * 64 * KB + (cl * 4 + 2 + lw) * KB);
      const float s = red_h(csum(g1f, csum(g0f, 0.f)));
      if (h == 0) __builtin_amdgcn_raw_buffer_store_b32(f2u(s), c.rs, l31 * 4, WS_CSP + aa * 4 * KB + cl * 256 + lw * 128, 16);
    }
    VMWAIT(0);
    asm volatile("" ::"v"(warm));
    wait_lds<1, 1>(c, F_FULL + lw, (unsigned)(4 + lw));
    *full = 6u;
    stamp<1>(c); stamp<3>(c);
    int seq = 6, pub = 6;
    v4u wc[56];
    const unsigned vo2 = (lane >> 3) * 1024 + (((lane & 7) ^ (((lane >> 4) & 1) << 2)) * 16);
    auto stream = [&](int seg, int i0, int i1) INL {
      const int src = (seg == 0 ? WS_WT1 + m * 96 * KB : WS_WT3 + g0 * 64 * KB) + lw * 8192;
#pragma unroll 1
      for (int i = i0; i < i1; ++i) {
        if (seq >= NSLOT) {
          const unsigned need = (unsigned)(seq - NSLOT + 1);
          bool first = true;
#pragma nounroll
          for (unsigned sp = 0; sp < SPIN_CAP; ++sp) {
            v4u f = *(volatile LAS v4u*)flg(c, F_FREE);
            if ((unsigned)__builtin_amdgcn_readfirstlane(umin(umin(f.x, f.y), umin(f.z, f.w))) >= need) break;
            if (first && pub < seq) { VMWAIT(0); pub = seq; *full = (unsigned)pub; }
            first = false;
            __builtin_amdgcn_s_sleep(1);
          }
          MEMBAR();
        }
        const unsigned dst = L_RING + (seq & (NSLOT - 1)) * SLOT + lw * 8192;
        if (seg == 1) {
          const char* sp = (const char*)wo + (size_t)(i * 2 + lw) * 65536 + cg * 128;
#pragma unroll
          for (int e = 0; e < 8; ++e) glds(sp + e * 8192, vo2, dst + e * 1024);
        } else {
          glds4(ws + src + i * SLOT, lv, dst);
          glds4(ws + src + i * SLOT + 4096, lv, dst + 4096);
        }
        ++seq;
        if (seq - pub > LDEP) {
          if (LDEP == 1) VMWAIT(8); else if (LDEP == 2) VMWAIT(16); else VMWAIT(24);
          pub = seq - LDEP; *full = (unsigned)pub;
        }
      }
    };
    auto drain = [&]() INL { VMWAIT(0); pub = seq; *full = (unsigned)seq; };
    auto flag = [&](int off) INL {
      const v2u tag2 = *(volatile LAS v2u*)flg(c, F_TAG);
      if (lane == 0) __builtin_amdgcn_raw_buffer_store_b64(tag2, c.rs, 0, off, 16);
    };
    *flg(c, F_JOB + 2 + lw) = 1u;
    wait_lds<4, 1>(c, F_JOB, 1u);
    flag(WS_F1 + ((m * 16 + cl) * 4 + lw) * 64);
    stream(1, 0, 4);
    drain();
    stamp<1>(c); stamp<3>(c);
    if (lw == 0) { poll_tags<64>(c, WS_F1 + m * 4096, *(volatile LAS v2u*)flg(c, F_TAG)); *flg(c, F_PTOK) = 1u; }
    else wait_lds<1, 1>(c, F_PTOK, 1u);
    stamp<1>(c); stamp<3>(c);
    stream(2, 0, 4 * ng);
    drain();
    stamp<1>(c);
    wait_lds<4, 2>(c, F_FREE, 12u);
    cache_load(wc, IC<7>{});
    cache_put(wc, IC<7>{}, seq);
    seq += 11; pub = seq;
    stamp<1>(c);
    stream(0, 0, 6);
    stream(1, 0, 4);
    stamp<1>(c);
    stream(2, 0, 4 * ng);
    drain();
    stamp<1>(c);
    cache_put(wc, IC<7>{}, seq);
  } else {
    const int hh = wave - 6;
    const int lv = lane * 16;
    const int pts = L_RING + (6 + hh) * SLOT;
    __builtin_amdgcn_s_setprio(3);
    {
      const unsigned vo1 = (lane >> 2) * 3072 + (lane & 3) * 16;
      const char* j0 = (const char*)wqkv + (size_t)(cl * 32) * 3072 + m * 64;
      glds(j0 + (1 + hh) * 1024, vo1, pts); glds(j0 + (1 + hh) * 1024 + 16 * 3072, vo1, pts + 1024);
      glds(j0 + hh * 16 * 3072, vo1, pts + 2048);
    }
    const int srow = cl * 128 + m * 8 + hh * 4 + (lane >> 4);
    v4u xs[4];
#pragma unroll
    for (int i = 0; i < 4; ++i) xs[i] = *(const GAS v4u*)((const GAS char*)x + (size_t)srow * 1024 + i * 256 + (lane & 15) * 16);
    const u64 e0 = __hip_atomic_load((GAS u64*)(ws + WS_E), RLX_AGENT);
    VMWAIT(0);
    const u64 tag = mix64(e0);
    PIN(xs[0]); PIN(xs[1]); PIN(xs[2]); PIN(xs[3]);
    const v2u tag2 = {(unsigned)tag, (unsigned)(tag >> 32)};
    if (hh == 0) *(volatile LAS v2u*)flg(c, F_TAG) = tag2;
    stamp<1>(c); stamp<3>(c);
    {
      const unsigned tb = ((unsigned)(mix64(tag) >> 32) >> ((lane & 3) * 8)) & 0xffu;
      const v4u f0 = tr_frag(L + pts + (hh ? trS : trN)), f1 = tr_frag(L + pts + 1024 + (hh ? trS : trN)), f2 = tr_frag(L + pts + 2048 + trN);
      const float s0 = red_h(csum(f1, csum(f0, 0.f))), s2 = red_h(csum(f2, 0.f));
      __builtin_amdgcn_raw_buffer_store_b32((f2u(h ? s2 : s0) & 0xffffff00u) | tb, c.rs, l31 * 4 + h * 4096, WS_C1P + m * 8 * KB + cl * 256 + hh * 128, 16);
      float sa = 0.f, sb = 0.f, qa = 0.f, qb = 0.f;
#pragma unroll
      for (int i = 0; i < 4; ++i) {
        sa = dot2(xs[i].y, ONES2, dot2(xs[i].x, ONES2, sa)); sb = dot2(xs[i].w, ONES2, dot2(xs[i].z, ONES2, sb));
        qa = dot2(xs[i].y, xs[i].y, dot2(xs[i].x, xs[i].x, qa)); qb = dot2(xs[i].w, xs[i].w, dot2(xs[i].z, xs[i].z, qb));
      }
      sa += sb; qa += qb;
      sa += dpp<0x128>(sa); qa += dpp<0x128>(qa); sa += dpp<0x124>(sa); qa += dpp<0x124>(qa);
      sa += dpp<0x122>(sa); qa += dpp<0x122>(qa); sa += dpp<0x121>(sa); qa += dpp<0x121>(qa);
      if ((lane & 15) == 0) st_ws(c, v4u{f2u(sa), tag2.x, f2u(qa), tag2.y}, srow * 16, WS_S0P);
      __builtin_amdgcn_s_setprio(2);
      MEMBAR();
      *flg(c, F_JOB + hh) = 1u;
      load_slot0(2 + hh);
      *flg(c, F_QGO + hh) = 1u;
      VMWAIT(0);
      wait_lds<1, 0>(c, F_FULL + hh, (unsigned)(2 + hh));
      *flg(c, F_FULL + hh) = (unsigned)(4 + hh);
      stamp<3>(c);
      {
        float t;
#pragma nounroll
        for (unsigned sp = 0; sp < SPIN_CAP; ++sp) {
          unsigned p[16];
#pragma unroll
          for (int i = 0; i < 16; ++i) p[i] = __builtin_amdgcn_raw_buffer_load_b32(c.rs, lane * 4 + i * 256, WS_C1P + m * 8 * KB + hh * 4096, 16);
          SB();
          unsigned chk = 0;
          t = 0.f;
#pragma unroll
          for (int i = 0; i < 16; ++i) { p[i] ^= tb; chk |= p[i]; t += u2f(p[i]); }
          if (!__any((chk & 0xffu) != 0u)) break;
          MEMBAR();
        }
        if (hh) t = red_h(t);
        if (hh == 0 || h == 0) *(LAS v4u*)(L + L_CS + hh * 1024 + lv) = cs_operand(t);
        MEMBAR();
        *flg(c, F_CS1R + hh) = 1u;
      }
      stamp<3>(c);
      __builtin_amdgcn_s_setprio(1);
      if (PTOUCH) {
        unsigned t = hh ? *(const GAS unsigned*)((const GAS char*)out + (size_t)(cl * 128 + rh * 64) * 1024)
                        : __builtin_amdgcn_raw_buffer_load_b32(c.rs, PTOUCH == 2 ? (lane & 31) * 65536 + 65532 : ACT_CL - 4, WS_ACT + cl * ACT_CL, 0);
        asm volatile("" ::"v"(t));
      }
      st_ws(c, f0, lv, WS_WT1 + m * 96 * KB + (cl * 4 + hh) * KB);
      st_ws(c, f1, lv, WS_WT1 + m * 96 * KB + (cl * 4 + 2 + hh) * KB);
      st_ws(c, f2, lv, WS_WT1 + m * 96 * KB + (64 + cl * 2 + hh) * KB);
    }
    VMWAIT(0);
    if (lane == 0) __builtin_amdgcn_raw_buffer_store_b64(tag2, c.rs, 0, WS_F1 + ((m * 16 + cl) * 4 + 2 + hh) * 64, 16);
    stamp<1>(c); stamp<3>(c);
    v4u wc[32];
    auto agent = [&](int layer) INL {
      const unsigned tg = layer ? tag2.y : tag2.x;
      const v4u T = {tg & 0xffu, (tg >> 8) & 0xffu, (tg >> 16) & 0xffu, tg >> 24};
      const int kvb = WS_KVP + ((layer * 16 + m) * 2 + hh) * 16 * KB, zb = WS_ZP + (layer * 16 + m) * 2 * KB + hh * 64;
      const int zv = (lane >> 2) * 128 + (lane & 3) * 16;
      wait_lds<1, 2>(c, F_KFL, 4u * (unsigned)(layer + 1));
      __builtin_amdgcn_s_setprio(3);
      stamp<1>(c);
      v4u zp;
#pragma nounroll
      for (unsigned sp = 0; sp < SPIN_CAP; ++sp) {
        zp = ld_ws(c, zv, zb) ^ T;
        if (!__any(((zp.x | zp.y | zp.z | zp.w) & 0xffu) != 0u)) break;
        MEMBAR();
      }
      f32x4 s;
#pragma nounroll
      for (unsigned sp = 0; sp < SPIN_CAP; ++sp) {
        v4u p[16];
#pragma unroll
        for (int i = 0; i < 16; ++i) p[i] = ld_fr(c, lv, kvb, i);
        SB();
        if (sp == 0) {
          f32x4 zs = __builtin_bit_cast(f32x4, zp);
#pragma unroll
          for (int i = 0; i < 4; ++i) { float v = zs[i]; v += dpp<0x128>(v); v += dpp<0x124>(v); zs[i] = red_g(v); }
          if (lane < 4) *(LAS v2u*)(L + L_ZF + hh * 32 + lane * 8) = v2u{pack2(zs[0], zs[1]), pack2(zs[2], zs[3])};
        }
        unsigned chk = 0;
#pragma unroll
        for (int i = 0; i < 16; ++i) {
          p[i] ^= T; chk |= (p[i].x | p[i].y) | (p[i].z | p[i].w);
          if (i == 0) s = __builtin_bit_cast(f32x4, p[0]); else s += __builtin_bit_cast(f32x4, p[i]);
          SB();
        }
        if (!__any((chk & 0xffu) != 0u)) break;
        MEMBAR();
      }
      stamp<1>(c);
      *(LAS v2u*)(L + L_KVF + lv + hh * 8) = v2u{pack2(s[0], s[1]), pack2(s[2], s[3])};
      MEMBAR();
      *flg(c, F_KVR + hh) = (unsigned)(layer + 1);
      MEMBAR();
      __builtin_amdgcn_s_setprio(0);
      stamp<1>(c);
    };
    cache_load(wc, IC<4>{});
    agent(0);
    if (hh == 1) {
      wait_lds<1, 2>(c, F_PTOK, 1u);
#pragma unroll 1
      for (int i = 0; i < ng; ++i) {
        float t = 0.f;
#pragma unroll
        for (int q = 0; q < 16; ++q) t += u2f(__builtin_amdgcn_raw_buffer_load_b32(c.rs, lane * 4 + q * 256, WS_CSP + (g0 + i) * 4 * KB, 16));
        *(LAS v4u*)(L + L_CS + (3 + 2 * i) * 512 + lv) = cs_operand(t);
      }
      MEMBAR();
      *flg(c, F_CS3R) = 2u;
      MEMBAR();
    }
    stamp<1>(c);
    __builtin_amdgcn_s_setprio(1);
    cache_put(wc, IC<4>{}, 10 + 4 * ng);
    stamp<1>(c);
    if (b == 0 && wave == 6 && lane == 0) __hip_atomic_store((GAS u64*)(ws + WS_E), tag, RLX_AGENT);
    stamp<1>(c);
    agent(1);
    __builtin_amdgcn_s_setprio(1);
    cache_put(wc, IC<4>{}, 31 + 8 * ng);
  }
}

extern "C" void kernel_launch(void* const* d_in, const int* in_sizes, int n_in, void* d_out, int out_size, void* d_ws, size_t ws_size,
                              hipStream_t stream) {
  fused_fwd<<<256, 512, 0, stream>>>((const u16*)d_in[0], (const u16*)d_in[1], (const u16*)d_in[2], (const u16*)d_in[4], (const u16*)d_in[5],
                                     (const u16*)d_in[6], (u16*)d_out, (char*)d_ws + ((ws_size - WS_END) & ~(size_t)0x1fffff));
}
```

```cpp
#include <hip/hip_runtime.h>
#include <stdint.h>
#include <utility>

#define GAS __attribute__((address_space(1)))
#define LAS __attribute__((address_space(3)))
#define RLX_AGENT __ATOMIC_RELAXED, __HIP_MEMORY_SCOPE_AGENT
typedef unsigned v4u __attribute__((ext_vector_type(4)));
typedef unsigned v2u __attribute__((ext_vector_type(2)));
typedef float f32x2 __attribute__((ext_vector_type(2)));
typedef float f32x4 __attribute__((ext_vector_type(4)));
typedef float f32x16 __attribute__((ext_vector_type(16)));
typedef short s16x8 __attribute__((ext_vector_type(8)));
typedef short s16x4 __attribute__((ext_vector_type(4)));
typedef __bf16 bf16x2 __attribute__((ext_vector_type(2)));
typedef unsigned short u16;
typedef unsigned long long u64;

#define STAMP 0
#define CYC 0
#define LDEP 2
#define XDELAY 12
#define WPACE 2
#define S2H 44
#define BACKOFF 4
#define PTOUCH 0
#define NG2A 18
#define NG2X 20
#define WARM 0
#define ROT 0
#define KVWARM 1
#define KVBACK 4
#define KVDELAY 0
#define QUIET 0
#define REUSE 1
#define WARM0 0
#define QAT 4u
#define CSDELAY 8
#define TOUCH 1
#define S0AT 0
#define XPRE 2
#define XW0 8
#define P4CAP 7
#define CODEPF 59392

constexpr int KB = 1024;
constexpr int WS_E = 0;
constexpr int WS_F1 = 4 * KB;
constexpr int WS_WT1 = 128 * KB;
constexpr int WS_WT3 = WS_WT1 + 16 * 96 * KB;
constexpr int WS_CSP = WS_WT3 + 43 * 64 * KB;
constexpr int WS_S0P = WS_CSP + 43 * 4 * KB;
constexpr int WS_C1P = WS_S0P + 32 * KB;
constexpr int KVR = 2176;
constexpr int WS_KVP = WS_C1P + 16 * 8 * KB;
constexpr int REC = 2560;
constexpr int WS_ACT = 6 * KB * KB, ACT_CL = 2 * KB * KB;
constexpr int WS_O = WS_ACT;
constexpr int WS_X = WS_O + 2 * 4 * 16 * REC;
constexpr int WS_H = WS_X + 3 * 4 * 16 * REC;
static_assert(WS_KVP + 512 * KVR <= WS_ACT && WS_H + (2 * 4 * 43 + 1) * REC <= WS_ACT + ACT_CL, "ws");
constexpr int WS_DBG = WS_ACT + 16 * ACT_CL;
constexpr int WS_END = WS_DBG + 256 * 2 * KB;

constexpr int SLOT = 16384, NSLOT = 8;
constexpr int NSTAMP = 36;
constexpr int L_FLG = 0;
constexpr int L_ZF = L_FLG + 128;
constexpr int L_CS = L_ZF + 64;
constexpr int L_KVF = L_CS + 9 * 512;
constexpr int L_STAMP = L_KVF + 1024;
constexpr int L_KVS = L_STAMP + 1152;
constexpr int L_XCH = 16384;
constexpr int L_RING = 32768;
constexpr int L_END = L_RING + NSLOT * SLOT;
static_assert(L_KVS + 4 * 2304 <= L_XCH && L_END == 163840 && (L_KVS & 15) == 0 && (L_CS & 15) == 0, "lds");
enum { F_FULL = 0, F_TAG = 2, F_FREE = 4, F_JOB = 8, F_KFL = 12, F_QGO = 24, F_CS3R = 15, F_PTOK = 14, F_KVR = 16, F_XF = 20, F_CS1R = 28 };
constexpr unsigned SPIN_CAP = 1u << 17;

template <int V> struct IC { static constexpr int v = V; __device__ constexpr operator int() const { return V; } };
template <class F, int... I> __device__ __forceinline__ void sfor_(F&& fn, std::integer_sequence<int, I...>) { (fn(IC<I>{}), ...); }
template <int N, class F> __device__ __forceinline__ void sfor(F&& fn) { sfor_(fn, std::make_integer_sequence<int, N>{}); }
#define INL __attribute__((always_inline))
#define MEMBAR() asm volatile("" ::: "memory")
#define SB() __builtin_amdgcn_sched_barrier(0)
#define VMWAIT(n) asm volatile("s_waitcnt vmcnt(" #n ")" ::: "memory")
#define PIN(x) asm volatile("" : "+v"(x))

__device__ __forceinline__ unsigned umin(unsigned a, unsigned b) { return a < b ? a : b; }
__device__ __forceinline__ unsigned umax(unsigned a, unsigned b) { return a > b ? a : b; }
__device__ __forceinline__ int gsplit(int i) { return (43 * i) >> 4; }
__device__ __forceinline__ f32x16 mfma(v4u a, v4u b, f32x16 c) {
  return __builtin_amdgcn_mfma_f32_32x32x16_bf16(__builtin_bit_cast(s16x8, a), __builtin_bit_cast(s16x8, b), c, 0, 0, 0);
}
__device__ __forceinline__ f32x16 zero16() { return f32x16{0.f, 0.f, 0.f, 0.f, 0.f, 0.f, 0.f, 0.f, 0.f, 0.f, 0.f, 0.f, 0.f, 0.f, 0.f, 0.f}; }
__device__ __forceinline__ unsigned pack2(float a, float b) {
  bf16x2 v = {(__bf16)a, (__bf16)b};
  return __builtin_bit_cast(unsigned, v);
}
__device__ __forceinline__ float u2f(unsigned u) { return __builtin_bit_cast(float, u); }
__device__ __forceinline__ unsigned f2u(float f) { return __builtin_bit_cast(unsigned, f); }
__device__ __forceinline__ float blo(unsigned u) { return u2f(u << 16); }
__device__ __forceinline__ float bhi(unsigned u) { return u2f(u & 0xffff0000u); }
__device__ __forceinline__ float relu(float x) { return u2f((unsigned)max((int)f2u(x), 0)); }
__device__ __forceinline__ float dot2(unsigned a, unsigned b, float acc) {
  return __builtin_amdgcn_fdot2_f32_bf16(__builtin_bit_cast(bf16x2, a), __builtin_bit_cast(bf16x2, b), acc, false);
}
constexpr unsigned ONES2 = 0x3f803f80u;
constexpr float LOG2E = 1.44269504f;
__device__ __forceinline__ float red_h(float v) {
  auto s = __builtin_amdgcn_permlane32_swap(f2u(v), f2u(v), false, false);
  return u2f((unsigned)s[0]) + u2f((unsigned)s[1]);
}
__device__ __forceinline__ float red_g(float v) {
  auto r = __builtin_amdgcn_permlane16_swap(f2u(v), f2u(v), false, false);
  return red_h(u2f((unsigned)r[0]) + u2f((unsigned)r[1]));
}
template <int CTRL> __device__ __forceinline__ float dpp(float v) { return u2f(__builtin_amdgcn_update_dpp(0, (int)f2u(v), CTRL, 0xf, 0xf, false)); }
__device__ __forceinline__ float exp2c(float x) { return __builtin_amdgcn_fmed3f(__builtin_amdgcn_exp2f(x), 0.f, 1.f); }
__device__ __forceinline__ void split2(float v, unsigned& hi, unsigned& lo) {
  hi = pack2(v, 0.f) & 0xffffu;
  lo = pack2(v - blo(hi), 0.f) & 0xffffu;
}
template <int D = 256> __device__ __forceinline__ v4u tr_frag(LAS char* p) {
  v2u lo = __builtin_bit_cast(v2u, __builtin_amdgcn_ds_read_tr16_b64_v4i16((LAS s16x4*)p));
  v2u hi = __builtin_bit_cast(v2u, __builtin_amdgcn_ds_read_tr16_b64_v4i16((LAS s16x4*)(p + D)));
  return v4u{lo.x, lo.y, hi.x, hi.y};
}
__device__ __forceinline__ void glds(const void* sbase, unsigned voff, unsigned ldst) {
  asm volatile("s_mov_b32 m0, %2\n\ts_nop 0\n\tglobal_load_lds_dwordx4 %0, %1" ::"v"(voff), "s"(sbase), "s"(ldst) : "memory");
}
__device__ __forceinline__ void glds4(const void* sbase, unsigned voff, unsigned ldst) {
  asm volatile(
      "s_mov_b32 m0, %2\n\ts_nop 0\n\tglobal_load_lds_dwordx4 %0, %1 sc1\n\tglobal_load_lds_dwordx4 %0, %1 offset:1024 sc1\n\t"
      "global_load_lds_dwordx4 %0, %1 offset:2048 sc1\n\tglobal_load_lds_dwordx4 %0, %1 offset:3072 sc1" ::"v"(voff), "s"(sbase), "s"(ldst)
      : "memory");
}

struct Ctx {
  LAS char* L;
  __amdgpu_buffer_rsrc_t rs;
  int lane, wave;
  int seq, known;
  int vb0, vb1, nb0, nb1;
  int rot;
  int fa;
  v2u fpre;
  v4u w0, w1, w2, w3;
  int nst;
  template <int I> __device__ __forceinline__ v4u& wf() { if constexpr (I == 0) return w0; else if constexpr (I == 1) return w1; else if constexpr (I == 2) return w2; else return w3; }
};
__device__ __forceinline__ volatile LAS unsigned* flg(Ctx& c, int i) { return (volatile LAS unsigned*)(c.L + L_FLG) + i; }
__device__ __forceinline__ void stamp0(Ctx& c) {
}
template <int K> __device__ __forceinline__ void stamp(Ctx& c) { if (STAMP == K) stamp0(c); }
template <int N, int SLP> __device__ __forceinline__ void wait_lds(Ctx& c, int i, unsigned val) {
#pragma nounroll
  for (unsigned sp = 0; sp < SPIN_CAP; ++sp) {
    unsigned v;
    if (N == 1) v = *flg(c, i);
    else if (N == 2) { v2u f = *(volatile LAS v2u*)flg(c, i); v = umin(f.x, f.y); }
    else { v4u f = *(volatile LAS v4u*)flg(c, i); v = umin(umin(f.x, f.y), umin(f.z, f.w)); }
    if ((unsigned)__builtin_amdgcn_readfirstlane(v) >= val) break;
    if (SLP) __builtin_amdgcn_s_sleep(SLP);
  }
  MEMBAR();
}
template <int N> __device__ __forceinline__ void poll_tags(Ctx& c, int base, v2u tag) {
#pragma nounroll
  for (unsigned sp = 0; sp < SPIN_CAP; ++sp) {
    const v2u v = __builtin_amdgcn_raw_buffer_load_b64(c.rs, (c.lane & (N - 1)) * 64, base, 16);
    if (__all(v.x == tag.x && v.y == tag.y)) break;
    __builtin_amdgcn_s_sleep(4);
    MEMBAR();
  }
  MEMBAR();
}
__device__ __forceinline__ v4u ld_ws(Ctx& c, int voff, int soff) { return __builtin_amdgcn_raw_buffer_load_b128(c.rs, voff, soff, 16); }
__device__ __forceinline__ void st_ws(Ctx& c, v4u v, int voff, int soff) {
  __builtin_amdgcn_raw_buffer_store_b128(v, c.rs, voff, soff, 16);
  asm volatile("s_nop 1" ::"v"(v) : "memory");
}
__device__ __forceinline__ int fresh_lane() { int l; asm volatile("v_mbcnt_lo_u32_b32 %0, -1, 0\n\tv_mbcnt_hi_u32_b32 %0, -1, %0" : "=v"(l)); __builtin_assume(l >= 0 && l < 64); return l; }
__device__ __forceinline__ int opq(int v) { asm volatile("" : "+s"(v)); return v; }
__device__ __forceinline__ int opqv(int v) { asm volatile("" : "+v"(v)); return v; }
__device__ __forceinline__ v4u ld_fr(Ctx& c, int lv, int sb, int i) { return ld_ws(c, lv + (i & 3) * KB, sb + (i >> 2) * 4 * KB); }

constexpr int fps(int mode) { return (mode == 1 || mode == 4) ? 8 : 16; }
constexpr bool two(int mode) { return mode == 1 || mode == 2 || mode == 4; }
__device__ __forceinline__ void slot_wait(Ctx& c, int sq) {
  if (__builtin_expect(c.known <= sq, 0)) {
#pragma nounroll
    for (unsigned sp = 0; sp < SPIN_CAP; ++sp) {
      v2u f = *(volatile LAS v2u*)flg(c, F_FULL);
      c.known = __builtin_amdgcn_readfirstlane(umin(f.x, f.y));
      if (c.known > sq) break;
    }
  }
  MEMBAR();
}
template <int MODE> __device__ __forceinline__ void slot_base(Ctx& c, int sq, int lb0, int lb1) {
  const int sb = (sq & (NSLOT - 1)) * SLOT;
  c.nb0 = lb0 + sb; PIN(c.nb0);
  if (two(MODE)) { c.nb1 = lb1 + sb; PIN(c.nb1); }
}
template <int MODE, int F> __device__ __forceinline__ v4u wread(Ctx& c) {
  constexpr int i = F % fps(MODE);
  if constexpr (i == 0) { c.vb0 = c.nb0; if (two(MODE)) c.vb1 = c.nb1; }
  LAS char* R = c.L + L_RING;
  if (MODE == 0) return *(LAS v4u*)(R + c.vb0 + i * KB);
  if (MODE == 1) return tr_frag<512>(R + ((i & 1) ? c.vb1 : c.vb0) + ((i >> 1) & 1) * 2 * KB + (i >> 2) * 8 * KB);
  if (MODE == 4) return *(LAS v4u*)(R + ((i & 1) ? c.vb1 : c.vb0) + ((i >> 1) & 1) * 2 * KB + (i >> 2) * 8 * KB);
  if (MODE == 2) return tr_frag(R + ((i & 1) ? c.vb1 : c.vb0) + i * KB);
  return tr_frag(R + c.vb0 + i * KB);
}
__device__ __forceinline__ void ring_publish(Ctx& c) { MEMBAR(); *(volatile LAS unsigned*)(c.L + c.fa) = (unsigned)c.seq; MEMBAR(); }
template <int MODE> __device__ __forceinline__ void wpre(Ctx& c, int lb0, int lb1) {
  slot_wait(c, c.seq);
  slot_base<MODE>(c, c.seq, lb0, lb1);
  sfor<4>([&](auto F) INL { c.wf<F.v>() = wread<MODE, F.v>(c); });
  if (fps(MODE) == 8) c.fpre = *(volatile LAS v2u*)flg(c, F_FULL);
}
struct NoSide { template <class T> __device__ __forceinline__ void operator()(T) const {} };
template <int NF, int TPS, int MODE, int NEXT, bool XA, int ZERO, int RELF, int EARLY, int CAP, int F0 = 0, int F1 = NF, class SIDE>
__device__ __forceinline__ void gemm(Ctx& c, v4u (&a)[32], f32x16& c0, f32x16& c1, int lb0, int lb1, int nb0, int nb1, SIDE side) {
  constexpr int FPS = fps(MODE);
  static_assert(NF % FPS == 0, "whole slots");
  sfor<F1 - F0>([&](auto FI) INL {
    constexpr IC<F0 + FI.v> F{};
    constexpr int f = F.v, s = f / TPS, t = f % TPS, fn = f + 4, i = fn % FPS;
    const v4u x = a[s & 31], wv = c.wf<f % 4>();
    f32x16& acc = t ? c1 : c0;
    const f32x16 cin = (s == 0 && ((ZERO >> t) & 1)) ? zero16() : acc;
    acc = XA ? mfma(x, wv, cin) : mfma(wv, x, cin);
    SB();
    if constexpr (fn < NF) {
      c.wf<f % 4>() = wread<MODE, fn>(c);
      constexpr bool lastslot = fn / FPS == NF / FPS - 1;
      if constexpr (!lastslot || NEXT >= 0) {
        if constexpr (i == FPS - 7) c.fpre = *(volatile LAS v2u*)flg(c, F_FULL);
        if constexpr (i == FPS - 4) { c.fpre.x = umin(c.fpre.x, c.fpre.y); PIN(c.fpre); }
        if constexpr (i == FPS - 3) { c.known = __builtin_amdgcn_readfirstlane(c.fpre.x); asm volatile("" : "+s"(c.known)); }
        if constexpr (i == FPS - 2) { if constexpr (lastslot) slot_base<(NEXT < 0 ? 0 : NEXT)>(c, c.seq + 1, nb0, nb1); else slot_base<MODE>(c, c.seq + 1, lb0, lb1); }
        if constexpr (i == FPS - 1) slot_wait(c, c.seq + 1);
      }
      if constexpr (i == FPS - 1) c.seq++;
    } else if constexpr (NEXT >= 0) {
      c.wf<f % 4>() = wread<(NEXT < 0 ? 0 : NEXT), fn - NF>(c);
      if constexpr (fps(NEXT) == 8 && fn - NF == 1) c.fpre = *(volatile LAS v2u*)flg(c, F_FULL);
    }
    if constexpr (fn <= NF && i == 0) { if constexpr ((f >= RELF || fn / FPS - 1 < EARLY) && fn / FPS - 1 < CAP) ring_publish(c); }
    else if constexpr (f == RELF && RELF > 0) ring_publish(c);
    side(F);
    SB();
  });
}
__device__ __forceinline__ unsigned sum4(unsigned m, v4u v) { return ((m + v.x + v.y) + v.z + v.w); }
template <bool EX> __device__ __forceinline__ void ld_chk(Ctx& c, v2u& k, int sb) {
  if (EX) k = __builtin_amdgcn_raw_buffer_load_b64(c.rs, c.lane * 8 + 2 * KB, sb, 16);
  else k.x = __builtin_amdgcn_raw_buffer_load_b32(c.rs, c.lane * 4 + 2 * KB, sb, 16);
}
template <int STR, bool EX> __device__ __forceinline__ void ld_rec(Ctx& c, v4u (&a)[32], v2u (&k)[8], int lv, int sb, int p, int e) {
  const int q = STR * p;
  const int so = sb + (q < 32 && ROT ? (c.rot + ((q & 15) >> 1) >= 8 ? c.rot * 2 * REC - 16 * REC : c.rot * 2 * REC) : 0) + q * REC;
  if (e < 2) a[(2 * p + e) & 31] = ld_ws(c, lv + e * KB, so);
  else ld_chk<EX>(c, k[p & 7], so);
}
template <bool EX> __device__ __forceinline__ unsigned rec_bad(v4u (&a)[32], v2u (&k)[8], int p, unsigned tag) {
  return sum4(sum4(EX ? k[p & 7].x + k[p & 7].y : k[p & 7].x, a[(2 * p) & 31]), a[(2 * p + 1) & 31]) ^ tag;
}
template <int N, int STR, bool EX> __device__ __forceinline__ void settle(Ctx& c, unsigned bad, v4u (&a)[32], v2u (&k)[8], int lv, int sb, int p0, unsigned tag) {
  if (__builtin_expect(__any(bad != 0u), 0)) {
    unsigned sp = 0;
#pragma nounroll
    do {
      if (BACKOFF) __builtin_amdgcn_s_sleep(BACKOFF);
      MEMBAR();
      bad = 0;
#pragma unroll
      for (int i = 0; i < 3 * N; ++i) ld_rec<STR, EX>(c, a, k, lv, sb, p0 + i / 3, i % 3);
#pragma unroll
      for (int i = 0; i < N; ++i) bad |= rec_bad<EX>(a, k, p0 + i, tag);
    } while (__any(bad != 0u) && ++sp < SPIN_CAP);
  }
}
template <int F, int NP, int STR, bool EX>
__device__ __forceinline__ void check_step(Ctx& c, unsigned& m0, unsigned& m1, v4u (&a)[32], v2u (&k)[8], int lv, int sb, unsigned tag) {
  constexpr int p = 2 * ((F + 8) / 8), r = (F + 8) % 8;
  if constexpr (p < NP) {
    if constexpr (r == 2) { m0 = sum4(EX ? k[p & 7].x + k[p & 7].y : k[p & 7].x, a[(2 * p) & 31]); PIN(m0); }
    if constexpr (r == 3) { m0 = sum4(m0, a[(2 * p + 1) & 31]); PIN(m0); }
    if constexpr (r == 4) { m1 = sum4(EX ? k[(p + 1) & 7].x + k[(p + 1) & 7].y : k[(p + 1) & 7].x, a[(2 * p + 2) & 31]); PIN(m1); }
    if constexpr (r == 5) { m1 = sum4(m1, a[(2 * p + 3) & 31]); PIN(m1); }
    if constexpr (r == 6) { m0 = (m0 ^ tag) | (m1 ^ tag); PIN(m0); }
    if constexpr (r == 7) settle<2, STR, EX>(c, m0, a, k, lv, sb, p, tag);
  }
}
template <int STR, bool EX> __device__ __forceinline__ void open_phase(Ctx& c, v4u (&a)[32], v2u (&k)[8], int lv, int sb, unsigned tag) {
  VMWAIT(0);
  if (XDELAY) __builtin_amdgcn_s_sleep(XDELAY);
#pragma unroll
  for (int i = 0; i < 12; ++i) ld_rec<STR, EX>(c, a, k, lv, sb, i / 3, i % 3);
  SB();
  settle<2, STR, EX>(c, rec_bad<EX>(a, k, 0, tag) | rec_bad<EX>(a, k, 1, tag), a, k, lv, sb, 0, tag);
}
template <int F, int NP, int STR, bool EX> __device__ __forceinline__ void ramp_step(Ctx& c, v4u (&a)[32], v2u (&k)[8], int lv, int sb) {
  if constexpr (F < 12) {
    constexpr int p = 4 + F / 2;
    if constexpr (p < NP) { ld_rec<STR, EX>(c, a, k, lv, sb, p, F & 1); if constexpr (F & 1) ld_rec<STR, EX>(c, a, k, lv, sb, p, 2); }
  } else {
    constexpr int p = 10 + (F - 12) / 4, e = (F - 12) % 4;
    if constexpr (p < NP && e < 3) ld_rec<STR, EX>(c, a, k, lv, sb, p, e);
  }
}
template <bool EX> __device__ __forceinline__ void st_chk(Ctx& c, unsigned sum, unsigned ex, int sb, unsigned tag) {
  if (EX) __builtin_amdgcn_raw_buffer_store_b64(v2u{tag - sum - ex, ex}, c.rs, c.lane * 8 + 2 * KB, sb, 16);
  else __builtin_amdgcn_raw_buffer_store_b32(tag - sum, c.rs, c.lane * 4 + 2 * KB, sb, 16);
}
__device__ __forceinline__ u64 mix64(u64 z) {
  z += 0x9E3779B97F4A7C15ull;
  z = (z ^ (z >> 30)) * 0xBF58476D1CE4E5B9ull;
  z = (z ^ (z >> 27)) * 0x94D049BB133111EBull;
  return z ^ (z >> 31);
}
__device__ __forceinline__ float csum(v4u f, float s) { return dot2(f.w, ONES2, dot2(f.z, ONES2, dot2(f.y, ONES2, dot2(f.x, ONES2, s)))); }
__device__ __forceinline__ v4u cs_operand(float s) { unsigned hi, lo; split2(s, hi, lo); return v4u{hi | (hi << 16), lo, 0u, 0u}; }

__global__ __launch_bounds__(512, 2) void fused_fwd(const u16* __restrict__ x, const u16* __restrict__ wqkv, const u16* __restrict__ wo,
                                                    const u16* __restrict__ wg, const u16* __restrict__ wu, const u16* __restrict__ wd,
                                                    u16* __restrict__ out, char* __restrict__ ws) {
  __shared__ __attribute__((aligned(16))) char smem[L_END];
  u64 pc0;
  asm volatile("s_getpc_b64 %0" : "=s"(pc0));
  Ctx c;
  c.L = (LAS char*)smem;
  c.rs = __builtin_amdgcn_make_buffer_rsrc((void*)ws, 0, WS_END, 0x00020000);
  const int wave = __builtin_amdgcn_readfirstlane(threadIdx.x >> 6);
  const int b = blockIdx.x, xc = b & 7, j = b >> 3;
  const int cl = (xc >> 1) * 4 + (j >> 3), m = (xc & 1) * 8 + (j & 7);
  const int rh = m >> 3, cg = m & 7;
  const int g0 = gsplit(m), ng = gsplit(m + 1) - g0;
  c.lane = 0; c.wave = wave; c.seq = 0; c.known = 0; c.nst = 0; c.vb0 = 0; c.vb1 = 0; c.nb0 = 0; c.nb1 = 0; c.fa = 0; c.fpre = v2u{0u, 0u};
  const int rot = (2 * cl + m) & 7;
  c.rot = ROT ? rot : 0;
  LAS char* L = c.L;
  if (threadIdx.x < 32) *flg(c, threadIdx.x) = 0u;
  stamp0(c);
  asm volatile("s_waitcnt lgkmcnt(0)\n\ts_barrier" ::: "memory");
#define LANEVARS                                                                                                              \
  const int lane = fresh_lane(), h = lane >> 5, l31 = lane & 31, lv = lane * 16;                                               \
  const int trN = (8 * h + ((lane >> 2) & 3)) * 64 + ((lane >> 4) & 1) * 32 + (lane & 3) * 8;                                   \
  const int trS = (8 * h + ((lane >> 2) & 3)) * 64 + ((lane >> 4) & 1) * 32 + (((lane & 1) << 1) | ((lane >> 1) & 1)) * 8;       \
  c.lane = lane; (void)trN; (void)trS; (void)l31;
  const int pp = wave & 1;
  auto load_half0 = [&](int sl) INL {
    const int lane = fresh_lane();
    const unsigned vo1 = (lane >> 2) * 3072 + (lane & 3) * 16;
    const char* wb = (const char*)wqkv + m * 64 + (sl < 4 ? (1 + pp) * 1024 : 0);
#pragma unroll
    for (int p = 0; p < 8; ++p) {
      const int s = sl < 4 ? sl * 8 + p : (sl - 4) * 16 + 2 * p + pp;
      glds(wb + (size_t)(((s + 4 * rot) & 31) * 16) * 3072, vo1, L_RING + sl * SLOT + p * 2048 + pp * 1024);
    }
  };
  auto cache_load = [&](auto& wc, auto NK, auto K0, auto K1) INL {
    const int lane = fresh_lane();
    const __amdgpu_buffer_rsrc_t rd = __builtin_amdgcn_make_buffer_rsrc((void*)wd, 0, 1376 * 1024, 0x00020000);
    const int vo2 = (lane >> 3) * 1024 + (((lane & 7) ^ (((lane >> 4) & 1) << 2)) * 16);
#pragma unroll
    for (int k = K0.v; k < K1.v; ++k) {
#pragma unroll
      for (int q = 0; q < 2; ++q) {
        const int hs = (NK.v == 5 ? k : k + 5) * 2 + pp;
        const int p = min((hs < 16 ? (hs & 8) | ((hs + c.rot) & 7) : hs) * 2 + q, 42);
        const int m3 = min(p - 32 + 1 + ((p - 32) >> 1), 15);
        const int aa = p < 32 ? gsplit(p & 15) + (p >> 4) : gsplit(m3) + 2;
#pragma unroll
        for (int e = 0; e < 4; ++e) wc[k * 8 + q * 4 + e] = __builtin_amdgcn_raw_buffer_load_b128(rd, vo2, aa * 32768 + cg * 128 + e * 8192, 0);
        SB();
        if (WPACE && NK.v == 5) __builtin_amdgcn_s_sleep(WPACE);
      }
    }
  };
  auto cache_put = [&](auto& wc, auto NK, auto FIRST, int base) INL {
    const int lane = fresh_lane(), h = lane >> 5;
    const int tk = (8 * h + ((lane >> 2) & 3)) * 128 + ((lane >> 4) & 1) * 32 + (((lane & 1) << 1) | ((lane >> 1) & 1)) * 8 + ((lane >> 3) & 1) * 64;
#pragma unroll
    for (int k = 0; k < NK.v; ++k) {
      const int sq = base + (NK.v == 5 ? k : k + 5);
      wait_lds<4, 1>(c, F_FREE, (unsigned)(sq - NSLOT + 1));
      LAS char* d = L + L_RING + (sq & (NSLOT - 1)) * SLOT + pp * 8192;
      if (FIRST.v && k == 0) {
#pragma unroll
        for (int kk = 0; kk < NK.v; ++kk) {
#pragma unroll
          for (int e = 0; e < 8; ++e) *(LAS v4u*)(d + lane * 16 + e * KB) = wc[kk * 8 + e];
          MEMBAR();
          SB();
#pragma unroll
          for (int e = 0; e < 8; ++e) wc[kk * 8 + e] = tr_frag<512>(d + (e >> 1) * 2048 + (tk ^ ((e & 1) * 64)));
          MEMBAR();
          SB();
        }
      }
#pragma unroll
      for (int e = 0; e < 8; ++e) *(LAS v4u*)(d + lane * 16 + e * KB) = wc[k * 8 + e];
      wait_lds<1, 1>(c, F_FULL + pp, (unsigned)sq);
      *flg(c, F_FULL + pp) = (unsigned)(sq + 1);
      MEMBAR();
    }
  };
  auto hpos = [&](int gi) INL { return gi * 16 + (gi < 2 ? m : g0 - 2 * m); };

  if (wave < 4) {
    __builtin_amdgcn_s_setprio(2);
    const int w = wave, mh = w >> 1, kh = w & 1;
    c.fa = opqv(L_FLG + (F_FREE + wave) * 4);
    v4u a[32], xr0, xr1;
    v2u k[8];
    f32x16 A0, A1, B0, B1;
    LANEVARS
    float rstd;
    unsigned tagx, tagy, tagc;
    v4u mfrag;
    auto set_stats = [&](float sum, float ssq) INL -> float {
      const float s = sum * (1.f / 512), q = ssq * (1.f / 512);
      unsigned hi, lo;
      split2(-s, hi, lo);
      mfrag = h == 0 ? v4u{hi | (lo << 16), hi, 0u, 0u} : v4u{0u, 0u, 0u, 0u};
      return fmaxf(q - s * s, 0.f) + 1e-5f;
    };
    auto cs_frag = [&](int tile) INL { return *(LAS v4u*)(L + L_CS + tile * 512 + l31 * 16); };
    auto rs_put = [&]() INL { const int lane = fresh_lane(); *(LAS float*)(L + L_KVS + w * 2304 + lane * 4) = h ? rstd * LOG2E : rstd; MEMBAR(); };
    auto rs_get = [&](int q) INL {
      const f32x4 r = *(LAS f32x4*)(L + L_KVS + w * 2304 + (8 * q + 4 * h) * 4), rl = *(LAS f32x4*)(L + L_KVS + w * 2304 + 128 + (8 * q + 4 * h) * 4);
#pragma unroll
      for (int i = 0; i < 4; ++i) { B0[4 * q + i] = r[i]; B1[4 * q + i] = rl[i]; }
    };
    {
      const __amdgpu_buffer_rsrc_t rx = __builtin_amdgcn_make_buffer_rsrc((void*)x, 0, 2048 * 1024, 0x00020000);
      const int xv = (lane >> 3) * 1024 + (lane & 7) * 16, xs = (cl * 128 + w * 32) * 1024;
      auto ldx = [&](int i) INL { a[i] = __builtin_amdgcn_raw_buffer_load_b128(rx, xv, xs + (i & 3) * 8192 + (((i >> 2) + rot) & 7) * 128, 0); };
      __builtin_amdgcn_s_setprio(3);
#pragma unroll
      for (int i = 0; i < XW0; ++i) ldx(i);
      __builtin_amdgcn_s_setprio(2);
#pragma unroll
      for (int i = XW0; i < 16; ++i) ldx(i);
      SB();
      stamp<3>(c);
      LAS char* sc = L + L_XCH + w * 4096;
      const int r8 = lane >> 3;
      const int wo_ = r8 * 128 + (((lane & 7) ^ (r8 >> 1)) * 16);
      const int ro = l31 * 128 + ((h ^ ((l31 >> 1) & 7)) * 16);
      auto xstep = [&](int kp, int e) INL {
        MEMBAR();
        if (e < 4) *(LAS v4u*)(sc + ((wo_ + e * 1024) ^ ((e & 1) * 64))) = a[kp * 4 + e];
        else a[kp * 4 + e - 4] = *(LAS v4u*)(sc + (ro ^ ((e - 4) * 32)));
        MEMBAR();
      };
#pragma unroll
      for (int kp = 0; kp < XPRE; ++kp) {
#pragma unroll
        for (int e = 0; e < 8; ++e) xstep(kp, e);
        if (kp < 2) {
#pragma unroll
          for (int i = 16 + 8 * kp; i < 24 + 8 * kp; ++i) ldx(i);
        }
      }
      stamp<3>(c);
      wpre<2>(c, trN, trS);
      stamp<3>(c);
      v4u s0;
      const int s0b = WS_S0P + (cl * 128 + w * 32) * 16;
      gemm<64, 2, 2, -1, true, 3, 0, 0, 99>(c, a, A0, A1, trN, trS, 0, 0, [&](auto F) INL {
        constexpr int f = F.v;
        if ((f >> 3) + XPRE < 8) xstep((f >> 3) + XPRE, f & 7);
        if (f == S0AT) s0 = ld_ws(c, l31 * 16, s0b);
        if (f == 52) {
          wait_lds<2, 0>(c, F_CS1R, 1u);
          const v2u tg = *(volatile LAS v2u*)flg(c, F_TAG);
          tagx = __builtin_amdgcn_readfirstlane(tg.x); tagy = __builtin_amdgcn_readfirstlane(tg.y); tagc = (tagx * 0x9E3779B1u + tagy) | 1u;
          if (__any(s0.y != tagx || s0.w != tagy)) {
            unsigned sp = 0;
#pragma nounroll
            do { MEMBAR(); s0 = ld_ws(c, l31 * 16, s0b); } while (__any(s0.y != tagx || s0.w != tagy) && ++sp < SPIN_CAP);
          }
          rstd = __builtin_amdgcn_rsqf(set_stats(u2f(s0.x), u2f(s0.z)));
          rs_put();
          xr0 = cs_frag(0); xr1 = cs_frag(1);
        }
        if (f == 57) { A0 = mfma(mfrag, xr0, A0); A1 = mfma(mfrag, xr1, A1); }
        if (f >= 58 && f < 62) rs_get(f - 58);
        if ((f & 15) == 15) stamp<3>(c);
      });
      const int xq = l31 * 1024 + h * 16, xqs = (cl * 128 + rh * 64 + mh * 32) * 1024 + cg * 128 + kh * 64;
      xr0 = __builtin_amdgcn_raw_buffer_load_b128(rx, xq, xqs, 0);
      xr1 = __builtin_amdgcn_raw_buffer_load_b128(rx, xq + 32, xqs, 0);
    }
#pragma unroll 1
    for (int layer = 0; layer < 2; ++layer) {
      const int lb = REUSE ? 0 : layer;
      stamp<1>(c);
      {
#pragma unroll
        for (int i = 0; i < 16; ++i) B1[i] *= A0[i];
        SB();
#pragma unroll
        for (int i = 0; i < 16; ++i) { B1[i] = exp2c(B1[i]); A0[i] = fmaf(A0[i], B0[i], 1.f); A1[i] *= B0[i]; }
        SB();
        v4u pk0, pk1, vv0, vv1;
#pragma unroll
        for (int i = 0; i < 16; ++i) A0[i] = fmaxf(A0[i], B1[i]) + 1e-6f;
#pragma unroll
        for (int i = 0; i < 4; ++i) {
          pk0[i] = pack2(A0[2 * i], A0[2 * i + 1]); vv0[i] = pack2(A1[2 * i], A1[2 * i + 1]);
          pk1[i] = pack2(A0[8 + 2 * i], A0[9 + 2 * i]); vv1[i] = pack2(A1[8 + 2 * i], A1[9 + 2 * i]);
        }
        f32x16 kv = mfma(pk0, vv0, zero16());
        kv = mfma(pk1, vv1, kv);
        const float z = red_h(csum(pk0, 0.f) + csum(pk1, 0.f));
        LAS char* ks = L + L_KVS + w * 2304;
        if (h == 0) *(LAS float*)(ks + 2048 + l31 * 4) = z;
        if (lane & 16) {
          *(LAS f32x4*)(ks + lv) = f32x4{kv[8], kv[9], kv[10], kv[11]};
          *(LAS f32x4*)(ks + 1024 + lv) = f32x4{kv[12], kv[13], kv[14], kv[15]};
          MEMBAR();
        } else {
          MEMBAR();
          *(LAS f32x4*)(ks + lv) = f32x4{kv[0], kv[1], kv[2], kv[3]};
          *(LAS f32x4*)(ks + 1024 + lv) = f32x4{kv[4], kv[5], kv[6], kv[7]};
        }
        MEMBAR();
        unsigned old = 0;
        if (lane == 0) old = __hip_atomic_fetch_add((LAS unsigned*)flg(c, F_KFL), 1u, __ATOMIC_RELAXED, __HIP_MEMORY_SCOPE_WORKGROUP);
        if ((__builtin_amdgcn_readfirstlane(old) & 3u) == 3u) {
          const int lane = fresh_lane();
          MEMBAR();
          f32x4 k0 = {0.f, 0.f, 0.f, 0.f}, k1 = k0;
          f32x2 zz = {0.f, 0.f};
#pragma unroll
          for (int ww = 0; ww < 4; ++ww) {
            k0 += *(LAS f32x4*)(L + L_KVS + ww * 2304 + lv);
            k1 += *(LAS f32x4*)(L + L_KVS + ww * 2304 + 1024 + lv);
            zz += *(LAS f32x2*)(L + L_KVS + ww * 2304 + 2048 + (lane & 15) * 8);
          }
          const unsigned tg = layer ? tagy : tagx;
          const unsigned tl = tg >> ((lane & 1) * 16), t0 = tl & 0xffu, t1 = (tl >> 8) & 0xffu;
          const int kvb = WS_KVP + ((layer * 16 + m) * 16 + cl) * KVR;
          auto st2 = [&](float u, float v, int vo) INL {
            __builtin_amdgcn_raw_buffer_store_b64(v2u{(f2u(u) & 0xffffff00u) | t0, (f2u(v) & 0xffffff00u) | t1}, c.rs, vo, kvb, 16);
          };
          st2(k0[0], k0[1], lane * 8); st2(k0[2], k0[3], lane * 8 + 512); st2(k1[0], k1[1], lane * 8 + 1024); st2(k1[2], k1[3], lane * 8 + 1536);
          if (lane < 16) st2(zz[0], zz[1], lane * 8 + 2048);
        }
        MEMBAR();
      }
      stamp<1>(c);
      B0 = mfma(cs_frag(2), mfrag, zero16());
      if (layer == 0) { wpre<3>(c, trN, trN); gemm<32, 1, 3, -1, false, 0, 0, 0, QUIET ? 0 : 99>(c, a, B0, B0, trN, trN, 0, 0, NoSide()); }
      else { wpre<0>(c, lv, lv); gemm<32, 1, 0, -1, false, 0, 0, 0, QUIET ? 0 : 99>(c, a, B0, B0, lv, lv, 0, 0, NoSide()); }
      const int lq = fresh_lane();
      const int kb0 = (8 * (lq >> 5) + ((lq >> 2) & 3)) * 128 + ((lq >> 4) & 1) * 32 + (((lq & 1) << 1) | ((lq >> 1) & 1)) * 8 + kh * 4096 + ((kh ^ (lq >> 3)) & 1) * 64;
      const int kb1 = kb0 ^ 64;
      auto res_init = [&](int i) INL { const unsigned u = i < 4 ? xr0[i & 3] : xr1[i & 3]; A0[2 * i] = blo(u); A0[2 * i + 1] = bhi(u); };
      {
        v4u pq0, pq1;
        const float rl = rstd * LOG2E;
#pragma unroll
        for (int i = 0; i < 16; ++i) B1[i] = exp2c(B0[i] * rl);
#pragma unroll
        for (int i = 0; i < 16; ++i) B0[i] = fmaxf(fmaf(B0[i], rstd, 1.f), B1[i]) + 1e-6f;
#pragma unroll
        for (int i = 0; i < 4; ++i) { pq0[i] = pack2(B0[2 * i], B0[2 * i + 1]); pq1[i] = pack2(B0[8 + 2 * i], B0[9 + 2 * i]); }
#pragma unroll
        for (int i = 0; i < 8; ++i) res_init(i);
        PIN(pq0); PIN(pq1); PIN(A0);
        const int ob = opq(WS_O + cl * ACT_CL + ((lb * 4 + w) * 16 + m) * REC);
        stamp<1>(c);
        {
          const int lane = fresh_lane();
          const unsigned tg = layer ? tagy : tagx;
          const unsigned tl = tg >> ((lane & 1) * 16), t0 = tl & 0xffu, t1 = (tl >> 8) & 0xffu;
          const int kvb = WS_KVP + (layer * 16 + m) * 16 * KVR;
          float s0 = 0.f, s1 = 0.f;
          v2u zp;
          if (KVDELAY) __builtin_amdgcn_s_sleep(KVDELAY);
#pragma nounroll
          for (unsigned sp = 0; sp < SPIN_CAP; ++sp) {
            v2u p[16];
            zp = __builtin_amdgcn_raw_buffer_load_b64(c.rs, (lane >> 2) * KVR + (lane & 3) * 8 + 2048, kvb + w * 32, 16);
#pragma unroll
            for (int i = 0; i < 16; ++i) p[i] = __builtin_amdgcn_raw_buffer_load_b64(c.rs, lane * 8, kvb + w * 512 + i * KVR, 16);
            SB();
            zp.x ^= t0; zp.y ^= t1;
            unsigned chk = zp.x | zp.y;
#pragma unroll
            for (int i = 0; i < 16; ++i) {
              p[i].x ^= t0; p[i].y ^= t1; chk |= p[i].x | p[i].y;
              s0 = i ? s0 + u2f(p[i].x) : u2f(p[i].x); s1 = i ? s1 + u2f(p[i].y) : u2f(p[i].y);
              PIN(s0); PIN(s1);
            }
            if (!__any((chk & 0xffu) != 0u)) break;
            if (KVBACK) __builtin_amdgcn_s_sleep(KVBACK);
            MEMBAR();
          }
          *(LAS unsigned*)(L + L_KVF + lv + w * 4) = pack2(s0, s1);
          float za = u2f(zp.x), zb = u2f(zp.y);
          za += dpp<0x128>(za); zb += dpp<0x128>(zb); za += dpp<0x124>(za); zb += dpp<0x124>(zb);
          za = red_g(za); zb = red_g(zb);
          if (lane < 4) *(LAS unsigned*)(L + L_ZF + w * 16 + lane * 4) = pack2(za, zb);
          MEMBAR();
          *flg(c, F_KVR + w) = (unsigned)(layer + 1);
        }
        if (WARM0 && layer == 0) wait_lds<2, 0>(c, F_JOB, 1u);
        v4u kvf;
        v2u z0, z1, z2, z3;
#pragma nounroll
        for (unsigned sp = 0; sp < SPIN_CAP; ++sp) {
          const v4u fl = *(volatile LAS v4u*)flg(c, F_KVR);
          MEMBAR();
          kvf = *(LAS v4u*)(L + L_KVF + lv);
          z0 = *(LAS v2u*)(L + L_ZF + h * 8); z1 = *(LAS v2u*)(L + L_ZF + 16 + h * 8);
          z2 = *(LAS v2u*)(L + L_ZF + 32 + h * 8); z3 = *(LAS v2u*)(L + L_ZF + 48 + h * 8);
          MEMBAR();
          if ((unsigned)__builtin_amdgcn_readfirstlane(umin(umin(fl.x, fl.y), umin(fl.z, fl.w))) > (unsigned)layer) break;
        }
        stamp<1>(c);
        const bool up = (lane & 16) != 0;
        const v4u zz = {0u, 0u, 0u, 0u};
        B0 = mfma(up ? zz : kvf, pq0, zero16());
        B0 = mfma(up ? kvf : zz, pq1, B0);
        const float d0 = dot2(pq0.y, z0.y, dot2(pq0.x, z0.x, 0.f)) + dot2(pq0.w, z1.y, dot2(pq0.z, z1.x, 0.f));
        const float d1 = dot2(pq1.y, z2.y, dot2(pq1.x, z2.x, 0.f)) + dot2(pq1.w, z3.y, dot2(pq1.z, z3.x, 0.f));
        const float r0 = __builtin_amdgcn_rcpf(red_h(d0) + 1e-6f), r1 = __builtin_amdgcn_rcpf(red_h(d1) + 1e-6f);
        v4u o0, o1;
#pragma unroll
        for (int i = 0; i < 4; ++i) { o0[i] = pack2(B0[2 * i] * r0, B0[2 * i + 1] * r0); o1[i] = pack2(B0[8 + 2 * i] * r1, B0[9 + 2 * i] * r1); }
        st_ws(c, o0, lv, ob);
        st_ws(c, o1, lv + KB, ob);
        st_chk<false>(c, sum4(sum4(0u, o0), o1), 0u, ob, tagc);
      }
      stamp<1>(c);
      const int rtA = opq(rh * 2 + mh), clb = opq(cl * ACT_CL);
      auto res_epilogue = [&](int xi, bool final_out, unsigned xseq) INL {
        LAS char* xs = L + L_XCH;
#pragma unroll
        for (int q = 0; q < 4; ++q) *(LAS f32x4*)(xs + w * 4096 + q * 1024 + lv) = f32x4{A1[4 * q], A1[4 * q + 1], A1[4 * q + 2], A1[4 * q + 3]};
        MEMBAR();
        *flg(c, F_XF + w) = xseq;
        f32x4 o0, o1, o2, o3;
#pragma nounroll
        for (unsigned sp = 0; sp < SPIN_CAP; ++sp) {
          const unsigned fl = *flg(c, F_XF + (w ^ 1));
          MEMBAR();
          o0 = *(LAS f32x4*)(xs + (w ^ 1) * 4096 + lv); o1 = *(LAS f32x4*)(xs + (w ^ 1) * 4096 + 1024 + lv);
          o2 = *(LAS f32x4*)(xs + (w ^ 1) * 4096 + 2048 + lv); o3 = *(LAS f32x4*)(xs + (w ^ 1) * 4096 + 3072 + lv);
          MEMBAR();
          if ((unsigned)__builtin_amdgcn_readfirstlane(fl) >= xseq) break;
        }
        xr0 = v4u{pack2(A0[0] + o0[0], A0[1] + o0[1]), pack2(A0[2] + o0[2], A0[3] + o0[3]), pack2(A0[4] + o1[0], A0[5] + o1[1]), pack2(A0[6] + o1[2], A0[7] + o1[3])};
        xr1 = v4u{pack2(A0[8] + o2[0], A0[9] + o2[1]), pack2(A0[10] + o2[2], A0[11] + o2[3]), pack2(A0[12] + o3[0], A0[13] + o3[1]), pack2(A0[14] + o3[2], A0[15] + o3[3])};
        if (final_out) {
          const int lo = fresh_lane();
          GAS char* ob = (GAS char*)out + (size_t)((cl * 4 + rtA) * 32 + (lo & 31)) * 1024 + cg * 128 + kh * 64 + (lo >> 5) * 16;
          *(GAS v4u*)ob = xr0;
          *(GAS v4u*)(ob + 32) = xr1;
        } else {
          const int xb = WS_X + clb + (((REUSE ? xi >> 1 : xi) * 4 + rtA) * 16 + cg * 2 + kh) * REC;
          st_ws(c, xr0, lv, xb);
          st_ws(c, xr1, lv + KB, xb);
          const float s = csum(xr0, 0.f) + csum(xr1, 0.f);
          float q0 = 0.f, q1 = 0.f;
#pragma unroll
          for (int i = 0; i < 4; ++i) { q0 = dot2(xr0[i], xr0[i], q0); q1 = dot2(xr1[i], xr1[i], q1); }
          const auto sw = __builtin_amdgcn_permlane32_swap(f2u(s), f2u(q0 + q1), false, false);
          st_chk<true>(c, sum4(sum4(0u, xr0), xr1), f2u(u2f((unsigned)sw[0]) + u2f((unsigned)sw[1])), xb, tagc);
        }
      };
      unsigned m0, m1;
      {
        const int ab = WS_O + clb + ((lb * 4 + rtA) * 16 + kh) * REC;
        wpre<1>(c, kb0, kb1);
        open_phase<2, false>(c, a, k, lv, ab, tagc);
        stamp<1>(c);
        gemm<32, 2, 1, -1, false, 2, 8, 0, QUIET ? 0 : 99>(c, a, A0, A1, kb0, kb1, 0, 0, [&](auto F) INL {
          ramp_step<F.v, 8, 2, false>(c, a, k, lv, ab);
          check_step<F.v, 8, 2, false>(c, m0, m1, a, k, lv, ab, tagc);
        });
        stamp<1>(c);
        res_epilogue(layer * 2, false, (unsigned)(layer * 2 + 1));
      }
      wpre<0>(c, lv, lv);
      stamp<1>(c);
      const int rtB = w;
      v4u cs0, cs1;
      float sacc;
      auto ln_side = [&](auto F, int xb, float& var) INL -> bool {
        constexpr int f = F.v;
        if constexpr (f == 0) { sacc = u2f(k[0].y) + u2f(k[1].y); PIN(sacc); }
        ramp_step<f, 16, 1, true>(c, a, k, lv, xb);
        check_step<f, 16, 1, true>(c, m0, m1, a, k, lv, xb, tagc);
        if constexpr ((f & 7) == 0 && f >= 8 && f <= 56) { sacc += u2f(k[(f / 4) & 7].y) + u2f(k[(f / 4 + 1) & 7].y); PIN(sacc); }
        if constexpr (f == 57) {
          const auto sw = __builtin_amdgcn_permlane32_swap(f2u(sacc), f2u(sacc), false, false);
          var = set_stats(u2f((unsigned)sw[0]), u2f((unsigned)sw[1]));
          PIN(mfrag);
          return true;
        }
        return false;
      };
      const int tb0 = opqv(lv) + kh * 5120, tb1 = tb0 ^ 1024;
      const int ab4 = WS_H + clb + ((lb * 4 + rtA) * 43 + kh) * REC;
      const int hb = WS_H + clb + (lb * 4 + rtB) * 43 * REC;
      float r2;
      v4u ho, hp;
      unsigned hs;
      float h0, h1;
      auto hside = [&](auto F, f32x16& G, f32x16& U, int gi) INL {
        constexpr int f = F.v, sl = (f - 1) / 2;
        if constexpr (f >= 1 && f < 17) {
          if constexpr (f & 1) { h0 = relu(G[2 * sl]) * r2; PIN(h0); h1 = relu(G[2 * sl + 1]) * r2; PIN(h1); }
          else {
            h0 *= U[2 * sl]; PIN(h0); h1 *= U[2 * sl + 1]; PIN(h1);
            if constexpr (sl < 4) { ho[sl & 3] = pack2(h0, h1); PIN(ho); } else { hp[sl & 3] = pack2(h0, h1); PIN(hp); }
          }
        }
        if constexpr (f == 17) { st_ws(c, ho, lv, hb + hpos(gi) * REC); hs = sum4(0u, ho); PIN(hs); }
        if constexpr (f == 18) { st_ws(c, hp, lv + KB, hb + hpos(gi) * REC); hs = sum4(hs, hp); PIN(hs); }
        if constexpr (f == 19) st_chk<false>(c, hs, 0u, hb + hpos(gi) * REC, tagc);
      };
      auto corr = [&](auto F, int at, f32x16& G, f32x16& U, int gi) INL {
        if (F.v == at - 4) { cs0 = cs_frag(3 + 2 * gi); cs1 = cs_frag(4 + 2 * gi); }
        if (F.v == at) { G = mfma(cs0, mfrag, G); U = mfma(cs1, mfrag, U); }
      };
      const int xb3 = WS_X + clb + ((REUSE ? layer : layer * 2) * 4 + rtB) * 16 * REC;
      open_phase<1, true>(c, a, k, lv, xb3, tagc);
      stamp<1>(c); stamp<2>(c);
      if (layer == 1) stamp<5>(c);
      gemm<64, 2, 0, 0, false, 3, 34, 0, 99>(c, a, A0, A1, lv, lv, lv, lv, [&](auto F) INL {
        float var;
        if (ln_side(F, xb3, var)) { r2 = __builtin_amdgcn_rcpf(var); PIN(r2); }
        if (F.v == 54 && layer == 0) wait_lds<1, 0>(c, F_CS3R, 1u);
        corr(F, 62, A0, A1, 0);
        if ((F.v & 7) == 7 && layer == 1) stamp<5>(c);
      });
      stamp<2>(c);
      if (ng == 3) {
        gemm<64, 2, 0, 0, false, 3, 0, 0, 99>(c, a, B0, B1, lv, lv, lv, lv, [&](auto F) INL {
          hside(F, A0, A1, 0);
          corr(F, 37, B0, B1, 1);
          if ((F.v & 7) == 7 && layer == 1) stamp<5>(c);
        });
        A0 = B0; A1 = B1;
        PIN(A0); PIN(A1);
      }
      stamp<2>(c);
      unsigned tg0 = 0, tg1 = 0;
      if (ng == 2) tg0 = (unsigned)__builtin_amdgcn_s_memrealtime();
      gemm<64, 2, 0, 4, false, 3, 0, 0, 99>(c, a, B0, B1, lv, lv, tb0, tb1, [&](auto F) INL {
        constexpr int f = F.v;
        hside(F, A0, A1, ng - 2);
        corr(F, 31, B0, B1, ng - 1);
        if ((F.v & 7) == 7 && layer == 1) stamp<5>(c);
        if constexpr (f >= 20 && f < 28) { res_init(f - 20); PIN(A0); }
        if (f == 31 && ng == 2) { tg1 = (unsigned)__builtin_amdgcn_s_memrealtime(); __builtin_amdgcn_s_sleep(NG2A); }
        if constexpr (f >= 32 && (f & 3) < 3) ld_rec<2, false>(c, a, k, lv, ab4, (f - 32) >> 2, f & 3);
        if constexpr (f >= 56) check_step<f - 64, 22, 2, false>(c, m0, m1, a, k, lv, ab4, tagc);
      });
      if (ng == 2) {
        sfor<20>([&](auto F) INL { hside(F, B0, B1, 1); });
        while ((unsigned)__builtin_amdgcn_s_memrealtime() - tg0 < 4 * (tg1 - tg0) + NG2X) __builtin_amdgcn_s_sleep(1);
      }
      stamp<1>(c); stamp<2>(c); stamp<4>(c);
      {
        auto side4 = [&](auto F) INL {
          constexpr int f = F.v;
          if constexpr (f < 10) { hside(IC<2 * f>{}, B0, B1, ng - 1); hside(IC<2 * f + 1>{}, B0, B1, ng - 1); }
          if constexpr (f < 32 && (f & 3) < 3) ld_rec<2, false>(c, a, k, lv, ab4, 8 + (f >> 2), f & 3);
          if constexpr (f >= S2H && f < S2H + 12) {
            ld_rec<2, false>(c, a, k, lv, ab4, 16 + (f - S2H) / 2, (f - S2H) & 1);
            if constexpr ((f - S2H) & 1) ld_rec<2, false>(c, a, k, lv, ab4, 16 + (f - S2H) / 2, 2);
          }
          if constexpr (f < 72) check_step<f, 22, 2, false>(c, m0, m1, a, k, lv, ab4, tagc);
          if constexpr (f == 76) settle<1, 2, false>(c, rec_bad<false>(a, k, 20, tagc), a, k, lv, ab4, 20, tagc);
          if constexpr (f == 80) { if (kh == 0) settle<1, 2, false>(c, rec_bad<false>(a, k, 21, tagc), a, k, lv, ab4, 21, tagc); }
          if ((f & 7) == 7) stamp<4>(c);
        };
        gemm<88, 2, 4, -1, false, 2, 32, 3, P4CAP, 0, 84>(c, a, A0, A1, tb0, tb1, 0, 0, side4);
        if (kh == 0) gemm<88, 2, 4, -1, false, 2, 32, 3, P4CAP, 84, 88>(c, a, A0, A1, tb0, tb1, 0, 0, NoSide());
        stamp<1>(c); stamp<2>(c);
        tagc += 0x632BE5AAu;
        res_epilogue(1, layer == 1, (unsigned)(layer * 2 + 2));
      }
      if (layer == 1) break;
      wpre<0>(c, lv, lv);
      stamp<1>(c);
      const int xb1 = WS_X + clb + ((REUSE ? 0 : 4) + rtB) * 16 * REC;
      open_phase<1, true>(c, a, k, lv, xb1, tagc);
      stamp<1>(c);
      gemm<64, 2, 0, -1, true, 3, 34, 0, 99>(c, a, A0, A1, lv, lv, 0, 0, [&](auto F) INL {
        constexpr int f = F.v;
        float var;
        if (ln_side(F, xb1, var)) { rstd = __builtin_amdgcn_rsqf(var); rs_put(); }
        if (f == 53) { cs0 = cs_frag(0); cs1 = cs_frag(1); }
        if (f == 61) { A0 = mfma(mfrag, cs0, A0); A1 = mfma(mfrag, cs1, A1); }
        if (f >= 59 && f < 63) rs_get(f - 59);
      });
      tagc += 0x632BE5AAu;
    }
  } else if (wave < 6) {
    const int lw = wave - 4;
    LANEVARS
    volatile LAS unsigned* full = flg(c, F_FULL + lw);
    __builtin_amdgcn_s_setprio(3);
    load_half0(0);
    if (CODEPF && lw == 1) {
      glds((const char*)(pc0 & ~(u64)255) + j * (CODEPF / 32), min(lane * 32, CODEPF / 32 - 32), L_RING + 7 * SLOT + 12288);
    } else {
      asm volatile("" ::: "memory");
    }
    __builtin_amdgcn_s_setprio(2);
    load_half0(1);
    load_half0(2);
    VMWAIT(16); *full = 1u;
    stamp<3>(c);
    load_half0(3);
    VMWAIT(16); *full = 2u;
    if (TOUCH == 2) wait_lds<2, 1>(c, F_CS1R, 1u);
    {
      const unsigned tv = min(lane, 42) * 128, td = L_RING + (6 + lw) * SLOT + (lw ? 13312 : 3072);
      glds((const char*)(lw ? wu : wg) + b * 5504, tv, td);
      if (lw) glds((const char*)wd + b * 5504, tv, td);
      else glds((const char*)wo + b * 2048, (lane & 15) * 128, td);
    }
    *flg(c, F_QGO + lw) = 1u;
    VMWAIT(10); *full = 3u;
    VMWAIT(2); *full = 4u;
    __builtin_amdgcn_s_setprio(1);
    load_half0(4);
    stamp<3>(c);
    load_half0(5);
    VMWAIT(8); *full = 5u;
    VMWAIT(0); *full = 6u;
    stamp<1>(c); stamp<3>(c);
    int seq = 6, pub = 6;
    v4u wc[48];
    auto stream = [&](int seg, int i0, int i1) INL {
      const int ln = fresh_lane(), lv = ln * 16;
      const unsigned vo2 = (ln >> 3) * 1024 + (((ln & 7) ^ (((ln >> 4) & 1) << 2)) * 16);
      const int src = (seg == 0 ? WS_WT1 + m * 96 * KB : WS_WT3 + g0 * 64 * KB);
#pragma unroll 1
      for (int i = i0; i < i1; ++i) {
        if (seq >= NSLOT) {
          const unsigned need = (unsigned)(seq - NSLOT + 1);
          bool first = true;
#pragma nounroll
          for (unsigned sp = 0; sp < SPIN_CAP; ++sp) {
            v4u f = *(volatile LAS v4u*)flg(c, F_FREE);
            if ((unsigned)__builtin_amdgcn_readfirstlane(umin(umin(f.x, f.y), umin(f.z, f.w))) >= need) break;
            if (first && pub < seq) { VMWAIT(0); pub = seq; *full = (unsigned)pub; }
            first = false;
            __builtin_amdgcn_s_sleep(1);
          }
          MEMBAR();
        }
        const unsigned dst = L_RING + (seq & (NSLOT - 1)) * SLOT + lw * 8192;
        const int hs = i * 2 + lw, hr = (hs & ~7) | ((hs + c.rot) & 7);
        if (seg == 1) {
          const char* sp = (const char*)wo + (size_t)hr * 65536 + cg * 128;
#pragma unroll
          for (int e = 0; e < 8; ++e) glds(sp + e * 8192, vo2, dst + e * 1024);
        } else if (seg == 0 && i >= 4) {
          glds4(ws + src + 65536 + ((hs * 2 + c.rot) & 7) * 4096, lv, dst);
          glds4(ws + src + 65536 + ((hs * 2 + 1 + c.rot) & 7) * 4096, lv, dst + 4096);
        } else {
          glds4(ws + src + hr * 8192, lv, dst);
          glds4(ws + src + hr * 8192 + 4096, lv, dst + 4096);
        }
        ++seq;
        if (seq - pub > LDEP) {
          if (LDEP == 1) VMWAIT(8); else if (LDEP == 2) VMWAIT(16); else VMWAIT(24);
          pub = seq - LDEP; *full = (unsigned)pub;
        }
      }
    };
    auto drain = [&]() INL { VMWAIT(0); if (pub < seq) { pub = seq; *full = (unsigned)seq; } };
    wait_lds<2, 1>(c, F_JOB, 1u);
    stream(1, 0, 4);
    drain();
    stamp<1>(c); stamp<3>(c);
    if (lw == 0) { poll_tags<32>(c, WS_F1 + m * 2048, *(volatile LAS v2u*)flg(c, F_TAG)); *flg(c, F_PTOK) = 1u; }
    else wait_lds<1, 1>(c, F_PTOK, 1u);
    stamp<1>(c); stamp<3>(c);
    stream(2, 0, 8);
    wait_lds<4, 2>(c, F_FREE, 12u);
    drain();
    cache_load(wc, IC<6>{}, IC<0>{}, IC<3>{});
    stream(2, 8, 4 * ng);
    drain();
    stamp<1>(c);
    cache_load(wc, IC<6>{}, IC<3>{}, IC<6>{});
    cache_put(wc, IC<6>{}, IC<1>{}, seq);
    seq += 11; pub = seq;
    stamp<1>(c);
    stream(0, 0, 6);
    stream(1, 0, 4);
    stamp<1>(c);
    stream(2, 0, 4 * ng);
    drain();
    stamp<1>(c);
    cache_put(wc, IC<6>{}, IC<0>{}, seq);
  } else {
    const int hh = wave - 6;
    LANEVARS
    const int pts = L_RING + (6 + hh) * SLOT;
    __builtin_amdgcn_s_setprio(3);
    {
      const unsigned vo1 = (lane >> 2) * 3072 + (lane & 3) * 16;
      const char* j0 = (const char*)wqkv + (size_t)(cl * 32) * 3072 + m * 64;
      glds(j0 + (1 + hh) * 1024, vo1, pts); glds(j0 + (1 + hh) * 1024 + 16 * 3072, vo1, pts + 1024);
      glds(j0 + hh * 16 * 3072, vo1, pts + 2048);
    }
    const int srow = cl * 128 + m * 8 + hh * 4 + (lane >> 4);
    v4u xs[4];
    v2u e0;
    {
      const GAS char* xp = (const GAS char*)x + (size_t)srow * 1024 + (lane & 15) * 16;
      const int zo = opqv(0);
      asm volatile("global_load_dwordx4 %0, %5, off\n\tglobal_load_dwordx4 %1, %5, off offset:256\n\tglobal_load_dwordx4 %2, %5, off offset:512\n\t"
                   "global_load_dwordx4 %3, %5, off offset:768\n\tglobal_load_dwordx2 %4, %6, %7 sc1"
                   : "=&v"(xs[0]), "=&v"(xs[1]), "=&v"(xs[2]), "=&v"(xs[3]), "=&v"(e0) : "v"(xp), "v"(zo), "s"(ws + WS_E) : "memory");
    }
    asm volatile("s_waitcnt vmcnt(0)" : "+v"(xs[0]), "+v"(xs[1]), "+v"(xs[2]), "+v"(xs[3]), "+v"(e0)::"memory");
    const u64 tag = mix64(((u64)(unsigned)__builtin_amdgcn_readfirstlane(e0.y) << 32) | (unsigned)__builtin_amdgcn_readfirstlane(e0.x));
    const v2u tag2 = {(unsigned)tag, (unsigned)(tag >> 32)};
    if (hh == 0) *(volatile LAS v2u*)flg(c, F_TAG) = tag2;
    stamp<1>(c); stamp<3>(c);
    {
      const unsigned tb = ((unsigned)(mix64(tag) >> 32) >> ((lane & 3) * 8)) & 0xffu;
      const v4u f0 = tr_frag(L + pts + (hh ? trS : trN)), f1 = tr_frag(L + pts + 1024 + (hh ? trS : trN)), f2 = tr_frag(L + pts + 2048 + trN);
      const float s0 = red_h(csum(f1, csum(f0, 0.f))), s2 = red_h(csum(f2, 0.f));
      __builtin_amdgcn_raw_buffer_store_b32((f2u(h ? s2 : s0) & 0xffffff00u) | tb, c.rs, l31 * 4 + h * 4096, WS_C1P + m * 8 * KB + cl * 256 + hh * 128, 16);
      float sa = 0.f, sb = 0.f, qa = 0.f, qb = 0.f;
#pragma unroll
      for (int i = 0; i < 4; ++i) {
        sa = dot2(xs[i].y, ONES2, dot2(xs[i].x, ONES2, sa)); sb = dot2(xs[i].w, ONES2, dot2(xs[i].z, ONES2, sb));
        qa = dot2(xs[i].y, xs[i].y, dot2(xs[i].x, xs[i].x, qa)); qb = dot2(xs[i].w, xs[i].w, dot2(xs[i].z, xs[i].z, qb));
      }
      sa += sb; qa += qb;
      sa += dpp<0x128>(sa); qa += dpp<0x128>(qa); sa += dpp<0x124>(sa); qa += dpp<0x124>(qa);
      sa += dpp<0x122>(sa); qa += dpp<0x122>(qa); sa += dpp<0x121>(sa); qa += dpp<0x121>(qa);
      if ((lane & 15) == 0) st_ws(c, v4u{f2u(sa), tag2.x, f2u(qa), tag2.y}, srow * 16, WS_S0P);
      if (KVWARM) {
        const unsigned nt = ~(hh ? tag2.y : tag2.x);
        const v4u W = {nt, nt >> 8, nt >> 16, nt >> 24};
        const int kvb = WS_KVP + ((hh * 16 + m) * 16 + cl) * KVR;
        st_ws(c, W, lv, kvb); st_ws(c, W, lv + KB, kvb);
        if (lane < 8) st_ws(c, W, lv + 2 * KB, kvb);
      }
      VMWAIT(0);
      if (CSDELAY) __builtin_amdgcn_s_sleep(CSDELAY);
      stamp<3>(c);
      {
        float t;
#pragma nounroll
        for (unsigned sp = 0; sp < SPIN_CAP; ++sp) {
          unsigned p[16];
#pragma unroll
          for (int i = 0; i < 16; ++i) p[i] = __builtin_amdgcn_raw_buffer_load_b32(c.rs, lane * 4 + i * 256, WS_C1P + m * 8 * KB + hh * 4096, 16);
          SB();
          unsigned chk = 0;
          t = 0.f;
#pragma unroll
          for (int i = 0; i < 16; ++i) { p[i] ^= tb; chk |= p[i]; t += u2f(p[i]); }
          if (!__any((chk & 0xffu) != 0u)) break;
          MEMBAR();
        }
        if (hh) t = red_h(t);
        if (hh == 0 || h == 0) *(LAS v4u*)(L + L_CS + hh * 1024 + lv) = cs_operand(t);
        MEMBAR();
        *flg(c, F_CS1R + hh) = 1u;
      }
      stamp<3>(c);
      __builtin_amdgcn_s_setprio(1);
      wait_lds<2, 1>(c, F_QGO, 1u);
      const int pt3 = pts + 4096;
      const unsigned vo3 = (lane >> 2) * 2752 + (lane & 3) * 16;
      const char* w3 = (const char*)(hh ? wu : wg) + (size_t)(cl * 32) * 2752;
#pragma unroll
      for (int i = 0; i < 3; ++i) {
        const int aa = g0 + min(i, ng - 1);
        glds(w3 + aa * 64, vo3, pt3 + i * 2048);
        glds(w3 + aa * 64 + 16 * 2752, vo3, pt3 + i * 2048 + 1024);
      }
      st_ws(c, f0, lv, WS_WT1 + m * 96 * KB + (cl * 4 + hh) * KB);
      st_ws(c, f1, lv, WS_WT1 + m * 96 * KB + (cl * 4 + 2 + hh) * KB);
      st_ws(c, f2, lv, WS_WT1 + m * 96 * KB + (64 + cl * 2 + hh) * KB);
      if (WARM0) {
        const v4u Z = {0u, 0u, 0u, 0u};
#pragma unroll
        for (int e = 0; e < 4; ++e) {
          const int off = cl * ACT_CL + (e < 2 ? WS_O + ((2 * hh + e) * 16 + m) * REC : WS_X + ((rh * 2 + hh) * 16 + cg * 2 + (e & 1)) * REC);
          st_ws(c, Z, lv, off); st_ws(c, Z, lv + KB, off);
          if (h == 0) st_ws(c, Z, lv + 2 * KB, off);
        }
      }
      VMWAIT(0);
#pragma unroll
      for (int i = 0; i < 3; ++i) {
        const int aa = g0 + min(i, ng - 1);
        const v4u g0f = tr_frag(L + pt3 + i * 2048 + trS), g1f = tr_frag(L + pt3 + i * 2048 + 1024 + trS);
        st_ws(c, g0f, lv, WS_WT3 + aa * 64 * KB + (cl * 4 + hh) * KB);
        st_ws(c, g1f, lv, WS_WT3 + aa * 64 * KB + (cl * 4 + 2 + hh) * KB);
        const float s = red_h(csum(g1f, csum(g0f, 0.f)));
        if (h == 0) __builtin_amdgcn_raw_buffer_store_b32(f2u(s), c.rs, l31 * 4, WS_CSP + aa * 4 * KB + cl * 256 + hh * 128, 16);
      }
      MEMBAR();
      *flg(c, F_JOB + hh) = 1u;
    }
    VMWAIT(0);
    if (lane == 0) __builtin_amdgcn_raw_buffer_store_b64(tag2, c.rs, 0, WS_F1 + ((m * 16 + cl) * 2 + hh) * 64, 16);
    stamp<1>(c); stamp<3>(c);
    v4u wc[40];
    cache_load(wc, IC<5>{}, IC<0>{}, IC<5>{});
    if (hh == 1) {
      wait_lds<1, 2>(c, F_PTOK, 1u);
#pragma unroll 1
      for (int i = 0; i < ng; ++i) {
        float t = 0.f;
#pragma unroll
        for (int q = 0; q < 16; ++q) t += u2f(__builtin_amdgcn_raw_buffer_load_b32(c.rs, lane * 4 + q * 256, WS_CSP + (g0 + i) * 4 * KB, 16));
        *(LAS v4u*)(L + L_CS + (3 + 2 * i) * 512 + lv) = cs_operand(t);
      }
      MEMBAR();
      *flg(c, F_CS3R) = 2u;
      MEMBAR();
    }
    stamp<1>(c);
    __builtin_amdgcn_s_setprio(1);
    cache_put(wc, IC<5>{}, IC<1>{}, 10 + 4 * ng);
    stamp<1>(c);
    if (b == 0 && wave == 6 && lane == 0) __hip_atomic_store((GAS u64*)(ws + WS_E), tag, RLX_AGENT);
    stamp<1>(c);
    __builtin_amdgcn_s_setprio(1);
    cache_put(wc, IC<5>{}, IC<0>{}, 31 + 8 * ng);
  }
}

extern "C" void kernel_launch(void* const* d_in, const int* in_sizes, int n_in, void* d_out, int out_size, void* d_ws, size_t ws_size,
                              hipStream_t stream) {
  fused_fwd<<<256, 512, 0, stream>>>((const u16*)d_in[0], (const u16*)d_in[1], (const u16*)d_in[2], (const u16*)d_in[4], (const u16*)d_in[5],
                                     (const u16*)d_in[6], (u16*)d_out, (char*)d_ws);
}
```

```cpp
#include <hip/hip_runtime.h>
#include <stdint.h>
#include <utility>

#define GAS __attribute__((address_space(1)))
#define LAS __attribute__((address_space(3)))
#define RLX_AGENT __ATOMIC_RELAXED, __HIP_MEMORY_SCOPE_AGENT
typedef unsigned v4u __attribute__((ext_vector_type(4)));
typedef unsigned v2u __attribute__((ext_vector_type(2)));
typedef float f32x2 __attribute__((ext_vector_type(2)));
typedef float f32x4 __attribute__((ext_vector_type(4)));
typedef float f32x16 __attribute__((ext_vector_type(16)));
typedef short s16x8 __attribute__((ext_vector_type(8)));
typedef short s16x4 __attribute__((ext_vector_type(4)));
typedef __bf16 bf16x2 __attribute__((ext_vector_type(2)));
typedef unsigned short u16;
typedef unsigned long long u64;

#define STAMP 0
#define CYC 0
#define LDEP 2
#define XDELAY 12
#define WPACE 2
#define S2H 44
#define BACKOFF 4
#define PTOUCH 0
#define NG2A 32
#define NG2T 150u
#define WARM 0
#define ROT 0
#define KVWARM 1
#define KVBACK 4
#define KVDELAY 18
#define QUIET 0
#define REUSE 1
#define WARM0 0
#define QAT 4u
#define CSDELAY 8
#define TOUCH 1
#define S0AT 0
#define LSLP 4
#define OUTMODE 0
#define OPENR 4
#define RELA 34
#define S0F 32
#define HPER 4
#define H2PER 3
#define PACE 1
#define PUTP 0
#define HVIA 0
#define GFAST 0
#define XD0 12
#define XD1 12
#define XD2 12
#define KVDP 8
#define NG2S 90u
#define RELD 62
#define DPER 6
#define XPRE 2
#define XW0 8
#define P4CAP 7
#define CODEPF 59392

constexpr int KB = 1024;
constexpr int WS_E = 0;
constexpr int WS_F1 = 4 * KB;
constexpr int WS_WT1 = 128 * KB;
constexpr int WS_WT3 = WS_WT1 + 16 * 96 * KB;
constexpr int WS_CSP = WS_WT3 + 43 * 64 * KB;
constexpr int WS_S0P = WS_CSP + 43 * 4 * KB;
constexpr int WS_C1P = WS_S0P + 32 * KB;
constexpr int KVR = 2176;
constexpr int WS_KVP = WS_C1P + 16 * 8 * KB;
constexpr int REC = 2560;
constexpr int WS_ACT = 6 * KB * KB, ACT_CL = 2 * KB * KB;
constexpr int WS_O = WS_ACT;
constexpr int WS_X = WS_O + 2 * 4 * 16 * REC;
constexpr int WS_H = WS_X + 3 * 4 * 16 * REC;
static_assert(WS_KVP + 512 * KVR <= WS_ACT && WS_H + (2 * 4 * 43 + 1) * REC <= WS_ACT + ACT_CL, "ws");
constexpr int WS_DBG = WS_ACT + 16 * ACT_CL;
constexpr int WS_END = WS_DBG + 256 * 2 * KB;

constexpr int SLOT = 16384, NSLOT = 8;
constexpr int NSTAMP = 36;
constexpr int L_FLG = 0;
constexpr int L_ZF = L_FLG + 128;
constexpr int L_CS = L_ZF + 64;
constexpr int L_KVF = L_CS + 9 * 512;
constexpr int L_STAMP = L_KVF + 1024;
constexpr int L_KVS = L_STAMP + 1152;
constexpr int L_XCH = 16384;
constexpr int L_RING = 32768;
constexpr int L_END = L_RING + NSLOT * SLOT;
static_assert(L_KVS + 4 * 2304 <= L_XCH && L_END == 163840 && (L_KVS & 15) == 0 && (L_CS & 15) == 0, "lds");
enum { F_FULL = 0, F_TAG = 2, F_FREE = 4, F_JOB = 8, F_KFL = 12, F_QGO = 10, F_HF = 24, F_HACK = 30, F_CS3R = 15, F_PTOK = 14, F_KVR = 16, F_XF = 20, F_CS1R = 28 };
constexpr unsigned SPIN_CAP = 1u << 17;

template <int V> struct IC { static constexpr int v = V; __device__ constexpr operator int() const { return V; } };
template <class F, int... I> __device__ __forceinline__ void sfor_(F&& fn, std::integer_sequence<int, I...>) { (fn(IC<I>{}), ...); }
template <int N, class F> __device__ __forceinline__ void sfor(F&& fn) { sfor_(fn, std::make_integer_sequence<int, N>{}); }
#define INL __attribute__((always_inline))
#define MEMBAR() asm volatile("" ::: "memory")
#define SB() __builtin_amdgcn_sched_barrier(0)
#define VMWAIT(n) asm volatile("s_waitcnt vmcnt(" #n ")" ::: "memory")
#define PIN(x) asm volatile("" : "+v"(x))

__device__ __forceinline__ unsigned umin(unsigned a, unsigned b) { return a < b ? a : b; }
__device__ __forceinline__ unsigned umax(unsigned a, unsigned b) { return a > b ? a : b; }
__device__ __forceinline__ int gsplit(int i) { return (43 * i) >> 4; }
__device__ __forceinline__ f32x16 mfma(v4u a, v4u b, f32x16 c) {
  return __builtin_amdgcn_mfma_f32_32x32x16_bf16(__builtin_bit_cast(s16x8, a), __builtin_bit_cast(s16x8, b), c, 0, 0, 0);
}
__device__ __forceinline__ f32x16 zero16() { return f32x16{0.f, 0.f, 0.f, 0.f, 0.f, 0.f, 0.f, 0.f, 0.f, 0.f, 0.f, 0.f, 0.f, 0.f, 0.f, 0.f}; }
__device__ __forceinline__ unsigned pack2(float a, float b) {
  bf16x2 v = {(__bf16)a, (__bf16)b};
  return __builtin_bit_cast(unsigned, v);
}
__device__ __forceinline__ float u2f(unsigned u) { return __builtin_bit_cast(float, u); }
__device__ __forceinline__ unsigned f2u(float f) { return __builtin_bit_cast(unsigned, f); }
__device__ __forceinline__ float blo(unsigned u) { return u2f(u << 16); }
__device__ __forceinline__ float bhi(unsigned u) { return u2f(u & 0xffff0000u); }
__device__ __forceinline__ float relu(float x) { return u2f((unsigned)max((int)f2u(x), 0)); }
__device__ __forceinline__ float dot2(unsigned a, unsigned b, float acc) {
  return __builtin_amdgcn_fdot2_f32_bf16(__builtin_bit_cast(bf16x2, a), __builtin_bit_cast(bf16x2, b), acc, false);
}
constexpr unsigned ONES2 = 0x3f803f80u;
constexpr float LOG2E = 1.44269504f;
__device__ __forceinline__ float red_h(float v) {
  auto s = __builtin_amdgcn_permlane32_swap(f2u(v), f2u(v), false, false);
  return u2f((unsigned)s[0]) + u2f((unsigned)s[1]);
}
__device__ __forceinline__ float red_g(float v) {
  auto r = __builtin_amdgcn_permlane16_swap(f2u(v), f2u(v), false, false);
  return red_h(u2f((unsigned)r[0]) + u2f((unsigned)r[1]));
}
template <int CTRL> __device__ __forceinline__ float dpp(float v) { return u2f(__builtin_amdgcn_update_dpp(0, (int)f2u(v), CTRL, 0xf, 0xf, false)); }
__device__ __forceinline__ float exp2c(float x) { return __builtin_amdgcn_fmed3f(__builtin_amdgcn_exp2f(x), 0.f, 1.f); }
__device__ __forceinline__ void split2(float v, unsigned& hi, unsigned& lo) {
  hi = pack2(v, 0.f) & 0xffffu;
  lo = pack2(v - blo(hi), 0.f) & 0xffffu;
}
template <int D = 256> __device__ __forceinline__ v4u tr_frag(LAS char* p) {
  v2u lo = __builtin_bit_cast(v2u, __builtin_amdgcn_ds_read_tr16_b64_v4i16((LAS s16x4*)p));
  v2u hi = __builtin_bit_cast(v2u, __builtin_amdgcn_ds_read_tr16_b64_v4i16((LAS s16x4*)(p + D)));
  return v4u{lo.x, lo.y, hi.x, hi.y};
}
__device__ __forceinline__ void glds(const void* sbase, unsigned voff, unsigned ldst) {
  asm volatile("s_mov_b32 m0, %2\n\ts_nop 0\n\tglobal_load_lds_dwordx4 %0, %1" ::"v"(voff), "s"(sbase), "s"(ldst) : "memory");
}
__device__ __forceinline__ void glds4(const void* sbase, unsigned voff, unsigned ldst) {
  asm volatile(
      "s_mov_b32 m0, %2\n\ts_nop 0\n\tglobal_load_lds_dwordx4 %0, %1 sc1\n\tglobal_load_lds_dwordx4 %0, %1 offset:1024 sc1\n\t"
      "global_load_lds_dwordx4 %0, %1 offset:2048 sc1\n\tglobal_load_lds_dwordx4 %0, %1 offset:3072 sc1" ::"v"(voff), "s"(sbase), "s"(ldst)
      : "memory");
}

struct Ctx {
  LAS char* L;
  __amdgpu_buffer_rsrc_t rs;
  int lane, wave;
  int seq, known;
  int vb0, vb1, nb0, nb1;
  int rot;
  int fa;
  v2u fpre;
  v4u w0, w1, w2, w3;
  int nst;
  unsigned dbgt;
  template <int I> __device__ __forceinline__ v4u& wf() { if constexpr (I == 0) return w0; else if constexpr (I == 1) return w1; else if constexpr (I == 2) return w2; else return w3; }
};
__device__ __forceinline__ volatile LAS unsigned* flg(Ctx& c, int i) { return (volatile LAS unsigned*)(c.L + L_FLG) + i; }
__device__ __forceinline__ void stamp0(Ctx& c) {
}
template <int K> __device__ __forceinline__ void stamp(Ctx& c) { if (STAMP == K) stamp0(c); }
template <int N, int SLP> __device__ __forceinline__ void wait_lds(Ctx& c, int i, unsigned val) {
#pragma nounroll
  for (unsigned sp = 0; sp < SPIN_CAP; ++sp) {
    unsigned v;
    if (N == 1) v = *flg(c, i);
    else if (N == 2) { v2u f = *(volatile LAS v2u*)flg(c, i); v = umin(f.x, f.y); }
    else { v4u f = *(volatile LAS v4u*)flg(c, i); v = umin(umin(f.x, f.y), umin(f.z, f.w)); }
    if ((unsigned)__builtin_amdgcn_readfirstlane(v) >= val) break;
    if (SLP) __builtin_amdgcn_s_sleep(SLP);
  }
  MEMBAR();
}
template <int N> __device__ __forceinline__ void poll_tags(Ctx& c, int base, v2u tag) {
#pragma nounroll
  for (unsigned sp = 0; sp < SPIN_CAP; ++sp) {
    const v2u v = __builtin_amdgcn_raw_buffer_load_b64(c.rs, (c.lane & (N - 1)) * 64, base, 16);
    if (__all(v.x == tag.x && v.y == tag.y)) break;
    __builtin_amdgcn_s_sleep(4);
    MEMBAR();
  }
  MEMBAR();
}
__device__ __forceinline__ v4u ld_ws(Ctx& c, int voff, int soff) { return __builtin_amdgcn_raw_buffer_load_b128(c.rs, voff, soff, 16); }
__device__ __forceinline__ void st_ws(Ctx& c, v4u v, int voff, int soff) {
  __builtin_amdgcn_raw_buffer_store_b128(v, c.rs, voff, soff, 16);
  asm volatile("s_nop 1" ::"v"(v) : "memory");
}
__device__ __forceinline__ int fresh_lane() { int l; asm volatile("v_mbcnt_lo_u32_b32 %0, -1, 0\n\tv_mbcnt_hi_u32_b32 %0, -1, %0" : "=v"(l)); __builtin_assume(l >= 0 && l < 64); return l; }
__device__ __forceinline__ int opq(int v) { asm volatile("" : "+s"(v)); return v; }
__device__ __forceinline__ int opqv(int v) { asm volatile("" : "+v"(v)); return v; }
__device__ __forceinline__ v4u ld_fr(Ctx& c, int lv, int sb, int i) { return ld_ws(c, lv + (i & 3) * KB, sb + (i >> 2) * 4 * KB); }

constexpr int fps(int mode) { return (mode == 1 || mode == 4) ? 8 : 16; }
constexpr bool two(int mode) { return mode == 1 || mode == 2 || mode == 4; }
__device__ __forceinline__ void slot_wait(Ctx& c, int sq) {
  if (__builtin_expect(c.known <= sq, 0)) {
    if (CYC == 2) c.dbgt -= (unsigned)__builtin_amdgcn_s_memtime();
#pragma nounroll
    for (unsigned sp = 0; sp < SPIN_CAP; ++sp) {
      v2u f = *(volatile LAS v2u*)flg(c, F_FULL);
      c.known = __builtin_amdgcn_readfirstlane(umin(f.x, f.y));
      if (c.known > sq) break;
    }
    if (CYC == 2) c.dbgt += (unsigned)__builtin_amdgcn_s_memtime();
  }
  MEMBAR();
}
template <int MODE> __device__ __forceinline__ void slot_base(Ctx& c, int sq, int lb0, int lb1) {
  const int sb = (sq & (NSLOT - 1)) * SLOT;
  c.nb0 = lb0 + sb; PIN(c.nb0);
  if (two(MODE)) { c.nb1 = lb1 + sb; PIN(c.nb1); }
}
template <int MODE, int F> __device__ __forceinline__ v4u wread(Ctx& c) {
  constexpr int i = F % fps(MODE);
  if constexpr (i == 0) { c.vb0 = c.nb0; if (two(MODE)) c.vb1 = c.nb1; }
  LAS char* R = c.L + L_RING;
  if (MODE == 0) return *(LAS v4u*)(R + c.vb0 + i * KB);
  if (MODE == 1) return tr_frag<512>(R + ((i & 1) ? c.vb1 : c.vb0) + ((i >> 1) & 1) * 2 * KB + (i >> 2) * 8 * KB);
  if (MODE == 4) return *(LAS v4u*)(R + ((i & 1) ? c.vb1 : c.vb0) + ((i >> 1) & 1) * 2 * KB + (i >> 2) * 8 * KB);
  if (MODE == 2) return tr_frag(R + ((i & 1) ? c.vb1 : c.vb0) + i * KB);
  return tr_frag(R + c.vb0 + i * KB);
}
__device__ __forceinline__ void ring_publish(Ctx& c) { MEMBAR(); *(volatile LAS unsigned*)(c.L + c.fa) = (unsigned)c.seq; MEMBAR(); }
template <int MODE> __device__ __forceinline__ void wpre(Ctx& c, int lb0, int lb1) {
  slot_wait(c, c.seq);
  slot_base<MODE>(c, c.seq, lb0, lb1);
  sfor<4>([&](auto F) INL { c.wf<F.v>() = wread<MODE, F.v>(c); });
  if (fps(MODE) == 8) c.fpre = *(volatile LAS v2u*)flg(c, F_FULL);
}
struct NoSide { template <class T> __device__ __forceinline__ void operator()(T) const {} };
constexpr u64 DUAL = 0xFEDC76B54A329180ull;
constexpr int dual_first(int ch) { for (int u = 0; u < 16; ++u) if (((DUAL >> (4 * u)) & 15) == (u64)ch) return u; return -1; }
template <int NF, int TPS, int MODE, int NEXT, bool XA, int ZERO, int RELF, int EARLY, int CAP, int F0, int F1, class SIDE>
__device__ __forceinline__ void gemm4(Ctx& c, v4u (&a)[32], f32x16& c0, f32x16& c1, f32x16& c2, f32x16& c3, int lb0, int lb1, int nb0, int nb1, SIDE side) {
  constexpr int FPS = fps(MODE);
  static_assert(NF % FPS == 0, "whole slots");
  sfor<F1 - F0>([&](auto FI) INL {
    constexpr IC<F0 + FI.v> F{};
    constexpr int f = F.v, un = NF == 128 ? (int)((DUAL >> (4 * (f / 8))) & 15) : 0;
    constexpr int s = NF == 128 ? 4 * (un & 7) + (f % 8) / 2 : f / TPS, t = f % TPS, fn = f + 4, i = fn % FPS;
    const v4u x = a[s & 31], wv = c.wf<f % 4>();
    f32x16& acc = un >= 8 ? (t ? c3 : c2) : (t ? c1 : c0);
    const f32x16 cin = (s == 0 && ((ZERO >> t) & 1)) ? zero16() : acc;
    acc = XA ? mfma(x, wv, cin) : mfma(wv, x, cin);
    SB();
    if constexpr (fn < NF) {
      c.wf<f % 4>() = wread<MODE, fn>(c);
      constexpr bool lastslot = fn / FPS == NF / FPS - 1;
      if constexpr (!lastslot || NEXT >= 0) {
        if constexpr (i == FPS - 7) c.fpre = *(volatile LAS v2u*)flg(c, F_FULL);
        if constexpr (i == FPS - 4) { c.fpre.x = umin(c.fpre.x, c.fpre.y); PIN(c.fpre); }
        if constexpr (i == FPS - 3) { c.known = __builtin_amdgcn_readfirstlane(c.fpre.x); asm volatile("" : "+s"(c.known)); }
        if constexpr (i == FPS - 2) { if constexpr (lastslot) slot_base<(NEXT < 0 ? 0 : NEXT)>(c, c.seq + 1, nb0, nb1); else slot_base<MODE>(c, c.seq + 1, lb0, lb1); }
        if constexpr (i == FPS - 1) slot_wait(c, c.seq + 1);
      }
      if constexpr (i == FPS - 1) c.seq++;
    } else if constexpr (NEXT >= 0) {
      c.wf<f % 4>() = wread<(NEXT < 0 ? 0 : NEXT), fn - NF>(c);
      if constexpr (fps(NEXT) == 8 && fn - NF == 1) c.fpre = *(volatile LAS v2u*)flg(c, F_FULL);
    }
    if constexpr (fn <= NF && i == 0) { if constexpr ((f >= RELF || fn / FPS - 1 < EARLY) && fn / FPS - 1 < CAP) ring_publish(c); }
    else if constexpr (f == RELF && RELF > 0) ring_publish(c);
    side(F);
    SB();
  });
}
template <int NF, int TPS, int MODE, int NEXT, bool XA, int ZERO, int RELF, int EARLY, int CAP, int F0 = 0, int F1 = NF, class SIDE>
__device__ __forceinline__ void gemm(Ctx& c, v4u (&a)[32], f32x16& c0, f32x16& c1, int lb0, int lb1, int nb0, int nb1, SIDE side) {
  gemm4<NF, TPS, MODE, NEXT, XA, ZERO, RELF, EARLY, CAP, F0, F1>(c, a, c0, c1, c0, c1, lb0, lb1, nb0, nb1, side);
}
__device__ __forceinline__ unsigned sum4(unsigned m, v4u v) { return ((m + v.x + v.y) + v.z + v.w); }
template <bool EX> __device__ __forceinline__ void ld_chk(Ctx& c, v2u& k, int sb) {
  if (EX) k = __builtin_amdgcn_raw_buffer_load_b64(c.rs, c.lane * 8 + 2 * KB, sb, 16);
  else k.x = __builtin_amdgcn_raw_buffer_load_b32(c.rs, c.lane * 4 + 2 * KB, sb, 16);
}
template <int STR, bool EX> __device__ __forceinline__ void ld_rec(Ctx& c, v4u (&a)[32], v2u (&k)[16], int lv, int sb, int p, int e) {
  const int q = STR * p;
  const int so = sb + (q < 32 && ROT ? (c.rot + ((q & 15) >> 1) >= 8 ? c.rot * 2 * REC - 16 * REC : c.rot * 2 * REC) : 0) + q * REC;
  if (e < 2) a[(2 * p + e) & 31] = ld_ws(c, lv + e * KB, so);
  else ld_chk<EX>(c, k[p & 15], so);
}
template <bool EX> __device__ __forceinline__ unsigned rec_bad(v4u (&a)[32], v2u (&k)[16], int p, unsigned tag) {
  return sum4(sum4(EX ? k[p & 15].x + k[p & 15].y : k[p & 15].x, a[(2 * p) & 31]), a[(2 * p + 1) & 31]) ^ tag;
}
template <int N, int STR, bool EX> __device__ __forceinline__ void settle(Ctx& c, unsigned bad, v4u (&a)[32], v2u (&k)[16], int lv, int sb, int p0, unsigned tag) {
  if (__builtin_expect(__any(bad != 0u), 0)) {
    if (CYC == 3) c.dbgt -= (unsigned)__builtin_amdgcn_s_memtime();
    unsigned sp = 0;
#pragma nounroll
    do {
      if (BACKOFF) __builtin_amdgcn_s_sleep(BACKOFF);
      MEMBAR();
      bad = 0;
#pragma unroll
      for (int i = 0; i < 3 * N; ++i) ld_rec<STR, EX>(c, a, k, lv, sb, p0 + i / 3, i % 3);
#pragma unroll
      for (int i = 0; i < N; ++i) bad |= rec_bad<EX>(a, k, p0 + i, tag);
    } while (__any(bad != 0u) && ++sp < SPIN_CAP);
    if (CYC == 3) c.dbgt += (unsigned)__builtin_amdgcn_s_memtime();
  }
}
template <int F, int NP, int STR, bool EX>
__device__ __forceinline__ void check_step(Ctx& c, unsigned& m0, unsigned& m1, v4u (&a)[32], v2u (&k)[16], int lv, int sb, unsigned tag) {
  constexpr int p = 2 * ((F + 8) / 8), r = (F + 8) % 8;
  if constexpr (p < NP) {
    if constexpr (r == 2) { m0 = sum4(EX ? k[p & 15].x + k[p & 15].y : k[p & 15].x, a[(2 * p) & 31]); PIN(m0); }
    if constexpr (r == 3) { m0 = sum4(m0, a[(2 * p + 1) & 31]); PIN(m0); }
    if constexpr (r == 4) { m1 = sum4(EX ? k[(p + 1) & 15].x + k[(p + 1) & 15].y : k[(p + 1) & 15].x, a[(2 * p + 2) & 31]); PIN(m1); }
    if constexpr (r == 5) { m1 = sum4(m1, a[(2 * p + 3) & 31]); PIN(m1); }
    if constexpr (r == 6) { m0 = (m0 ^ tag) | (m1 ^ tag); PIN(m0); }
    if constexpr (r == 7) settle<2, STR, EX>(c, m0, a, k, lv, sb, p, tag);
  }
}
template <int STR, bool EX, int XD> __device__ __forceinline__ void open_phase(Ctx& c, v4u (&a)[32], v2u (&k)[16], int lv, int sb, unsigned tag) {
  VMWAIT(0);
  if (XD) __builtin_amdgcn_s_sleep(XD);
#pragma unroll
  for (int i = 0; i < 3 * OPENR; ++i) ld_rec<STR, EX>(c, a, k, lv, sb, i / 3, i % 3);
  SB();
  settle<2, STR, EX>(c, rec_bad<EX>(a, k, 0, tag) | rec_bad<EX>(a, k, 1, tag), a, k, lv, sb, 0, tag);
}
template <int F, int P0, int P1, int STR, bool EX> __device__ __forceinline__ void req_step(Ctx& c, v4u (&a)[32], v2u (&k)[16], int lv, int sb) {
  if constexpr (F >= 0 && P0 + F / 2 < P1) {
    ld_rec<STR, EX>(c, a, k, lv, sb, P0 + F / 2, F & 1);
    if constexpr (F & 1) ld_rec<STR, EX>(c, a, k, lv, sb, P0 + F / 2, 2);
  }
}
template <int G, int PER, int P0, int P1, int STR, bool EX> __device__ __forceinline__ void req_pace(Ctx& c, v4u (&a)[32], v2u (&k)[16], int lv, int sb) {
  if constexpr (G >= 0 && P0 + G / PER < P1) {
    constexpr int r = G % PER, D = PER / 3;
    if constexpr (r % D == 0 && r / D < 3) ld_rec<STR, EX>(c, a, k, lv, sb, P0 + G / PER, r / D);
  }
}
template <bool EX> __device__ __forceinline__ void st_chk(Ctx& c, unsigned sum, unsigned ex, int sb, unsigned tag) {
  if (EX) __builtin_amdgcn_raw_buffer_store_b64(v2u{tag - sum - ex, ex}, c.rs, c.lane * 8 + 2 * KB, sb, 16);
  else __builtin_amdgcn_raw_buffer_store_b32(tag - sum, c.rs, c.lane * 4 + 2 * KB, sb, 16);
}
__device__ __forceinline__ u64 mix64(u64 z) {
  z += 0x9E3779B97F4A7C15ull;
  z = (z ^ (z >> 30)) * 0xBF58476D1CE4E5B9ull;
  z = (z ^ (z >> 27)) * 0x94D049BB133111EBull;
  return z ^ (z >> 31);
}
__device__ __forceinline__ float csum(v4u f, float s) { return dot2(f.w, ONES2, dot2(f.z, ONES2, dot2(f.y, ONES2, dot2(f.x, ONES2, s)))); }
__device__ __forceinline__ v4u cs_operand(float s) { unsigned hi, lo; split2(s, hi, lo); return v4u{hi | (hi << 16), lo, 0u, 0u}; }

__global__ __launch_bounds__(512, 2) void fused_fwd(const u16* __restrict__ x, const u16* __restrict__ wqkv, const u16* __restrict__ wo,
                                                    const u16* __restrict__ wg, const u16* __restrict__ wu, const u16* __restrict__ wd,
                                                    u16* __restrict__ out, char* __restrict__ ws) {
  __shared__ __attribute__((aligned(16))) char smem[L_END];
  u64 pc0;
  asm volatile("s_getpc_b64 %0" : "=s"(pc0));
  Ctx c;
  c.L = (LAS char*)smem;
  c.rs = __builtin_amdgcn_make_buffer_rsrc((void*)ws, 0, WS_END, 0x00020000);
  const int wave = __builtin_amdgcn_readfirstlane(threadIdx.x >> 6);
  const int b = blockIdx.x, xc = b & 7, j = b >> 3;
  const int cl = (xc >> 1) * 4 + (j >> 3), m = (xc & 1) * 8 + (j & 7);
  const int rh = m >> 3, cg = m & 7;
  const int g0 = gsplit(m), ng = gsplit(m + 1) - g0;
  c.lane = 0; c.wave = wave; c.seq = 0; c.known = 0; c.nst = 0; c.dbgt = 1; c.vb0 = 0; c.vb1 = 0; c.nb0 = 0; c.nb1 = 0; c.fa = 0; c.fpre = v2u{0u, 0u};
  const int rot = (2 * cl + m) & 7;
  c.rot = ROT ? rot : 0;
  LAS char* L = c.L;
  if (threadIdx.x < 32) *flg(c, threadIdx.x) = 0u;
  stamp0(c);
  asm volatile("s_waitcnt lgkmcnt(0)\n\ts_barrier" ::: "memory");
#define LANEVARS                                                                                                              \
  const int lane = fresh_lane(), h = lane >> 5, l31 = lane & 31, lv = lane * 16;                                               \
  const int trN = (8 * h + ((lane >> 2) & 3)) * 64 + ((lane >> 4) & 1) * 32 + (lane & 3) * 8;                                   \
  const int trS = (8 * h + ((lane >> 2) & 3)) * 64 + ((lane >> 4) & 1) * 32 + (((lane & 1) << 1) | ((lane >> 1) & 1)) * 8;       \
  (void)trN; (void)trS; (void)l31; (void)lv;
  const int pp = wave & 1;
  auto load_half0 = [&](int sl) INL {
    const int lane = fresh_lane();
    const unsigned vo1 = (lane >> 2) * 3072 + (lane & 3) * 16;
    const char* wb = (const char*)wqkv + m * 64 + (sl < 4 ? (1 + pp) * 1024 : 0);
#pragma unroll
    for (int p = 0; p < 8; ++p) {
      const int s = sl < 4 ? sl * 8 + p : (sl - 4) * 16 + 2 * p + pp;
      glds(wb + (size_t)(((s + 4 * rot) & 31) * 16) * 3072, vo1, L_RING + sl * SLOT + p * 2048 + pp * 1024);
    }
  };
  auto cache_load = [&](auto& wc, auto NK, auto K0, auto K1) INL {
    const int lane = fresh_lane();
    const __amdgpu_buffer_rsrc_t rd = __builtin_amdgcn_make_buffer_rsrc((void*)wd, 0, 1376 * 1024, 0x00020000);
    const int vo2 = (lane >> 3) * 1024 + (((lane & 7) ^ (((lane >> 4) & 1) << 2)) * 16);
#pragma unroll
    for (int k = K0.v; k < K1.v; ++k) {
#pragma unroll
      for (int q = 0; q < 2; ++q) {
        const int hs = (NK.v == 5 ? k : k + 5) * 2 + pp;
        const int p = min((hs < 16 ? (hs & 8) | ((hs + c.rot) & 7) : hs) * 2 + q, 42);
        const int m3 = min(p - 32 + 1 + ((p - 32) >> 1), 15);
        const int aa = p < 32 ? gsplit(p & 15) + (p >> 4) : gsplit(m3) + 2;
#pragma unroll
        for (int e = 0; e < 4; ++e) wc[k * 8 + q * 4 + e] = __builtin_amdgcn_raw_buffer_load_b128(rd, vo2, aa * 32768 + cg * 128 + e * 8192, 0);
        SB();
        if (WPACE && NK.v == 5) __builtin_amdgcn_s_sleep(WPACE);
      }
    }
  };
  auto cache_put = [&](auto& wc, auto NK, auto FIRST, int base) INL {
    const int lane = fresh_lane(), h = lane >> 5;
    const int tk = (8 * h + ((lane >> 2) & 3)) * 128 + ((lane >> 4) & 1) * 32 + (((lane & 1) << 1) | ((lane >> 1) & 1)) * 8 + ((lane >> 3) & 1) * 64;
#pragma unroll
    for (int k = 0; k < NK.v; ++k) {
      const int sq = base + (NK.v == 5 ? k : k + 5);
      wait_lds<4, LSLP>(c, F_FREE, (unsigned)(sq - NSLOT + 1));
      LAS char* d = L + L_RING + (sq & (NSLOT - 1)) * SLOT + pp * 8192;
      if (FIRST.v && k == 0) {
#pragma unroll
        for (int kk = 0; kk < NK.v; ++kk) {
#pragma unroll
          for (int e = 0; e < 8; ++e) *(LAS v4u*)(d + lane * 16 + e * KB) = wc[kk * 8 + e];
          MEMBAR();
          SB();
#pragma unroll
          for (int e = 0; e < 8; ++e) wc[kk * 8 + e] = tr_frag<512>(d + (e >> 1) * 2048 + (tk ^ ((e & 1) * 64)));
          MEMBAR();
          SB();
        }
      }
#pragma unroll
      for (int e = 0; e < 8; ++e) {
        *(LAS v4u*)(d + lane * 16 + e * KB) = wc[k * 8 + e];
        if (PUTP) { MEMBAR(); asm volatile("s_nop %0" ::"n"(PUTP)); }
      }
      wait_lds<1, 2>(c, F_FULL + pp, (unsigned)sq);
      *flg(c, F_FULL + pp) = (unsigned)(sq + 1);
      MEMBAR();
    }
  };
  auto hpos = [&](int gi) INL { return gi * 16 + (gi < 2 ? m : g0 - 2 * m); };

  if (wave < 4) {
    __builtin_amdgcn_s_setprio(2);
    const int w = wave, mh = w >> 1, kh = w & 1;
    c.fa = opqv(L_FLG + (F_FREE + wave) * 4);
    v4u a[32], xr0, xr1;
    v2u k[16];
    f32x16 A0, A1, B0, B1;
    LANEVARS
    c.lane = lane;
    float rstd;
    unsigned tagx, tagy, tagc;
    v4u mfrag;
    auto set_stats = [&](float sum, float ssq) INL -> float {
      const float s = sum * (1.f / 512), q = ssq * (1.f / 512);
      unsigned hi, lo;
      split2(-s, hi, lo);
      mfrag = h == 0 ? v4u{hi | (lo << 16), hi, 0u, 0u} : v4u{0u, 0u, 0u, 0u};
      return fmaxf(q - s * s, 0.f) + 1e-5f;
    };
    auto cs_frag = [&](int tile) INL { return *(LAS v4u*)(L + L_CS + tile * 512 + l31 * 16); };
    auto rs_put = [&]() INL { const int lane = fresh_lane(); *(LAS float*)(L + L_KVS + w * 2304 + lane * 4) = h ? rstd * LOG2E : rstd; MEMBAR(); };
    auto rs_get = [&](int q) INL {
      const int h = fresh_lane() >> 5;
      const f32x4 r = *(LAS f32x4*)(L + L_KVS + w * 2304 + (8 * q + 4 * h) * 4), rl = *(LAS f32x4*)(L + L_KVS + w * 2304 + 128 + (8 * q + 4 * h) * 4);
#pragma unroll
      for (int i = 0; i < 4; ++i) { B0[4 * q + i] = r[i]; B1[4 * q + i] = rl[i]; }
    };
    {
      const __amdgpu_buffer_rsrc_t rx = __builtin_amdgcn_make_buffer_rsrc((void*)x, 0, 2048 * 1024, 0x00020000);
      const int xv = (lane >> 3) * 1024 + (lane & 7) * 16, xs = (cl * 128 + w * 32) * 1024;
      auto ldx = [&](int i) INL { a[i] = __builtin_amdgcn_raw_buffer_load_b128(rx, xv, xs + (i & 3) * 8192 + (((i >> 2) + rot) & 7) * 128, 0); };
      __builtin_amdgcn_s_setprio(3);
#pragma unroll
      for (int i = 0; i < XW0; ++i) ldx(i);
      __builtin_amdgcn_s_setprio(2);
#pragma unroll
      for (int i = XW0; i < 16; ++i) ldx(i);
      SB();
      stamp<3>(c);
      LAS char* sc = L + L_XCH + w * 4096;
      const int r8 = lane >> 3;
      const int wo_ = r8 * 128 + (((lane & 7) ^ (r8 >> 1)) * 16);
      const int ro = l31 * 128 + ((h ^ ((l31 >> 1) & 7)) * 16);
      auto xstep = [&](int kp, int e) INL {
        MEMBAR();
        if (e < 4) *(LAS v4u*)(sc + ((wo_ + e * 1024) ^ ((e & 1) * 64))) = a[kp * 4 + e];
        else a[kp * 4 + e - 4] = *(LAS v4u*)(sc + (ro ^ ((e - 4) * 32)));
        MEMBAR();
      };
#pragma unroll
      for (int kp = 0; kp < XPRE; ++kp) {
#pragma unroll
        for (int e = 0; e < 8; ++e) xstep(kp, e);
        if (kp < 2) {
#pragma unroll
          for (int i = 16 + 8 * kp; i < 24 + 8 * kp; ++i) ldx(i);
        }
      }
      stamp<3>(c);
      wpre<2>(c, trN, trS);
      stamp<3>(c);
      v4u s0;
      const int s0b = WS_S0P + (cl * 128 + w * 32) * 16;
      gemm<64, 2, 2, -1, true, 3, 0, 0, 99>(c, a, A0, A1, trN, trS, 0, 0, [&](auto F) INL {
        constexpr int f = F.v;
        if ((f >> 3) + XPRE < 8) xstep((f >> 3) + XPRE, f & 7);
        if (f == S0AT) s0 = ld_ws(c, l31 * 16, s0b);
        if (f == 52) {
          wait_lds<2, 0>(c, F_CS1R, 1u);
          const v2u tg = *(volatile LAS v2u*)flg(c, F_TAG);
          tagx = __builtin_amdgcn_readfirstlane(tg.x); tagy = __builtin_amdgcn_readfirstlane(tg.y); tagc = (tagx * 0x9E3779B1u + tagy) | 1u;
          if (__any(s0.y != tagx || s0.w != tagy)) {
            unsigned sp = 0;
#pragma nounroll
            do { MEMBAR(); s0 = ld_ws(c, l31 * 16, s0b); } while (__any(s0.y != tagx || s0.w != tagy) && ++sp < SPIN_CAP);
          }
          rstd = __builtin_amdgcn_rsqf(set_stats(u2f(s0.x), u2f(s0.z)));
          rs_put();
          xr0 = cs_frag(0); xr1 = cs_frag(1);
        }
        if (f == 57) { A0 = mfma(mfrag, xr0, A0); A1 = mfma(mfrag, xr1, A1); }
        if (f >= 58 && f < 62) rs_get(f - 58);
        if ((f & 15) == 15) stamp<3>(c);
      });
      const int xq = l31 * 1024 + h * 16, xqs = (cl * 128 + rh * 64 + mh * 32) * 1024 + cg * 128 + kh * 64;
      xr0 = __builtin_amdgcn_raw_buffer_load_b128(rx, xq, xqs, 0);
      xr1 = __builtin_amdgcn_raw_buffer_load_b128(rx, xq + 32, xqs, 0);
    }
#pragma unroll 1
    for (int layer = 0; layer < 2; ++layer) {
      const int lb = REUSE ? 0 : layer;
      stamp<1>(c);
      bool publisher;
      {
        LANEVARS
#pragma unroll
        for (int i = 0; i < 16; ++i) B1[i] *= A0[i];
        SB();
#pragma unroll
        for (int i = 0; i < 16; ++i) { B1[i] = exp2c(B1[i]); A0[i] = fmaf(A0[i], B0[i], 1.f); A1[i] *= B0[i]; }
        SB();
        v4u pk0, pk1, vv0, vv1;
#pragma unroll
        for (int i = 0; i < 16; ++i) A0[i] = fmaxf(A0[i], B1[i]) + 1e-6f;
#pragma unroll
        for (int i = 0; i < 4; ++i) {
          pk0[i] = pack2(A0[2 * i], A0[2 * i + 1]); vv0[i] = pack2(A1[2 * i], A1[2 * i + 1]);
          pk1[i] = pack2(A0[8 + 2 * i], A0[9 + 2 * i]); vv1[i] = pack2(A1[8 + 2 * i], A1[9 + 2 * i]);
        }
        f32x16 kv = mfma(pk0, vv0, zero16());
        kv = mfma(pk1, vv1, kv);
        const float z = red_h(csum(pk0, 0.f) + csum(pk1, 0.f));
        LAS char* ks = L + L_KVS + w * 2304;
        if (h == 0) *(LAS float*)(ks + 2048 + l31 * 4) = z;
        if (lane & 16) {
          *(LAS f32x4*)(ks + lv) = f32x4{kv[8], kv[9], kv[10], kv[11]};
          *(LAS f32x4*)(ks + 1024 + lv) = f32x4{kv[12], kv[13], kv[14], kv[15]};
          MEMBAR();
        } else {
          MEMBAR();
          *(LAS f32x4*)(ks + lv) = f32x4{kv[0], kv[1], kv[2], kv[3]};
          *(LAS f32x4*)(ks + 1024 + lv) = f32x4{kv[4], kv[5], kv[6], kv[7]};
        }
        MEMBAR();
        unsigned old = 0;
        if (lane == 0) old = __hip_atomic_fetch_add((LAS unsigned*)flg(c, F_KFL), 1u, __ATOMIC_RELAXED, __HIP_MEMORY_SCOPE_WORKGROUP);
        publisher = (__builtin_amdgcn_readfirstlane(old) & 3u) == 3u;
        if (publisher) {
          const int lane = fresh_lane();
          MEMBAR();
          f32x4 k0 = {0.f, 0.f, 0.f, 0.f}, k1 = k0;
          f32x2 zz = {0.f, 0.f};
#pragma unroll
          for (int ww = 0; ww < 4; ++ww) {
            k0 += *(LAS f32x4*)(L + L_KVS + ww * 2304 + lv);
            k1 += *(LAS f32x4*)(L + L_KVS + ww * 2304 + 1024 + lv);
            zz += *(LAS f32x2*)(L + L_KVS + ww * 2304 + 2048 + (lane & 15) * 8);
          }
          const unsigned tg = layer ? tagy : tagx;
          const unsigned tl = tg >> ((lane & 1) * 16), t0 = tl & 0xffu, t1 = (tl >> 8) & 0xffu;
          const int kvb = WS_KVP + ((layer * 16 + m) * 16 + cl) * KVR;
          auto st2 = [&](float u, float v, int vo) INL {
            __builtin_amdgcn_raw_buffer_store_b64(v2u{(f2u(u) & 0xffffff00u) | t0, (f2u(v) & 0xffffff00u) | t1}, c.rs, vo, kvb, 16);
          };
          st2(k0[0], k0[1], lane * 8); st2(k0[2], k0[3], lane * 8 + 512); st2(k1[0], k1[1], lane * 8 + 1024); st2(k1[2], k1[3], lane * 8 + 1536);
          if (lane < 16) st2(zz[0], zz[1], lane * 8 + 2048);
        }
        MEMBAR();
      }
      stamp<1>(c);
      B0 = mfma(cs_frag(2), mfrag, zero16());
      if (layer == 0) { LANEVARS wpre<3>(c, trN, trN); gemm<32, 1, 3, -1, false, 0, 0, 0, QUIET ? 0 : 99>(c, a, B0, B0, trN, trN, 0, 0, NoSide()); }
      else { wpre<0>(c, lv, lv); gemm<32, 1, 0, -1, false, 0, 0, 0, QUIET ? 0 : 99>(c, a, B0, B0, lv, lv, 0, 0, NoSide()); }
      const int lq = fresh_lane();
      const int kb0 = (8 * (lq >> 5) + ((lq >> 2) & 3)) * 128 + ((lq >> 4) & 1) * 32 + (((lq & 1) << 1) | ((lq >> 1) & 1)) * 8 + kh * 4096 + ((kh ^ (lq >> 3)) & 1) * 64;
      const int kb1 = kb0 ^ 64;
      auto res_init = [&](f32x16& R, int i) INL { const unsigned u = i < 4 ? xr0[i & 3] : xr1[i & 3]; R[2 * i] = blo(u); R[2 * i + 1] = bhi(u); };
      {
        LANEVARS
        v4u pq0, pq1;
        const float rl = rstd * LOG2E;
#pragma unroll
        for (int i = 0; i < 16; ++i) B1[i] = exp2c(B0[i] * rl);
#pragma unroll
        for (int i = 0; i < 16; ++i) B0[i] = fmaxf(fmaf(B0[i], rstd, 1.f), B1[i]) + 1e-6f;
#pragma unroll
        for (int i = 0; i < 4; ++i) { pq0[i] = pack2(B0[2 * i], B0[2 * i + 1]); pq1[i] = pack2(B0[8 + 2 * i], B0[9 + 2 * i]); }
#pragma unroll
        for (int i = 0; i < 8; ++i) res_init(A0, i);
        PIN(pq0); PIN(pq1); PIN(A0);
        const int ob = opq(WS_O + cl * ACT_CL + ((lb * 4 + w) * 16 + m) * REC);
        stamp<1>(c);
        {
          const int lane = fresh_lane();
          const unsigned tg = layer ? tagy : tagx;
          const unsigned tl = tg >> ((lane & 1) * 16), t0 = tl & 0xffu, t1 = (tl >> 8) & 0xffu;
          const int kvb = WS_KVP + (layer * 16 + m) * 16 * KVR;
          float s0 = 0.f, s1 = 0.f;
          v2u zp;
          if (publisher) __builtin_amdgcn_s_sleep(KVDP); else __builtin_amdgcn_s_sleep(KVDELAY);
#pragma nounroll
          for (unsigned sp = 0; sp < SPIN_CAP; ++sp) {
            v2u p[16];
            zp = __builtin_amdgcn_raw_buffer_load_b64(c.rs, (lane >> 2) * KVR + (lane & 3) * 8 + 2048, kvb + w * 32, 16);
#pragma unroll
            for (int i = 0; i < 16; ++i) p[i] = __builtin_amdgcn_raw_buffer_load_b64(c.rs, lane * 8, kvb + w * 512 + i * KVR, 16);
            SB();
            zp.x ^= t0; zp.y ^= t1;
            unsigned chk = zp.x | zp.y;
#pragma unroll
            for (int i = 0; i < 16; ++i) {
              p[i].x ^= t0; p[i].y ^= t1; chk |= p[i].x | p[i].y;
              s0 = i ? s0 + u2f(p[i].x) : u2f(p[i].x); s1 = i ? s1 + u2f(p[i].y) : u2f(p[i].y);
              PIN(s0); PIN(s1);
            }
            if (!__any((chk & 0xffu) != 0u)) break;
            if (KVBACK) __builtin_amdgcn_s_sleep(KVBACK);
            MEMBAR();
          }
          *(LAS unsigned*)(L + L_KVF + lv + w * 4) = pack2(s0, s1);
          float za = u2f(zp.x), zb = u2f(zp.y);
          za += dpp<0x128>(za); zb += dpp<0x128>(zb); za += dpp<0x124>(za); zb += dpp<0x124>(zb);
          za = red_g(za); zb = red_g(zb);
          if (lane < 4) *(LAS unsigned*)(L + L_ZF + w * 16 + lane * 4) = pack2(za, zb);
          MEMBAR();
          *flg(c, F_KVR + w) = (unsigned)(layer + 1);
        }
        if (WARM0 && layer == 0) wait_lds<2, 0>(c, F_JOB, 1u);
        v4u kvf;
        v2u z0, z1, z2, z3;
#pragma nounroll
        for (unsigned sp = 0; sp < SPIN_CAP; ++sp) {
          const v4u fl = *(volatile LAS v4u*)flg(c, F_KVR);
          MEMBAR();
          kvf = *(LAS v4u*)(L + L_KVF + lv);
          z0 = *(LAS v2u*)(L + L_ZF + h * 8); z1 = *(LAS v2u*)(L + L_ZF + 16 + h * 8);
          z2 = *(LAS v2u*)(L + L_ZF + 32 + h * 8); z3 = *(LAS v2u*)(L + L_ZF + 48 + h * 8);
          MEMBAR();
          if ((unsigned)__builtin_amdgcn_readfirstlane(umin(umin(fl.x, fl.y), umin(fl.z, fl.w))) > (unsigned)layer) break;
        }
        stamp<1>(c);
        const bool up = (lane & 16) != 0;
        const v4u zz = {0u, 0u, 0u, 0u};
        B0 = mfma(up ? zz : kvf, pq0, zero16());
        B0 = mfma(up ? kvf : zz, pq1, B0);
        const float d0 = dot2(pq0.y, z0.y, dot2(pq0.x, z0.x, 0.f)) + dot2(pq0.w, z1.y, dot2(pq0.z, z1.x, 0.f));
        const float d1 = dot2(pq1.y, z2.y, dot2(pq1.x, z2.x, 0.f)) + dot2(pq1.w, z3.y, dot2(pq1.z, z3.x, 0.f));
        const float r0 = __builtin_amdgcn_rcpf(red_h(d0) + 1e-6f), r1 = __builtin_amdgcn_rcpf(red_h(d1) + 1e-6f);
        v4u o0, o1;
#pragma unroll
        for (int i = 0; i < 4; ++i) { o0[i] = pack2(B0[2 * i] * r0, B0[2 * i + 1] * r0); o1[i] = pack2(B0[8 + 2 * i] * r1, B0[9 + 2 * i] * r1); }
        st_ws(c, o0, lv, ob);
        st_ws(c, o1, lv + KB, ob);
        st_chk<false>(c, sum4(sum4(0u, o0), o1), 0u, ob, tagc);
      }
      stamp<1>(c);
      const int rtA = opq(rh * 2 + mh), clb = opq(cl * ACT_CL);
      auto res_epilogue = [&](f32x16& A0, f32x16& A1, int xi, bool final_out, unsigned xseq) INL {
        LAS char* xs = L + L_XCH;
        const int lv = fresh_lane() * 16;
#pragma unroll
        for (int q = 0; q < 4; ++q) *(LAS f32x4*)(xs + w * 4096 + q * 1024 + lv) = f32x4{A1[4 * q], A1[4 * q + 1], A1[4 * q + 2], A1[4 * q + 3]};
        MEMBAR();
        *flg(c, F_XF + w) = xseq;
        f32x4 o0, o1, o2, o3;
#pragma nounroll
        for (unsigned sp = 0; sp < SPIN_CAP; ++sp) {
          const unsigned fl = *flg(c, F_XF + (w ^ 1));
          MEMBAR();
          o0 = *(LAS f32x4*)(xs + (w ^ 1) * 4096 + lv); o1 = *(LAS f32x4*)(xs + (w ^ 1) * 4096 + 1024 + lv);
          o2 = *(LAS f32x4*)(xs + (w ^ 1) * 4096 + 2048 + lv); o3 = *(LAS f32x4*)(xs + (w ^ 1) * 4096 + 3072 + lv);
          MEMBAR();
          if ((unsigned)__builtin_amdgcn_readfirstlane(fl) >= xseq) break;
        }
        xr0 = v4u{pack2(A0[0] + o0[0], A0[1] + o0[1]), pack2(A0[2] + o0[2], A0[3] + o0[3]), pack2(A0[4] + o1[0], A0[5] + o1[1]), pack2(A0[6] + o1[2], A0[7] + o1[3])};
        xr1 = v4u{pack2(A0[8] + o2[0], A0[9] + o2[1]), pack2(A0[10] + o2[2], A0[11] + o2[3]), pack2(A0[12] + o3[0], A0[13] + o3[1]), pack2(A0[14] + o3[2], A0[15] + o3[3])};
        if (final_out) {
          const int lo = fresh_lane();
          GAS char* ob = (GAS char*)out + (size_t)((cl * 4 + rtA) * 32 + (lo & 31)) * 1024 + cg * 128 + kh * 64 + (lo >> 5) * 16;
          *(GAS v4u*)ob = xr0;
          *(GAS v4u*)(ob + 32) = xr1;
        } else {
          const int xb = WS_X + clb + (((REUSE ? xi >> 1 : xi) * 4 + rtA) * 16 + cg * 2 + kh) * REC;
          st_ws(c, xr0, lv, xb);
          st_ws(c, xr1, lv + KB, xb);
          const float s = csum(xr0, 0.f) + csum(xr1, 0.f);
          float q0 = 0.f, q1 = 0.f;
#pragma unroll
          for (int i = 0; i < 4; ++i) { q0 = dot2(xr0[i], xr0[i], q0); q1 = dot2(xr1[i], xr1[i], q1); }
          const auto sw = __builtin_amdgcn_permlane32_swap(f2u(s), f2u(q0 + q1), false, false);
          st_chk<true>(c, sum4(sum4(0u, xr0), xr1), f2u(u2f((unsigned)sw[0]) + u2f((unsigned)sw[1])), xb, tagc);
        }
      };
      unsigned m0, m1;
      {
        const int ab = WS_O + clb + ((lb * 4 + rtA) * 16 + kh) * REC;
        wpre<1>(c, kb0, kb1);
        open_phase<2, false, XD0>(c, a, k, lv, ab, tagc);
        stamp<1>(c);
        gemm<32, 2, 1, -1, false, 2, 8, 0, QUIET ? 0 : 99>(c, a, A0, A1, kb0, kb1, 0, 0, [&](auto F) INL {
          req_step<F.v, OPENR, 8, 2, false>(c, a, k, lv, ab);
          check_step<F.v, 8, 2, false>(c, m0, m1, a, k, lv, ab, tagc);
        });
        stamp<1>(c);
        res_epilogue(A0, A1, layer * 2, false, (unsigned)(layer * 2 + 1));
      }
      wpre<0>(c, lv, lv);
      stamp<1>(c);
      const int rtB = w;
      v4u cs0;
      float sacc;
      auto ln_side = [&](auto F, int xb, float& var) INL -> bool {
        constexpr int f = F.v;
        if constexpr (f == 0) { sacc = u2f(k[0].y) + u2f(k[1].y); PIN(sacc); }
        if constexpr (PACE) {
          req_step<f, OPENR, 10, 1, true>(c, a, k, lv, xb);
          if constexpr (f >= 12 && f < 36 && (f & 3) < 3) ld_rec<1, true>(c, a, k, lv, xb, 10 + (f - 12) / 4, f & 3);
        } else req_step<f, OPENR, 16, 1, true>(c, a, k, lv, xb);
        check_step<f, 16, 1, true>(c, m0, m1, a, k, lv, xb, tagc);
        if constexpr ((f & 7) == 0 && f >= 8 && f <= 56) { sacc += u2f(k[(f / 4) & 15].y) + u2f(k[(f / 4 + 1) & 15].y); PIN(sacc); }
        if constexpr (f == 57) {
          const auto sw = __builtin_amdgcn_permlane32_swap(f2u(sacc), f2u(sacc), false, false);
          var = set_stats(u2f((unsigned)sw[0]), u2f((unsigned)sw[1]));
          PIN(mfrag);
          return true;
        }
        return false;
      };
      const int ab4 = WS_H + clb + ((lb * 4 + rtA) * 43 + kh) * REC;
      const int hb = WS_H + clb + (lb * 4 + rtB) * 43 * REC;
      float r2;
      v4u ho;
      unsigned hs;
      float h0, h1;
      auto hside = [&](auto F, auto VIA, f32x16& G, f32x16& U, int gi) INL {
        constexpr int f = F.v, sl = (f - 1) / 2;
        if constexpr (f >= 1 && f < 17) {
          if constexpr (f & 1) { h0 = relu(G[2 * sl]) * r2; PIN(h0); h1 = relu(G[2 * sl + 1]) * r2; PIN(h1); }
          else {
            h0 *= U[2 * sl]; PIN(h0); h1 *= U[2 * sl + 1]; PIN(h1);
            ho[sl & 3] = pack2(h0, h1); PIN(ho);
          }
        }
        if constexpr (VIA.v) {
          if constexpr (f == 9 || f == 17) *(LAS v4u*)(L + L_XCH + (w ^ 1) * 4096 + (f == 17) * KB + lv) = ho;
          if constexpr (f == 17) { MEMBAR(); *flg(c, F_HF + w) = (unsigned)(layer + 1); MEMBAR(); }
        } else {
          if constexpr (f == 9 || f == 17) { st_ws(c, ho, lv + (f == 17) * KB, hb + hpos(gi) * REC); hs = sum4(f == 9 ? 0u : hs, ho); PIN(hs); }
          if constexpr (f == 18) st_chk<false>(c, hs, 0u, hb + hpos(gi) * REC, tagc);
        }
      };
      auto corr = [&](auto F, int at, f32x16& G, f32x16& U, int gi) INL {
        if (F.v == at - 6) cs0 = cs_frag(3 + 2 * gi);
        if (F.v == at - 3) { G = mfma(cs0, mfrag, G); SB(); cs0 = cs_frag(4 + 2 * gi); }
        if (F.v == at) U = mfma(cs0, mfrag, U);
      };
      const int xb3 = WS_X + clb + ((REUSE ? layer : layer * 2) * 4 + rtB) * 16 * REC;
      open_phase<1, true, XD1>(c, a, k, lv, xb3, tagc);
      stamp<1>(c); stamp<2>(c);
      gemm4<128, 2, 0, 0, false, 3, RELD, 0, 99, 0, 128>(c, a, A0, A1, B0, B1, lv, lv, lv, lv, [&](auto F) INL {
        constexpr int f = F.v, u = f / 8;
        if constexpr (f == 0) { sacc = u2f(k[0].y) + u2f(k[1].y); PIN(sacc); }
        req_pace<f, DPER, OPENR, 16, 1, true>(c, a, k, lv, xb3);
        sfor<8>([&](auto CH) INL {
          if constexpr (CH.v > 0 && dual_first(CH.v) == u + 1) check_step<8 * (CH.v - 1) + f % 8, 16, 1, true>(c, m0, m1, a, k, lv, xb3, tagc);
          if constexpr (CH.v > 0 && f == 8 * dual_first(CH.v)) { sacc += u2f(k[2 * CH.v].y) + u2f(k[2 * CH.v + 1].y); PIN(sacc); }
        });
        if constexpr (f == 89) {
          const auto sw = __builtin_amdgcn_permlane32_swap(f2u(sacc), f2u(sacc), false, false);
          r2 = __builtin_amdgcn_rcpf(set_stats(u2f((unsigned)sw[0]), u2f((unsigned)sw[1])));
          PIN(mfrag); PIN(r2);
        }
        if (f == 86 && layer == 0) wait_lds<1, 0>(c, F_CS3R, 1u);
        corr(F, 95, A0, A1, 0);
        if constexpr (f >= 96) hside(IC<f - 96>{}, IC<0>{}, A0, A1, 0);
        corr(F, 123, B0, B1, 1);
      });
      stamp<2>(c);
      const int tb0 = fresh_lane() * 16 + kh * 5120, tb1 = tb0 ^ 1024;
      constexpr int NPRE = (64 - S0F) / HPER;
      static_assert(NPRE >= 8 && NPRE <= 16, "pace");
      if (ng == 3) {
        gemm<64, 2, 0, 4, false, 3, 0, 0, 99>(c, a, A0, A1, lv, lv, tb0, tb1, [&](auto F) INL {
          constexpr int f = F.v;
          hside(F, IC<0>{}, B0, B1, 1);
          corr(F, 47, A0, A1, 2);
          if constexpr ((f & 1) && f >= 21 && f < 37) { res_init(B0, (f - 21) >> 1); PIN(B0); }
          req_pace<f - S0F, HPER, 0, NPRE, 2, false>(c, a, k, lv, ab4);
          if constexpr (f >= 56) check_step<f - 64, 22, 2, false>(c, m0, m1, a, k, lv, ab4, tagc);
        });
      }
      if (opq(ng) == 2) {
        const unsigned tg0 = (unsigned)__builtin_amdgcn_s_memrealtime();
        sfor<20>([&](auto F) INL { hside(F, IC<0>{}, B0, B1, 1); });
#pragma unroll
        for (int i = 0; i < 8; ++i) res_init(B0, i);
        while ((unsigned)__builtin_amdgcn_s_memrealtime() - tg0 < NG2S) __builtin_amdgcn_s_sleep(2);
        sfor<2 * NPRE>([&](auto F) INL { req_step<F.v, 0, NPRE, 2, false>(c, a, k, lv, ab4); });
        wpre<4>(c, tb0, tb1);
        settle<2, 2, false>(c, rec_bad<false>(a, k, 0, tagc) | rec_bad<false>(a, k, 1, tagc), a, k, lv, ab4, 0, tagc);
        while ((unsigned)__builtin_amdgcn_s_memrealtime() - tg0 < NG2T) __builtin_amdgcn_s_sleep(2);
      }
      stamp<1>(c); stamp<2>(c); stamp<4>(c); stamp<6>(c);
      {
        auto side4 = [&](auto F) INL {
          constexpr int f = F.v;
          if constexpr (f < 10) { hside(IC<2 * f>{}, IC<0>{}, A0, A1, 2 * ng - 4); hside(IC<2 * f + 1>{}, IC<0>{}, A0, A1, 2 * ng - 4); }
          req_pace<f, HPER, NPRE, 16, 2, false>(c, a, k, lv, ab4);
          req_pace<f - S2H, H2PER, 16, 22, 2, false>(c, a, k, lv, ab4);
          if constexpr (f < 72) check_step<f, 22, 2, false>(c, m0, m1, a, k, lv, ab4, tagc);
          if constexpr (f == 76) settle<1, 2, false>(c, rec_bad<false>(a, k, 20, tagc), a, k, lv, ab4, 20, tagc);
          if constexpr (f == 80) { if (kh == 0) settle<1, 2, false>(c, rec_bad<false>(a, k, 21, tagc), a, k, lv, ab4, 21, tagc); }
          if constexpr (f == 73 && HVIA) wait_lds<1, 0>(c, F_HACK + mh, (unsigned)(layer + 1));
          if ((f & 7) == 7) stamp<4>(c);
          if (f == 15 || f == 39 || f == 55 || f == 65 || f == 83) stamp<6>(c);
        };
        gemm<88, 2, 4, -1, false, 2, 32, 3, P4CAP, 0, 84>(c, a, B0, B1, tb0, tb1, 0, 0, side4);
        if (kh == 0) gemm<88, 2, 4, -1, false, 2, 32, 3, P4CAP, 84, 88>(c, a, B0, B1, tb0, tb1, 0, 0, NoSide());
        stamp<1>(c); stamp<2>(c);
        tagc += 0x632BE5AAu;
        res_epilogue(B0, B1, 1, layer == 1, (unsigned)(layer * 2 + 2));
      }
      if (layer == 1) break;
      wpre<0>(c, lv, lv);
      stamp<1>(c);
      const int xb1 = WS_X + clb + ((REUSE ? 0 : 4) + rtB) * 16 * REC;
      open_phase<1, true, XD2>(c, a, k, lv, xb1, tagc);
      stamp<1>(c);
      gemm<64, 2, 0, -1, true, 3, RELA, 0, 99>(c, a, A0, A1, lv, lv, 0, 0, [&](auto F) INL {
        constexpr int f = F.v;
        float var;
        if (ln_side(F, xb1, var)) { rstd = __builtin_amdgcn_rsqf(var); rs_put(); }
        if (f == 56) cs0 = cs_frag(0);
        if (f == 60) { A0 = mfma(mfrag, cs0, A0); SB(); cs0 = cs_frag(1); }
        if (f == 63) A1 = mfma(mfrag, cs0, A1);
        if (f >= 59 && f < 63) rs_get(f - 59);
      });
      tagc += 0x632BE5AAu;
    }
  } else if (wave < 6) {
    const int lw = wave - 4;
    LANEVARS
    c.lane = lane;
    volatile LAS unsigned* full = flg(c, F_FULL + lw);
    __builtin_amdgcn_s_setprio(3);
    load_half0(0);
    if (CODEPF && lw == 1) {
      glds((const char*)(pc0 & ~(u64)255) + j * (CODEPF / 32), min(lane * 32, CODEPF / 32 - 32), L_RING + 7 * SLOT + 12288);
    } else {
      asm volatile("" ::: "memory");
    }
    __builtin_amdgcn_s_setprio(2);
    load_half0(1);
    load_half0(2);
    VMWAIT(16); *full = 1u;
    stamp<3>(c);
    load_half0(3);
    VMWAIT(16); *full = 2u;
    if (TOUCH == 2) wait_lds<2, 1>(c, F_CS1R, 1u);
    {
      const unsigned tv = min(lane, 42) * 128, td = L_RING + (6 + lw) * SLOT + (lw ? 13312 : 3072);
      glds((const char*)(lw ? wu : wg) + b * 5504, tv, td);
      if (lw) glds((const char*)wd + b * 5504, tv, td);
      else glds((const char*)wo + b * 2048, (lane & 15) * 128, td);
    }
    *flg(c, F_QGO + lw) = 1u;
    VMWAIT(10); *full = 3u;
    VMWAIT(2); *full = 4u;
    __builtin_amdgcn_s_setprio(1);
    load_half0(4);
    stamp<3>(c);
    load_half0(5);
    VMWAIT(8); *full = 5u;
    VMWAIT(0); *full = 6u;
    stamp<1>(c); stamp<3>(c);
    int seq = 6, pub = 6;
    v4u wc[48];
    auto stream = [&](int seg, int i0, int i1) INL {
      const int ln = fresh_lane(), lv = ln * 16;
      const unsigned vo2 = (ln >> 3) * 1024 + (((ln & 7) ^ (((ln >> 4) & 1) << 2)) * 16);
      const int src = (seg == 0 ? WS_WT1 + m * 96 * KB : WS_WT3 + g0 * 64 * KB);
#pragma unroll 1
      for (int i = i0; i < i1; ++i) {
        if (seq >= NSLOT) {
          const unsigned need = (unsigned)(seq - NSLOT + 1);
          bool first = true;
#pragma nounroll
          for (unsigned sp = 0; sp < SPIN_CAP; ++sp) {
            v4u f = *(volatile LAS v4u*)flg(c, F_FREE);
            if ((unsigned)__builtin_amdgcn_readfirstlane(umin(umin(f.x, f.y), umin(f.z, f.w))) >= need) break;
            if (first && pub < seq) { VMWAIT(0); pub = seq; *full = (unsigned)pub; }
            first = false;
            __builtin_amdgcn_s_sleep(LSLP);
          }
          MEMBAR();
        }
        const unsigned dst = L_RING + (seq & (NSLOT - 1)) * SLOT + lw * 8192;
        const int hs = i * 2 + lw, hr = (seg == 2 && hs < 16) ? (int)((DUAL >> (4 * hs)) & 15) : hs;
        if (seg == 1) {
          const char* sp = (const char*)wo + (size_t)hr * 65536 + cg * 128;
#pragma unroll
          for (int e = 0; e < 8; ++e) glds(sp + e * 8192, vo2, dst + e * 1024);
        } else if (seg == 0 && i >= 4) {
          glds4(ws + src + 65536 + ((hs * 2 + c.rot) & 7) * 4096, lv, dst);
          glds4(ws + src + 65536 + ((hs * 2 + 1 + c.rot) & 7) * 4096, lv, dst + 4096);
        } else {
          glds4(ws + src + hr * 8192, lv, dst);
          glds4(ws + src + hr * 8192 + 4096, lv, dst + 4096);
        }
        ++seq;
        if (seq - pub > LDEP) {
          if (LDEP == 1) VMWAIT(8); else if (LDEP == 2) VMWAIT(16); else VMWAIT(24);
          pub = seq - LDEP; *full = (unsigned)pub;
        }
      }
    };
    auto drain = [&]() INL { VMWAIT(0); if (pub < seq) { pub = seq; *full = (unsigned)seq; } };
    wait_lds<2, 1>(c, F_JOB, 1u);
    stream(1, 0, 4);
    drain();
    stamp<1>(c); stamp<3>(c);
    if (lw == 0) { poll_tags<32>(c, WS_F1 + m * 2048, *(volatile LAS v2u*)flg(c, F_TAG)); *flg(c, F_PTOK) = 1u; }
    else wait_lds<1, 1>(c, F_PTOK, 1u);
    stamp<1>(c); stamp<3>(c);
    stream(2, 0, 8);
    wait_lds<4, 2>(c, F_FREE, 12u);
    drain();
    cache_load(wc, IC<6>{}, IC<0>{}, IC<3>{});
    stream(2, 8, 4 * ng);
    drain();
    stamp<1>(c);
    cache_load(wc, IC<6>{}, IC<3>{}, IC<6>{});
    cache_put(wc, IC<6>{}, IC<1>{}, seq);
    seq += 11; pub = seq;
    stamp<1>(c);
    stream(0, 0, 6);
    stream(1, 0, 4);
    stamp<1>(c);
    stream(2, 0, 4 * ng);
    drain();
    stamp<1>(c);
    cache_put(wc, IC<6>{}, IC<0>{}, seq);
  } else {
    const int hh = wave - 6;
    LANEVARS
    c.lane = lane;
    const int pts = L_RING + (6 + hh) * SLOT;
    __builtin_amdgcn_s_setprio(3);
    {
      const unsigned vo1 = (lane >> 2) * 3072 + (lane & 3) * 16;
      const char* j0 = (const char*)wqkv + (size_t)(cl * 32) * 3072 + m * 64;
      glds(j0 + (1 + hh) * 1024, vo1, pts); glds(j0 + (1 + hh) * 1024 + 16 * 3072, vo1, pts + 1024);
      glds(j0 + hh * 16 * 3072, vo1, pts + 2048);
    }
    const int srow = cl * 128 + m * 8 + hh * 4 + (lane >> 4);
    v4u xs[4];
    v2u e0;
    {
      const GAS char* xp = (const GAS char*)x + (size_t)srow * 1024 + (lane & 15) * 16;
      const int zo = opqv(0);
      asm volatile("global_load_dwordx4 %0, %5, off\n\tglobal_load_dwordx4 %1, %5, off offset:256\n\tglobal_load_dwordx4 %2, %5, off offset:512\n\t"
                   "global_load_dwordx4 %3, %5, off offset:768\n\tglobal_load_dwordx2 %4, %6, %7 sc1"
                   : "=&v"(xs[0]), "=&v"(xs[1]), "=&v"(xs[2]), "=&v"(xs[3]), "=&v"(e0) : "v"(xp), "v"(zo), "s"(ws + WS_E) : "memory");
    }
    asm volatile("s_waitcnt vmcnt(0)" : "+v"(xs[0]), "+v"(xs[1]), "+v"(xs[2]), "+v"(xs[3]), "+v"(e0)::"memory");
    const u64 tag = mix64(((u64)(unsigned)__builtin_amdgcn_readfirstlane(e0.y) << 32) | (unsigned)__builtin_amdgcn_readfirstlane(e0.x));
    const v2u tag2 = {(unsigned)tag, (unsigned)(tag >> 32)};
    if (hh == 0) *(volatile LAS v2u*)flg(c, F_TAG) = tag2;
    stamp<1>(c); stamp<3>(c);
    {
      const unsigned tb = ((unsigned)(mix64(tag) >> 32) >> ((lane & 3) * 8)) & 0xffu;
      const v4u f0 = tr_frag(L + pts + (hh ? trS : trN)), f1 = tr_frag(L + pts + 1024 + (hh ? trS : trN)), f2 = tr_frag(L + pts + 2048 + trN);
      const float s0 = red_h(csum(f1, csum(f0, 0.f))), s2 = red_h(csum(f2, 0.f));
      __builtin_amdgcn_raw_buffer_store_b32((f2u(h ? s2 : s0) & 0xffffff00u) | tb, c.rs, l31 * 4 + h * 4096, WS_C1P + m * 8 * KB + cl * 256 + hh * 128, 16);
      float sa = 0.f, sb = 0.f, qa = 0.f, qb = 0.f;
#pragma unroll
      for (int i = 0; i < 4; ++i) {
        sa = dot2(xs[i].y, ONES2, dot2(xs[i].x, ONES2, sa)); sb = dot2(xs[i].w, ONES2, dot2(xs[i].z, ONES2, sb));
        qa = dot2(xs[i].y, xs[i].y, dot2(xs[i].x, xs[i].x, qa)); qb = dot2(xs[i].w, xs[i].w, dot2(xs[i].z, xs[i].z, qb));
      }
      sa += sb; qa += qb;
      sa += dpp<0x128>(sa); qa += dpp<0x128>(qa); sa += dpp<0x124>(sa); qa += dpp<0x124>(qa);
      sa += dpp<0x122>(sa); qa += dpp<0x122>(qa); sa += dpp<0x121>(sa); qa += dpp<0x121>(qa);
      if ((lane & 15) == 0) st_ws(c, v4u{f2u(sa), tag2.x, f2u(qa), tag2.y}, srow * 16, WS_S0P);
      if (KVWARM) {
        const unsigned nt = ~(hh ? tag2.y : tag2.x);
        const v4u W = {nt, nt >> 8, nt >> 16, nt >> 24};
        const int kvb = WS_KVP + ((hh * 16 + m) * 16 + cl) * KVR;
        st_ws(c, W, lv, kvb); st_ws(c, W, lv + KB, kvb);
        if (lane < 8) st_ws(c, W, lv + 2 * KB, kvb);
      }
      VMWAIT(0);
      if (CSDELAY) __builtin_amdgcn_s_sleep(CSDELAY);
      stamp<3>(c);
      {
        float t;
#pragma nounroll
        for (unsigned sp = 0; sp < SPIN_CAP; ++sp) {
          unsigned p[16];
#pragma unroll
          for (int i = 0; i < 16; ++i) p[i] = __builtin_amdgcn_raw_buffer_load_b32(c.rs, lane * 4 + i * 256, WS_C1P + m * 8 * KB + hh * 4096, 16);
          SB();
          unsigned chk = 0;
          t = 0.f;
#pragma unroll
          for (int i = 0; i < 16; ++i) { p[i] ^= tb; chk |= p[i]; t += u2f(p[i]); }
          if (!__any((chk & 0xffu) != 0u)) break;
          MEMBAR();
        }
        if (hh) t = red_h(t);
        if (hh == 0 || h == 0) *(LAS v4u*)(L + L_CS + hh * 1024 + lv) = cs_operand(t);
        MEMBAR();
        *flg(c, F_CS1R + hh) = 1u;
      }
      stamp<3>(c);
      __builtin_amdgcn_s_setprio(1);
      wait_lds<2, 1>(c, F_QGO, 1u);
      const int pt3 = pts + 4096;
      const unsigned vo3 = (lane >> 2) * 2752 + (lane & 3) * 16;
      const char* w3 = (const char*)(hh ? wu : wg) + (size_t)(cl * 32) * 2752;
#pragma unroll
      for (int i = 0; i < 3; ++i) {
        const int aa = g0 + min(i, ng - 1);
        glds(w3 + aa * 64, vo3, pt3 + i * 2048);
        glds(w3 + aa * 64 + 16 * 2752, vo3, pt3 + i * 2048 + 1024);
      }
      st_ws(c, f0, lv, WS_WT1 + m * 96 * KB + (cl * 4 + hh) * KB);
      st_ws(c, f1, lv, WS_WT1 + m * 96 * KB + (cl * 4 + 2 + hh) * KB);
      st_ws(c, f2, lv, WS_WT1 + m * 96 * KB + (64 + cl * 2 + hh) * KB);
      if (WARM0) {
        const v4u Z = {0u, 0u, 0u, 0u};
#pragma unroll
        for (int e = 0; e < 4; ++e) {
          const int off = cl * ACT_CL + (e < 2 ? WS_O + ((2 * hh + e) * 16 + m) * REC : WS_X + ((rh * 2 + hh) * 16 + cg * 2 + (e & 1)) * REC);
          st_ws(c, Z, lv, off); st_ws(c, Z, lv + KB, off);
          if (h == 0) st_ws(c, Z, lv + 2 * KB, off);
        }
      }
      VMWAIT(0);
#pragma unroll
      for (int i = 0; i < 3; ++i) {
        const int aa = g0 + min(i, ng - 1);
        const v4u g0f = tr_frag(L + pt3 + i * 2048 + trS), g1f = tr_frag(L + pt3 + i * 2048 + 1024 + trS);
        st_ws(c, g0f, lv, WS_WT3 + aa * 64 * KB + (cl * 4 + hh) * KB);
        st_ws(c, g1f, lv, WS_WT3 + aa * 64 * KB + (cl * 4 + 2 + hh) * KB);
        const float s = red_h(csum(g1f, csum(g0f, 0.f)));
        if (h == 0) __builtin_amdgcn_raw_buffer_store_b32(f2u(s), c.rs, l31 * 4, WS_CSP + aa * 4 * KB + cl * 256 + hh * 128, 16);
      }
      MEMBAR();
      *flg(c, F_JOB + hh) = 1u;
    }
    VMWAIT(0);
    if (lane == 0) __builtin_amdgcn_raw_buffer_store_b64(tag2, c.rs, 0, WS_F1 + ((m * 16 + cl) * 2 + hh) * 64, 16);
    stamp<1>(c); stamp<3>(c);
    v4u wc[40];
    auto hpub = [&](int layer) INL {
      const int lane = fresh_lane(), lv = lane * 16;
      c.lane = lane;
      const unsigned tg = ((tag2.x * 0x9E3779B1u + tag2.y) | 1u) + layer * 2 * 0x632BE5AAu;
      __builtin_amdgcn_s_setprio(3);
#pragma unroll 1
      for (int e = 0; e < 2; ++e) {
        const int ww = 2 * hh + e;
        wait_lds<1, 1>(c, F_HF + ww, (unsigned)(layer + 1));
        const v4u o0 = *(LAS v4u*)(L + L_XCH + (ww ^ 1) * 4096 + lv), o1 = *(LAS v4u*)(L + L_XCH + (ww ^ 1) * 4096 + KB + lv);
        const int hb = WS_H + cl * ACT_CL + (((REUSE ? 0 : layer) * 4 + ww) * 43 + hpos(ng - 1)) * REC;
        st_ws(c, o0, lv, hb);
        st_ws(c, o1, lv + KB, hb);
        st_chk<false>(c, sum4(sum4(0u, o0), o1), 0u, hb, tg);
      }
      VMWAIT(0);
      *flg(c, F_HACK + hh) = (unsigned)(layer + 1);
      __builtin_amdgcn_s_setprio(1);
    };
    cache_load(wc, IC<5>{}, IC<0>{}, IC<5>{});
    if (hh == 1) {
      wait_lds<1, 2>(c, F_PTOK, 1u);
#pragma unroll 1
      for (int i = 0; i < ng; ++i) {
        float t = 0.f;
#pragma unroll
        for (int q = 0; q < 16; ++q) t += u2f(__builtin_amdgcn_raw_buffer_load_b32(c.rs, lane * 4 + q * 256, WS_CSP + (g0 + i) * 4 * KB, 16));
        *(LAS v4u*)(L + L_CS + (3 + 2 * i) * 512 + lv) = cs_operand(t);
      }
      MEMBAR();
      *flg(c, F_CS3R) = 2u;
      MEMBAR();
    }
    stamp<1>(c);
    __builtin_amdgcn_s_setprio(1);
    cache_put(wc, IC<5>{}, IC<1>{}, 10 + 4 * ng);
    if (HVIA) hpub(0);
    stamp<1>(c);
    if (b == 0 && wave == 6 && lane == 0) __hip_atomic_store((GAS u64*)(ws + WS_E), tag, RLX_AGENT);
    stamp<1>(c);
    __builtin_amdgcn_s_setprio(1);
    cache_put(wc, IC<5>{}, IC<0>{}, 31 + 8 * ng);
    if (HVIA) hpub(1);
  }
}

extern "C" void kernel_launch(void* const* d_in, const int* in_sizes, int n_in, void* d_out, int out_size, void* d_ws, size_t ws_size,
                              hipStream_t stream) {
  fused_fwd<<<256, 512, 0, stream>>>((const u16*)d_in[0], (const u16*)d_in[1], (const u16*)d_in[2], (const u16*)d_in[4], (const u16*)d_in[5],
                                     (const u16*)d_in[6], (u16*)d_out, (char*)d_ws);
}
```

```cpp
#include <hip/hip_runtime.h>
#include <stdint.h>
#include <utility>

#define GAS __attribute__((address_space(1)))
#define LAS __attribute__((address_space(3)))
#define RLX_AGENT __ATOMIC_RELAXED, __HIP_MEMORY_SCOPE_AGENT
typedef unsigned v4u __attribute__((ext_vector_type(4)));
typedef unsigned v2u __attribute__((ext_vector_type(2)));
typedef float f32x2 __attribute__((ext_vector_type(2)));
typedef float f32x4 __attribute__((ext_vector_type(4)));
typedef float f32x16 __attribute__((ext_vector_type(16)));
typedef short s16x8 __attribute__((ext_vector_type(8)));
typedef short s16x4 __attribute__((ext_vector_type(4)));
typedef __bf16 bf16x2 __attribute__((ext_vector_type(2)));
typedef unsigned short u16;
typedef unsigned long long u64;

#define STAMP 0
#define CYC 0
#define LDEP 2
#define XDELAY 12
#define WPACE 2
#define S2H 44
#define BACKOFF 4
#define PTOUCH 0
#define NG2A 32
#define NG2T 150u
#define WARM 0
#define ROT 0
#define KVWARM 1
#define KVBACK 4
#define KVDELAY 18
#define QUIET 0
#define REUSE 1
#define WARM0 0
#define QAT 4u
#define CSDELAY 8
#define TOUCH 1
#define S0AT 0
#define LSLP 4
#define OUTMODE 0
#define OPENR 4
#define RELA 34
#define S0F 32
#define HPER 4
#define H2PER 3
#define PACE 1
#define PUTP 0
#define HVIA 0
#define GFAST 0
#define XD0 12
#define XD1 12
#define XD2 12
#define KVDP 8
#define NG2S 90u
#define RELD 62
#define DPER 6
#define EVD 0
#define ALT 1
#define PUTPRI 1
#define CORD 3
#define STAT0 40
#define LAG1 1
#define TORD 0
#define CE 0
#define XPRE 6
#define XW0 8
#define P4CAP 7
#define CODEPF 59392

constexpr int KB = 1024;
constexpr int WS_E = 0;
constexpr int WS_F1 = 4 * KB;
constexpr int WS_WT1 = 128 * KB;
constexpr int WS_WT3 = WS_WT1 + 16 * 96 * KB;
constexpr int WS_CSP = WS_WT3 + 43 * 64 * KB;
constexpr int WS_S0P = WS_CSP + 43 * 4 * KB;
constexpr int WS_C1P = WS_S0P + 32 * KB;
constexpr int KVR = 2176;
constexpr int WS_KVP = WS_C1P + 16 * 8 * KB;
constexpr int REC = 2560;
constexpr int WS_ACT = 6 * KB * KB, ACT_CL = 2 * KB * KB;
constexpr int WS_O = WS_ACT;
constexpr int WS_X = WS_O + 2 * 4 * 16 * REC;
constexpr int WS_H = WS_X + 3 * 4 * 16 * REC;
static_assert(WS_KVP + 512 * KVR <= WS_ACT && WS_H + (2 * 4 * 43 + 1) * REC <= WS_ACT + ACT_CL, "ws");
constexpr int WS_DBG = WS_ACT + 16 * ACT_CL;
constexpr int WS_END = WS_DBG + 256 * 2 * KB;

constexpr int SLOT = 16384, NSLOT = 8;
constexpr int NSTAMP = 36;
constexpr int L_FLG = 0;
constexpr int L_ZF = L_FLG + 128;
constexpr int L_CS = L_ZF + 64;
constexpr int L_KVF = L_CS + 9 * 512;
constexpr int L_STAMP = L_KVF + 1024;
constexpr int L_KVS = L_STAMP + 1152;
constexpr int L_XCH = 16384;
constexpr int L_RING = 32768;
constexpr int L_END = L_RING + NSLOT * SLOT;
static_assert(L_KVS + 4 * 2304 <= L_XCH && L_END == 163840 && (L_KVS & 15) == 0 && (L_CS & 15) == 0, "lds");
enum { F_FULL = 0, F_TAG = 2, F_FREE = 4, F_JOB = 8, F_KFL = 12, F_QGO = 10, F_CONV = 24, F_CS3R = 15, F_PTOK = 14, F_KVR = 16, F_XF = 20, F_CS1R = 28 };
constexpr unsigned SPIN_CAP = 1u << 17;

template <int V> struct IC { static constexpr int v = V; __device__ constexpr operator int() const { return V; } };
template <class F, int... I> __device__ __forceinline__ void sfor_(F&& fn, std::integer_sequence<int, I...>) { (fn(IC<I>{}), ...); }
template <int N, class F> __device__ __forceinline__ void sfor(F&& fn) { sfor_(fn, std::make_integer_sequence<int, N>{}); }
#define INL __attribute__((always_inline))
#define MEMBAR() asm volatile("" ::: "memory")
#define SB() __builtin_amdgcn_sched_barrier(0)
#define VMWAIT(n) asm volatile("s_waitcnt vmcnt(" #n ")" ::: "memory")
#define PIN(x) asm volatile("" : "+v"(x))

__device__ __forceinline__ unsigned umin(unsigned a, unsigned b) { return a < b ? a : b; }
__device__ __forceinline__ unsigned umax(unsigned a, unsigned b) { return a > b ? a : b; }
__device__ __forceinline__ int gsplit(int i) { return (43 * i) >> 4; }
__device__ __forceinline__ f32x16 mfma(v4u a, v4u b, f32x16 c) {
  return __builtin_amdgcn_mfma_f32_32x32x16_bf16(__builtin_bit_cast(s16x8, a), __builtin_bit_cast(s16x8, b), c, 0, 0, 0);
}
__device__ __forceinline__ f32x16 zero16() { return f32x16{0.f, 0.f, 0.f, 0.f, 0.f, 0.f, 0.f, 0.f, 0.f, 0.f, 0.f, 0.f, 0.f, 0.f, 0.f, 0.f}; }
__device__ __forceinline__ unsigned pack2(float a, float b) {
  bf16x2 v = {(__bf16)a, (__bf16)b};
  return __builtin_bit_cast(unsigned, v);
}
__device__ __forceinline__ float u2f(unsigned u) { return __builtin_bit_cast(float, u); }
__device__ __forceinline__ unsigned f2u(float f) { return __builtin_bit_cast(unsigned, f); }
__device__ __forceinline__ float blo(unsigned u) { return u2f(u << 16); }
__device__ __forceinline__ float bhi(unsigned u) { return u2f(u & 0xffff0000u); }
__device__ __forceinline__ float relu(float x) { return u2f((unsigned)max((int)f2u(x), 0)); }
__device__ __forceinline__ float dot2(unsigned a, unsigned b, float acc) {
  return __builtin_amdgcn_fdot2_f32_bf16(__builtin_bit_cast(bf16x2, a), __builtin_bit_cast(bf16x2, b), acc, false);
}
constexpr unsigned ONES2 = 0x3f803f80u;
constexpr float LOG2E = 1.44269504f;
__device__ __forceinline__ float red_h(float v) {
  auto s = __builtin_amdgcn_permlane32_swap(f2u(v), f2u(v), false, false);
  return u2f((unsigned)s[0]) + u2f((unsigned)s[1]);
}
__device__ __forceinline__ float red_g(float v) {
  auto r = __builtin_amdgcn_permlane16_swap(f2u(v), f2u(v), false, false);
  return red_h(u2f((unsigned)r[0]) + u2f((unsigned)r[1]));
}
template <int CTRL> __device__ __forceinline__ float dpp(float v) { return u2f(__builtin_amdgcn_update_dpp(0, (int)f2u(v), CTRL, 0xf, 0xf, false)); }
__device__ __forceinline__ float exp2c(float x) { return __builtin_amdgcn_fmed3f(__builtin_amdgcn_exp2f(x), 0.f, 1.f); }
__device__ __forceinline__ void split2(float v, unsigned& hi, unsigned& lo) {
  hi = pack2(v, 0.f) & 0xffffu;
  lo = pack2(v - blo(hi), 0.f) & 0xffffu;
}
template <int D = 256> __device__ __forceinline__ v4u tr_frag(LAS char* p) {
  v2u lo = __builtin_bit_cast(v2u, __builtin_amdgcn_ds_read_tr16_b64_v4i16((LAS s16x4*)p));
  v2u hi = __builtin_bit_cast(v2u, __builtin_amdgcn_ds_read_tr16_b64_v4i16((LAS s16x4*)(p + D)));
  return v4u{lo.x, lo.y, hi.x, hi.y};
}
__device__ __forceinline__ void glds(const void* sbase, unsigned voff, unsigned ldst) {
  asm volatile("s_mov_b32 m0, %2\n\ts_nop 0\n\tglobal_load_lds_dwordx4 %0, %1" ::"v"(voff), "s"(sbase), "s"(ldst) : "memory");
}
__device__ __forceinline__ void glds4(const void* sbase, unsigned voff, unsigned ldst) {
  asm volatile(
      "s_mov_b32 m0, %2\n\ts_nop 0\n\tglobal_load_lds_dwordx4 %0, %1 sc1\n\tglobal_load_lds_dwordx4 %0, %1 offset:1024 sc1\n\t"
      "global_load_lds_dwordx4 %0, %1 offset:2048 sc1\n\tglobal_load_lds_dwordx4 %0, %1 offset:3072 sc1" ::"v"(voff), "s"(sbase), "s"(ldst)
      : "memory");
}

struct Ctx {
  LAS char* L;
  __amdgpu_buffer_rsrc_t rs;
  int lane, wave;
  int seq, known;
  int vb0, vb1, nb0, nb1;
  int rot;
  int fa;
  v2u fpre;
  v4u w0, w1, w2, w3;
  int nst;
  unsigned dbgt;
  template <int I> __device__ __forceinline__ v4u& wf() { if constexpr (I == 0) return w0; else if constexpr (I == 1) return w1; else if constexpr (I == 2) return w2; else return w3; }
};
__device__ __forceinline__ volatile LAS unsigned* flg(Ctx& c, int i) { return (volatile LAS unsigned*)(c.L + L_FLG) + i; }
__device__ __forceinline__ void stamp0(Ctx& c) {
}
template <int K> __device__ __forceinline__ void stamp(Ctx& c) { if (STAMP == K) stamp0(c); }
template <int N, int SLP> __device__ __forceinline__ void wait_lds(Ctx& c, int i, unsigned val) {
#pragma nounroll
  for (unsigned sp = 0; sp < SPIN_CAP; ++sp) {
    unsigned v;
    if (N == 1) v = *flg(c, i);
    else if (N == 2) { v2u f = *(volatile LAS v2u*)flg(c, i); v = umin(f.x, f.y); }
    else { v4u f = *(volatile LAS v4u*)flg(c, i); v = umin(umin(f.x, f.y), umin(f.z, f.w)); }
    if ((unsigned)__builtin_amdgcn_readfirstlane(v) >= val) break;
    if (SLP) __builtin_amdgcn_s_sleep(SLP);
  }
  MEMBAR();
}
template <int N> __device__ __forceinline__ void poll_tags(Ctx& c, int base, v2u tag) {
#pragma nounroll
  for (unsigned sp = 0; sp < SPIN_CAP; ++sp) {
    const v2u v = __builtin_amdgcn_raw_buffer_load_b64(c.rs, (c.lane & (N - 1)) * 64, base, 16);
    if (__all(v.x == tag.x && v.y == tag.y)) break;
    __builtin_amdgcn_s_sleep(4);
    MEMBAR();
  }
  MEMBAR();
}
__device__ __forceinline__ v4u ld_ws(Ctx& c, int voff, int soff) { return __builtin_amdgcn_raw_buffer_load_b128(c.rs, voff, soff, 16); }
__device__ __forceinline__ void st_ws(Ctx& c, v4u v, int voff, int soff) {
  __builtin_amdgcn_raw_buffer_store_b128(v, c.rs, voff, soff, 16);
  asm volatile("s_nop 1" ::"v"(v) : "memory");
}
__device__ __forceinline__ int fresh_lane() { int l; asm volatile("v_mbcnt_lo_u32_b32 %0, -1, 0\n\tv_mbcnt_hi_u32_b32 %0, -1, %0" : "=v"(l)); __builtin_assume(l >= 0 && l < 64); return l; }
__device__ __forceinline__ int opq(int v) { asm volatile("" : "+s"(v)); return v; }
__device__ __forceinline__ int opqv(int v) { asm volatile("" : "+v"(v)); return v; }
__device__ __forceinline__ v4u ld_fr(Ctx& c, int lv, int sb, int i) { return ld_ws(c, lv + (i & 3) * KB, sb + (i >> 2) * 4 * KB); }

constexpr int fps(int mode) { return (mode == 1 || mode == 4) ? 8 : 16; }
constexpr bool two(int mode) { return mode == 1 || mode == 2; }
__device__ __forceinline__ void slot_wait(Ctx& c, int sq) {
  if (__builtin_expect(c.known <= sq, 0)) {
    if (CYC == 2) c.dbgt -= (unsigned)__builtin_amdgcn_s_memtime();
#pragma nounroll
    for (unsigned sp = 0; sp < SPIN_CAP; ++sp) {
      v2u f = *(volatile LAS v2u*)flg(c, F_FULL);
      c.known = __builtin_amdgcn_readfirstlane(umin(f.x, f.y));
      if (c.known > sq) break;
    }
    if (CYC == 2) c.dbgt += (unsigned)__builtin_amdgcn_s_memtime();
  }
  MEMBAR();
}
template <int MODE> __device__ __forceinline__ void slot_base(Ctx& c, int sq, int lb0, int lb1) {
  const int sb = (sq & (NSLOT - 1)) * SLOT;
  c.nb0 = lb0 + sb; PIN(c.nb0);
  if (two(MODE)) { c.nb1 = lb1 + sb; PIN(c.nb1); }
}
template <int MODE> __device__ __forceinline__ void slot_next(Ctx& c) {
  c.nb0 = (c.vb0 + SLOT) & (NSLOT * SLOT - 1); PIN(c.nb0);
  if (two(MODE)) { c.nb1 = (c.vb1 + SLOT) & (NSLOT * SLOT - 1); PIN(c.nb1); }
}
template <int MODE, int F, bool OPEN = (F % fps(MODE) == 0)> __device__ __forceinline__ v4u wread(Ctx& c) {
  constexpr int i = F % fps(MODE);
  if constexpr (OPEN) { c.vb0 = c.nb0; if (two(MODE)) c.vb1 = c.nb1; }
  LAS char* R = c.L + L_RING;
  if (MODE == 0) return *(LAS v4u*)(R + c.vb0 + i * KB);
  if (MODE == 1) return tr_frag<512>(R + ((i & 1) ? c.vb1 : c.vb0) + ((i >> 1) & 1) * 2 * KB + (i >> 2) * 8 * KB);
  if (MODE == 4) return *(LAS v4u*)(R + c.vb0 + (i & 3) * KB + (i >> 2) * 8 * KB);
  if (MODE == 2) return tr_frag(R + ((i & 1) ? c.vb1 : c.vb0) + i * KB);
  return tr_frag(R + c.vb0 + i * KB);
}
__device__ __forceinline__ void ring_publish(Ctx& c) { MEMBAR(); *(volatile LAS unsigned*)(c.L + c.fa) = (unsigned)c.seq; MEMBAR(); }
template <int MODE> __device__ __forceinline__ void wpre(Ctx& c, int lb0, int lb1) {
  slot_wait(c, c.seq);
  slot_base<MODE>(c, c.seq, lb0, lb1);
  sfor<4>([&](auto F) INL { c.wf<F.v>() = wread<MODE, F.v>(c); });
  if (fps(MODE) == 8) c.fpre = *(volatile LAS v2u*)flg(c, F_FULL);
}
struct NoSide { template <class T> __device__ __forceinline__ void operator()(T) const {} };
constexpr int fmap(int f, int nf, int fpsl, int tf) {
  if (tf < 0 || f < nf - fpsl || f >= nf) return f;
  const int i = f - (nf - fpsl), hf = fpsl / 2;
  return nf - fpsl + 2 * (i % hf) + (i < hf ? tf : 1 - tf);
}
constexpr u64 DUAL = 0xFEDC76B54A329180ull;
constexpr int dual_first(int ch) { for (int u = 0; u < 16; ++u) if (((DUAL >> (4 * u)) & 15) == (u64)ch) return u; return -1; }
template <int NF, int TPS, int MODE, int NEXT, bool XA, int ZERO, int RELF, int EARLY, int CAP, int F0, int F1, int TF, class SIDE>
__device__ __forceinline__ void gemm4(Ctx& c, v4u (&a)[32], f32x16& c0, f32x16& c1, f32x16& c2, f32x16& c3, int lb0, int lb1, int nb0, int nb1, SIDE side) {
  constexpr int FPS = fps(MODE);
  static_assert(NF % FPS == 0, "whole slots");
  sfor<F1 - F0>([&](auto FI) INL {
    constexpr IC<F0 + FI.v> F{};
    constexpr int f = F.v, un = NF == 128 ? (int)((DUAL >> (4 * (f / 8))) & 15) : 0;
    constexpr int fm = fmap(f, NF, FPS, TF);
    constexpr int s = NF == 128 ? 4 * (un & 7) + (f % 8) / 2 : fm / TPS, t = fm % TPS, fn = f + 4, i = fn % FPS;
    const v4u x = a[s & 31], wv = c.wf<f % 4>();
    f32x16& acc = un >= 8 ? (t ? c3 : c2) : (t ? c1 : c0);
    const f32x16 cin = (s == 0 && ((ZERO >> t) & 1)) ? zero16() : acc;
    acc = XA ? mfma(x, wv, cin) : mfma(wv, x, cin);
    SB();
    if constexpr (fn < NF) {
      c.wf<f % 4>() = wread<MODE, fmap(fn, NF, FPS, TF), i == 0>(c);
      constexpr bool lastslot = fn / FPS == NF / FPS - 1;
      if constexpr (!lastslot || NEXT >= 0) {
        constexpr int sl = fn / FPS, ahead = FPS == 8 && sl + 2 < NF / FPS ? 2 : 1;
        if constexpr (FPS == 16 || !(sl & 1)) {
          if constexpr (i == FPS - 7) c.fpre = *(volatile LAS v2u*)flg(c, F_FULL);
          if constexpr (i == FPS - 4) { c.fpre.x = umin(c.fpre.x, c.fpre.y); PIN(c.fpre); }
          if constexpr (i == FPS - 3) { c.known = __builtin_amdgcn_readfirstlane(c.fpre.x); asm volatile("" : "+s"(c.known)); }
          if constexpr (i == FPS - 1) slot_wait(c, c.seq + ahead);
        }
        if constexpr (i == FPS - 2) {
          if constexpr (lastslot && NEXT != MODE) slot_base<(NEXT < 0 ? 0 : NEXT)>(c, c.seq + 1, nb0, nb1); else slot_next<MODE>(c);
        }
      }
      if constexpr (i == FPS - 1) c.seq++;
    } else if constexpr (NEXT >= 0) {
      c.wf<f % 4>() = wread<(NEXT < 0 ? 0 : NEXT), fn - NF>(c);
      if constexpr (fps(NEXT) == 8 && fn - NF == 1) c.fpre = *(volatile LAS v2u*)flg(c, F_FULL);
    }
    if constexpr (fn <= NF && i == 0) { if constexpr ((f >= RELF || fn / FPS - 1 < EARLY) && fn / FPS - 1 < CAP) ring_publish(c); }
    else if constexpr (f == RELF && RELF > 0) ring_publish(c);
    side(F);
    SB();
  });
}
template <int NF, int TPS, int MODE, int NEXT, bool XA, int ZERO, int RELF, int EARLY, int CAP, int TF = -1, int F0 = 0, int F1 = NF, class SIDE>
__device__ __forceinline__ void gemm(Ctx& c, v4u (&a)[32], f32x16& c0, f32x16& c1, int lb0, int lb1, int nb0, int nb1, SIDE side) {
  gemm4<NF, TPS, MODE, NEXT, XA, ZERO, RELF, EARLY, CAP, F0, F1, TF>(c, a, c0, c1, c0, c1, lb0, lb1, nb0, nb1, side);
}
__device__ __forceinline__ unsigned sum4(unsigned m, v4u v) { return ((m + v.x + v.y) + v.z + v.w); }
template <bool EX> __device__ __forceinline__ void ld_chk(Ctx& c, v2u& k, int sb) {
  if (EX) k = __builtin_amdgcn_raw_buffer_load_b64(c.rs, c.lane * 8 + 2 * KB, sb, 16);
  else k.x = __builtin_amdgcn_raw_buffer_load_b32(c.rs, c.lane * 4 + 2 * KB, sb, 16);
}
template <int STR, bool EX> __device__ __forceinline__ void ld_rec(Ctx& c, v4u (&a)[32], v2u (&k)[16], int lv, int sb, int p, int e) {
  const int q = STR * p;
  const int so = sb + (q < 32 && ROT ? (c.rot + ((q & 15) >> 1) >= 8 ? c.rot * 2 * REC - 16 * REC : c.rot * 2 * REC) : 0) + q * REC;
  if (e < 2) a[(2 * p + e) & 31] = ld_ws(c, lv + e * KB, so);
  else ld_chk<EX>(c, k[p & 15], so);
}
template <bool EX> __device__ __forceinline__ unsigned rec_bad(v4u (&a)[32], v2u (&k)[16], int p, unsigned tag) {
  return sum4(sum4(EX ? k[p & 15].x + k[p & 15].y : k[p & 15].x, a[(2 * p) & 31]), a[(2 * p + 1) & 31]) ^ tag;
}
template <int N, int STR, bool EX> __device__ __forceinline__ void settle(Ctx& c, unsigned bad, v4u (&a)[32], v2u (&k)[16], int lv, int sb, int p0, unsigned tag) {
  if (__builtin_expect(__any(bad != 0u), 0)) {
    if (CYC == 3) c.dbgt -= (unsigned)__builtin_amdgcn_s_memtime();
    unsigned sp = 0;
#pragma nounroll
    do {
      if (BACKOFF) __builtin_amdgcn_s_sleep(BACKOFF);
      MEMBAR();
      bad = 0;
#pragma unroll
      for (int i = 0; i < 3 * N; ++i) ld_rec<STR, EX>(c, a, k, lv, sb, p0 + i / 3, i % 3);
#pragma unroll
      for (int i = 0; i < N; ++i) bad |= rec_bad<EX>(a, k, p0 + i, tag);
    } while (__any(bad != 0u) && ++sp < SPIN_CAP);
    if (CYC == 3) c.dbgt += (unsigned)__builtin_amdgcn_s_memtime();
  }
}
template <int F, int NP, int STR, bool EX>
__device__ __forceinline__ void check_step(Ctx& c, unsigned& m0, unsigned& m1, v4u (&a)[32], v2u (&k)[16], int lv, int sb, unsigned tag) {
  constexpr int p = 2 * ((F + 8) / 8), r = (F + 8) % 8;
  if constexpr (p < NP) {
    if constexpr (r == 2) { m0 = sum4(EX ? k[p & 15].x + k[p & 15].y : k[p & 15].x, a[(2 * p) & 31]); PIN(m0); }
    if constexpr (r == 3) { m0 = sum4(m0, a[(2 * p + 1) & 31]); PIN(m0); }
    if constexpr (r == 4) { m1 = sum4(EX ? k[(p + 1) & 15].x + k[(p + 1) & 15].y : k[(p + 1) & 15].x, a[(2 * p + 2) & 31]); PIN(m1); }
    if constexpr (r == 5) { m1 = sum4(m1, a[(2 * p + 3) & 31]); PIN(m1); }
    if constexpr (r == 6) { m0 = (m0 ^ tag) | (m1 ^ tag); PIN(m0); }
    if constexpr (r == 7) settle<2, STR, EX>(c, m0, a, k, lv, sb, p, tag);
  }
}
template <int STR, bool EX, int XD> __device__ __forceinline__ void open_phase(Ctx& c, v4u (&a)[32], v2u (&k)[16], int lv, int sb, unsigned tag) {
  VMWAIT(0);
  if (XD) __builtin_amdgcn_s_sleep(XD);
#pragma unroll
  for (int i = 0; i < 3 * OPENR; ++i) ld_rec<STR, EX>(c, a, k, lv, sb, i / 3, i % 3);
  SB();
  settle<2, STR, EX>(c, rec_bad<EX>(a, k, 0, tag) | rec_bad<EX>(a, k, 1, tag), a, k, lv, sb, 0, tag);
}
template <int F, int P0, int P1, int STR, bool EX> __device__ __forceinline__ void req_step(Ctx& c, v4u (&a)[32], v2u (&k)[16], int lv, int sb) {
  if constexpr (F >= 0 && P0 + F / 2 < P1) {
    ld_rec<STR, EX>(c, a, k, lv, sb, P0 + F / 2, F & 1);
    if constexpr (F & 1) ld_rec<STR, EX>(c, a, k, lv, sb, P0 + F / 2, 2);
  }
}
template <int G, int PER, int P0, int P1, int STR, bool EX> __device__ __forceinline__ void req_pace(Ctx& c, v4u (&a)[32], v2u (&k)[16], int lv, int sb) {
  if constexpr (G >= 0 && P0 + G / PER < P1) {
    constexpr int r = G % PER, D = PER / 3;
    if constexpr (r % D == 0 && r / D < 3) ld_rec<STR, EX>(c, a, k, lv, sb, P0 + G / PER, r / D);
  }
}
template <bool EX> __device__ __forceinline__ void st_chk(Ctx& c, unsigned sum, unsigned ex, int sb, unsigned tag) {
  if (EX) __builtin_amdgcn_raw_buffer_store_b64(v2u{tag - sum - ex, ex}, c.rs, c.lane * 8 + 2 * KB, sb, 16);
  else __builtin_amdgcn_raw_buffer_store_b32(tag - sum, c.rs, c.lane * 4 + 2 * KB, sb, 16);
}
__device__ __forceinline__ u64 mix64(u64 z) {
  z += 0x9E3779B97F4A7C15ull;
  z = (z ^ (z >> 30)) * 0xBF58476D1CE4E5B9ull;
  z = (z ^ (z >> 27)) * 0x94D049BB133111EBull;
  return z ^ (z >> 31);
}
__device__ __forceinline__ float csum(v4u f, float s) { return dot2(f.w, ONES2, dot2(f.z, ONES2, dot2(f.y, ONES2, dot2(f.x, ONES2, s)))); }
__device__ __forceinline__ v4u cs_operand(float s) { unsigned hi, lo; split2(s, hi, lo); return v4u{hi | (hi << 16), lo, 0u, 0u}; }

__global__ __launch_bounds__(512, 2) void fused_fwd(const u16* __restrict__ x, const u16* __restrict__ wqkv, const u16* __restrict__ wo,
                                                    const u16* __restrict__ wg, const u16* __restrict__ wu, const u16* __restrict__ wd,
                                                    u16* __restrict__ out, char* __restrict__ ws) {
  __shared__ __attribute__((aligned(16))) char smem[L_END];
  u64 pc0;
  asm volatile("s_getpc_b64 %0" : "=s"(pc0));
  Ctx c;
  c.L = (LAS char*)smem;
  c.rs = __builtin_amdgcn_make_buffer_rsrc((void*)ws, 0, WS_END, 0x00020000);
  const int wave = __builtin_amdgcn_readfirstlane(threadIdx.x >> 6);
  const int b = blockIdx.x, xc = b & 7, j = b >> 3;
  const int cl = (xc >> 1) * 4 + (j >> 3), m = (xc & 1) * 8 + (j & 7);
  const int rh = m >> 3, cg = m & 7;
  const int g0 = gsplit(m), ng = gsplit(m + 1) - g0;
  c.lane = 0; c.wave = wave; c.seq = 0; c.known = 0; c.nst = 0; c.dbgt = 1; c.vb0 = 0; c.vb1 = 0; c.nb0 = 0; c.nb1 = 0; c.fa = 0; c.fpre = v2u{0u, 0u};
  const int rot = (2 * cl + m) & 7;
  c.rot = ROT ? rot : 0;
  LAS char* L = c.L;
  if (threadIdx.x < 32) *flg(c, threadIdx.x) = 0u;
  stamp0(c);
  asm volatile("s_waitcnt lgkmcnt(0)\n\ts_barrier" ::: "memory");
#define LANEVARS                                                                                                              \
  const int lane = fresh_lane(), h = lane >> 5, l31 = lane & 31, lv = lane * 16;                                               \
  const int trN = (8 * h + ((lane >> 2) & 3)) * 64 + ((lane >> 4) & 1) * 32 + (lane & 3) * 8;                                   \
  const int trS = (8 * h + ((lane >> 2) & 3)) * 64 + ((lane >> 4) & 1) * 32 + (((lane & 1) << 1) | ((lane >> 1) & 1)) * 8;       \
  (void)trN; (void)trS; (void)l31; (void)lv;
  const int pp = wave & 1;
  auto load_half0 = [&](int sl) INL {
    const int lane = fresh_lane();
    const unsigned vo1 = (lane >> 2) * 3072 + (lane & 3) * 16;
    const char* wb = (const char*)wqkv + m * 64 + (sl < 4 ? (1 + pp) * 1024 : 0);
#pragma unroll
    for (int p = 0; p < 8; ++p) {
      const int s = sl < 4 ? sl * 8 + p : (sl - 4) * 16 + 2 * p + pp;
      glds(wb + (size_t)(((s + 4 * rot) & 31) * 16) * 3072, vo1, L_RING + sl * SLOT + p * 2048 + pp * 1024);
    }
  };
  auto cache_load = [&](auto& wc, auto NK, auto K0, auto K1) INL {
    const int lane = fresh_lane();
    const __amdgpu_buffer_rsrc_t rd = __builtin_amdgcn_make_buffer_rsrc((void*)wd, 0, 1376 * 1024, 0x00020000);
    const int vo2 = (lane >> 3) * 1024 + (((lane & 7) ^ (((lane >> 4) & 1) << 2)) * 16);
#pragma unroll
    for (int k = K0.v; k < K1.v; ++k) {
#pragma unroll
      for (int q = 0; q < 2; ++q) {
        const int hs = (ALT ? (NK.v == 5 ? 2 * k : k < 5 ? 2 * k + 1 : 10) : (NK.v == 5 ? k : k + 5)) * 2 + pp;
        const int p = min((hs < 16 ? (hs & 8) | ((hs + c.rot) & 7) : hs) * 2 + q, 42);
        const int m3 = min(p - 32 + 1 + ((p - 32) >> 1), 15);
        const int aa = p < 32 ? gsplit(p & 15) + (p >> 4) : gsplit(m3) + 2;
#pragma unroll
        for (int e = 0; e < 4; ++e) wc[k * 8 + q * 4 + e] = __builtin_amdgcn_raw_buffer_load_b128(rd, vo2, aa * 32768 + cg * 128 + e * 8192, 0);
        SB();
        if (WPACE && NK.v == 5) __builtin_amdgcn_s_sleep(WPACE);
      }
    }
  };
  auto cache_conv = [&](auto& wc, auto NK) INL {
    const int lane = fresh_lane(), h = lane >> 5;
    const int tk = (8 * h + ((lane >> 2) & 3)) * 128 + ((lane >> 4) & 1) * 32 + (((lane & 1) << 1) | ((lane >> 1) & 1)) * 8 + ((lane >> 3) & 1) * 64;
    LAS char* d = L + L_XCH + (wave - 4) * 4096;
    wait_lds<4, 2>(c, F_FREE, 4u);
#pragma unroll
    for (int q = 0; q < 2 * NK.v; ++q) {
#pragma unroll
      for (int e = 0; e < 4; ++e) *(LAS v4u*)(d + lane * 16 + e * KB) = wc[q * 4 + e];
      MEMBAR(); SB();
#pragma unroll
      for (int e = 0; e < 4; ++e) wc[q * 4 + e] = tr_frag<512>(d + (e >> 1) * 2048 + (tk ^ (((e ^ q) & 1) * 64)));
      MEMBAR(); SB();
    }
    *flg(c, F_CONV + wave - 4) = 1u;
    MEMBAR();
  };
  auto cache_put = [&](auto& wc, auto NK, int base) INL {
    const int lane = fresh_lane();
#pragma unroll
    for (int k = 0; k < NK.v; ++k) {
      const int sq = base + (ALT ? (NK.v == 5 ? 2 * k : k < 5 ? 2 * k + 1 : 10) : (NK.v == 5 ? k : k + 5));
      wait_lds<4, LSLP>(c, F_FREE, (unsigned)(sq - NSLOT + 1));
      __builtin_amdgcn_s_setprio(PUTPRI);
      LAS char* d = L + L_RING + (sq & (NSLOT - 1)) * SLOT + pp * 8192;
#pragma unroll
      for (int e = 0; e < 8; ++e) {
        *(LAS v4u*)(d + lane * 16 + e * KB) = wc[k * 8 + e];
        if (PUTP) { MEMBAR(); asm volatile("s_nop %0" ::"n"(PUTP)); }
      }
      wait_lds<1, 2>(c, F_FULL + pp, (unsigned)sq);
      *flg(c, F_FULL + pp) = (unsigned)(sq + 1);
      MEMBAR();
      __builtin_amdgcn_s_setprio(1);
    }
  };
  auto hpos = [&](int gi) INL { return gi * 16 + (gi < 2 ? m : g0 - 2 * m); };

  if (wave < 4) {
    __builtin_amdgcn_s_setprio(2);
    const int w = wave, mh = w >> 1, kh = w & 1;
    c.fa = opqv(L_FLG + (F_FREE + wave) * 4);
    asm volatile("" : "+s"(c.rs));
    v4u a[32], xr0, xr1;
    v2u k[16];
    f32x16 A0, A1, B0, B1;
    LANEVARS
    c.lane = lane;
    float rstd;
    unsigned tagx, tagy, tagc;
    v4u mfrag;
    auto set_stats = [&](float sum, float ssq) INL -> float {
      const float s = sum * (1.f / 512), q = ssq * (1.f / 512);
      unsigned hi, lo;
      split2(-s, hi, lo);
      mfrag = h == 0 ? v4u{hi | (lo << 16), hi, 0u, 0u} : v4u{0u, 0u, 0u, 0u};
      return fmaxf(q - s * s, 0.f) + 1e-5f;
    };
    auto cs_frag = [&](int tile) INL { return *(LAS v4u*)(L + L_CS + tile * 512 + l31 * 16); };
    auto rs_put = [&]() INL { const int lane = fresh_lane(); *(LAS float*)(L + L_KVS + w * 2304 + lane * 4) = h ? rstd * LOG2E : rstd; MEMBAR(); };
    auto rs_get = [&](int q) INL {
      const int h = fresh_lane() >> 5;
      const f32x4 r = *(LAS f32x4*)(L + L_KVS + w * 2304 + (8 * q + 4 * h) * 4), rl = *(LAS f32x4*)(L + L_KVS + w * 2304 + 128 + (8 * q + 4 * h) * 4);
#pragma unroll
      for (int i = 0; i < 4; ++i) { B0[4 * q + i] = r[i]; B1[4 * q + i] = rl[i]; }
    };
    {
      const __amdgpu_buffer_rsrc_t rx = __builtin_amdgcn_make_buffer_rsrc((void*)x, 0, 2048 * 1024, 0x00020000);
      const int xv = (lane >> 3) * 1024 + (lane & 7) * 16, xs = (cl * 128 + w * 32) * 1024;
      auto ldx = [&](int i) INL { a[i] = __builtin_amdgcn_raw_buffer_load_b128(rx, xv, xs + (i & 3) * 8192 + (((i >> 2) + rot) & 7) * 128, 0); };
      __builtin_amdgcn_s_setprio(3);
#pragma unroll
      for (int i = 0; i < XW0; ++i) ldx(i);
      __builtin_amdgcn_s_setprio(2);
#pragma unroll
      for (int i = XW0; i < 16; ++i) ldx(i);
      SB();
      stamp<3>(c);
      LAS char* sc = L + L_XCH + w * 4096;
      const int r8 = lane >> 3;
      const int wo_ = r8 * 128 + (((lane & 7) ^ (r8 >> 1)) * 16);
      const int ro = l31 * 128 + ((h ^ ((l31 >> 1) & 7)) * 16);
      auto xstep = [&](int kp, int e) INL {
        MEMBAR();
        if (e < 4) *(LAS v4u*)(sc + ((wo_ + e * 1024) ^ ((e & 1) * 64))) = a[kp * 4 + e];
        else a[kp * 4 + e - 4] = *(LAS v4u*)(sc + (ro ^ ((e - 4) * 32)));
        MEMBAR();
      };
#pragma unroll
      for (int kp = 0; kp < XPRE; ++kp) {
#pragma unroll
        for (int e = 0; e < 8; ++e) xstep(kp, e);
        if (kp < 2) {
#pragma unroll
          for (int i = 16 + 8 * kp; i < 24 + 8 * kp; ++i) ldx(i);
        }
      }
      stamp<3>(c);
      wpre<2>(c, trN, trS);
      stamp<3>(c);
      v4u s0;
      const int s0b = WS_S0P + (cl * 128 + w * 32) * 16;
      gemm<64, 2, 2, -1, true, 3, 0, 0, 99>(c, a, A0, A1, trN, trS, 0, 0, [&](auto F) INL {
        constexpr int f = F.v;
        if ((f >> 3) + XPRE < 8) xstep((f >> 3) + XPRE, f & 7);
        if (f == S0AT) s0 = ld_ws(c, l31 * 16, s0b);
        if (f == STAT0) {
          wait_lds<2, 0>(c, F_CS1R, 1u);
          const v2u tg = *(volatile LAS v2u*)flg(c, F_TAG);
          tagx = __builtin_amdgcn_readfirstlane(tg.x); tagy = __builtin_amdgcn_readfirstlane(tg.y); tagc = (tagx * 0x9E3779B1u + tagy) | 1u;
          if (__any(s0.y != tagx || s0.w != tagy)) {
            unsigned sp = 0;
#pragma nounroll
            do { MEMBAR(); s0 = ld_ws(c, l31 * 16, s0b); } while (__any(s0.y != tagx || s0.w != tagy) && ++sp < SPIN_CAP);
          }
          rstd = __builtin_amdgcn_rsqf(set_stats(u2f(s0.x), u2f(s0.z)));
          rs_put();
          xr0 = cs_frag(0); xr1 = cs_frag(1);
        }
        if (f == STAT0 + 5) { A0 = mfma(mfrag, xr0, A0); A1 = mfma(mfrag, xr1, A1); }
        if (f >= 54 && f < 62 && !(f & 1)) rs_get((f - 54) >> 1);
        if ((f & 15) == 15) stamp<3>(c);
      });
      const int xq = l31 * 1024 + h * 16, xqs = (cl * 128 + rh * 64 + mh * 32) * 1024 + cg * 128 + kh * 64;
      xr0 = __builtin_amdgcn_raw_buffer_load_b128(rx, xq, xqs, 0);
      xr1 = __builtin_amdgcn_raw_buffer_load_b128(rx, xq + 32, xqs, 0);
    }
#pragma unroll 1
    for (int layer = 0; layer < 2; ++layer) {
      const int lb = REUSE ? 0 : layer;
      stamp<1>(c);
      bool publisher;
      {
        LANEVARS
#pragma unroll
        for (int i = 0; i < 16; ++i) B1[i] *= A0[i];
        SB();
#pragma unroll
        for (int i = 0; i < 16; ++i) { B1[i] = exp2c(B1[i]); A0[i] = fmaf(A0[i], B0[i], 1.f); A1[i] *= B0[i]; }
        SB();
        v4u pk0, pk1, vv0, vv1;
#pragma unroll
        for (int i = 0; i < 16; ++i) A0[i] = fmaxf(A0[i], B1[i]) + 1e-6f;
#pragma unroll
        for (int i = 0; i < 4; ++i) {
          pk0[i] = pack2(A0[2 * i], A0[2 * i + 1]); vv0[i] = pack2(A1[2 * i], A1[2 * i + 1]);
          pk1[i] = pack2(A0[8 + 2 * i], A0[9 + 2 * i]); vv1[i] = pack2(A1[8 + 2 * i], A1[9 + 2 * i]);
        }
        f32x16 kv = mfma(pk0, vv0, zero16());
        kv = mfma(pk1, vv1, kv);
        const float z = red_h(csum(pk0, 0.f) + csum(pk1, 0.f));
        LAS char* ks = L + L_KVS + w * 2304;
        if (h == 0) *(LAS float*)(ks + 2048 + l31 * 4) = z;
        if (lane & 16) {
          *(LAS f32x4*)(ks + lv) = f32x4{kv[8], kv[9], kv[10], kv[11]};
          *(LAS f32x4*)(ks + 1024 + lv) = f32x4{kv[12], kv[13], kv[14], kv[15]};
          MEMBAR();
        } else {
          MEMBAR();
          *(LAS f32x4*)(ks + lv) = f32x4{kv[0], kv[1], kv[2], kv[3]};
          *(LAS f32x4*)(ks + 1024 + lv) = f32x4{kv[4], kv[5], kv[6], kv[7]};
        }
        MEMBAR();
        unsigned old = 0;
        if (lane == 0) old = __hip_atomic_fetch_add((LAS unsigned*)flg(c, F_KFL), 1u, __ATOMIC_RELAXED, __HIP_MEMORY_SCOPE_WORKGROUP);
        publisher = (__builtin_amdgcn_readfirstlane(old) & 3u) == 3u;
        if (publisher) {
          const int lane = fresh_lane();
          MEMBAR();
          f32x4 k0 = {0.f, 0.f, 0.f, 0.f}, k1 = k0;
          f32x2 zz = {0.f, 0.f};
#pragma unroll
          for (int ww = 0; ww < 4; ++ww) {
            k0 += *(LAS f32x4*)(L + L_KVS + ww * 2304 + lv);
            k1 += *(LAS f32x4*)(L + L_KVS + ww * 2304 + 1024 + lv);
            zz += *(LAS f32x2*)(L + L_KVS + ww * 2304 + 2048 + (lane & 15) * 8);
          }
          const unsigned tg = layer ? tagy : tagx;
          const unsigned tl = tg >> ((lane & 1) * 16), t0 = tl & 0xffu, t1 = (tl >> 8) & 0xffu;
          const int kvb = WS_KVP + ((layer * 16 + m) * 16 + cl) * KVR;
          auto st2 = [&](float u, float v, int vo) INL {
            __builtin_amdgcn_raw_buffer_store_b64(v2u{(f2u(u) & 0xffffff00u) | t0, (f2u(v) & 0xffffff00u) | t1}, c.rs, vo, kvb, 16);
          };
          st2(k0[0], k0[1], lane * 8); st2(k0[2], k0[3], lane * 8 + 512); st2(k1[0], k1[1], lane * 8 + 1024); st2(k1[2], k1[3], lane * 8 + 1536);
          if (lane < 16) st2(zz[0], zz[1], lane * 8 + 2048);
        }
        MEMBAR();
      }
      stamp<1>(c);
      B0 = mfma(cs_frag(2), mfrag, zero16());
      if (layer == 0) { LANEVARS wpre<3>(c, trN, trN); gemm<32, 1, 3, -1, false, 0, 0, 0, 99>(c, a, B0, B0, trN, trN, 0, 0, NoSide()); }
      else { wpre<0>(c, lv, lv); gemm<32, 1, 0, -1, false, 0, 0, 0, 99>(c, a, B0, B0, lv, lv, 0, 0, NoSide()); }
      const int lq = fresh_lane();
      const int kb0 = (8 * (lq >> 5) + ((lq >> 2) & 3)) * 128 + ((lq >> 4) & 1) * 32 + (((lq & 1) << 1) | ((lq >> 1) & 1)) * 8 + kh * 4096 + ((kh ^ (lq >> 3)) & 1) * 64;
      const int kb1 = kb0 ^ 64;
      auto res_init = [&](f32x16& R, int i) INL { const unsigned u = i < 4 ? xr0[i & 3] : xr1[i & 3]; R[2 * i] = blo(u); R[2 * i + 1] = bhi(u); };
      {
        LANEVARS
        v4u pq0, pq1;
        const float rl = rstd * LOG2E;
#pragma unroll
        for (int i = 0; i < 16; ++i) B1[i] = exp2c(B0[i] * rl);
#pragma unroll
        for (int i = 0; i < 16; ++i) B0[i] = fmaxf(fmaf(B0[i], rstd, 1.f), B1[i]) + 1e-6f;
#pragma unroll
        for (int i = 0; i < 4; ++i) { pq0[i] = pack2(B0[2 * i], B0[2 * i + 1]); pq1[i] = pack2(B0[8 + 2 * i], B0[9 + 2 * i]); }
#pragma unroll
        for (int i = 0; i < 8; ++i) res_init(A0, i);
        PIN(pq0); PIN(pq1); PIN(A0);
        const int ob = opq(WS_O + cl * ACT_CL + ((lb * 4 + w) * 16 + m) * REC);
        stamp<1>(c);
        {
          const int lane = fresh_lane();
          const unsigned tg = layer ? tagy : tagx;
          const unsigned tl = tg >> ((lane & 1) * 16), t0 = tl & 0xffu, t1 = (tl >> 8) & 0xffu;
          const int kvb = WS_KVP + (layer * 16 + m) * 16 * KVR;
          float s0 = 0.f, s1 = 0.f;
          v2u zp;
          if (publisher) __builtin_amdgcn_s_sleep(KVDP); else __builtin_amdgcn_s_sleep(KVDELAY);
#pragma nounroll
          for (unsigned sp = 0; sp < SPIN_CAP; ++sp) {
            v2u p[16];
            zp = __builtin_amdgcn_raw_buffer_load_b64(c.rs, (lane >> 2) * KVR + (lane & 3) * 8 + 2048, kvb + w * 32, 16);
#pragma unroll
            for (int i = 0; i < 16; ++i) p[i] = __builtin_amdgcn_raw_buffer_load_b64(c.rs, lane * 8, kvb + w * 512 + i * KVR, 16);
            SB();
            zp.x ^= t0; zp.y ^= t1;
            unsigned chk = zp.x | zp.y;
#pragma unroll
            for (int i = 0; i < 16; ++i) {
              p[i].x ^= t0; p[i].y ^= t1; chk |= p[i].x | p[i].y;
              s0 = i ? s0 + u2f(p[i].x) : u2f(p[i].x); s1 = i ? s1 + u2f(p[i].y) : u2f(p[i].y);
              PIN(s0); PIN(s1);
            }
            if (!__any((chk & 0xffu) != 0u)) break;
            if (KVBACK) __builtin_amdgcn_s_sleep(KVBACK);
            MEMBAR();
          }
          *(LAS unsigned*)(L + L_KVF + lv + w * 4) = pack2(s0, s1);
          float za = u2f(zp.x), zb = u2f(zp.y);
          za += dpp<0x128>(za); zb += dpp<0x128>(zb); za += dpp<0x124>(za); zb += dpp<0x124>(zb);
          za = red_g(za); zb = red_g(zb);
          if (lane < 4) *(LAS unsigned*)(L + L_ZF + w * 16 + lane * 4) = pack2(za, zb);
          MEMBAR();
          *flg(c, F_KVR + w) = (unsigned)(layer + 1);
        }
        if (WARM0 && layer == 0) wait_lds<2, 0>(c, F_JOB, 1u);
        v4u kvf;
        v2u z0, z1, z2, z3;
#pragma nounroll
        for (unsigned sp = 0; sp < SPIN_CAP; ++sp) {
          const v4u fl = *(volatile LAS v4u*)flg(c, F_KVR);
          MEMBAR();
          kvf = *(LAS v4u*)(L + L_KVF + lv);
          z0 = *(LAS v2u*)(L + L_ZF + h * 8); z1 = *(LAS v2u*)(L + L_ZF + 16 + h * 8);
          z2 = *(LAS v2u*)(L + L_ZF + 32 + h * 8); z3 = *(LAS v2u*)(L + L_ZF + 48 + h * 8);
          MEMBAR();
          if ((unsigned)__builtin_amdgcn_readfirstlane(umin(umin(fl.x, fl.y), umin(fl.z, fl.w))) > (unsigned)layer) break;
        }
        stamp<1>(c);
        const bool up = (lane & 16) != 0;
        const v4u zz = {0u, 0u, 0u, 0u};
        B0 = mfma(up ? zz : kvf, pq0, zero16());
        B0 = mfma(up ? kvf : zz, pq1, B0);
        const float d0 = dot2(pq0.y, z0.y, dot2(pq0.x, z0.x, 0.f)) + dot2(pq0.w, z1.y, dot2(pq0.z, z1.x, 0.f));
        const float d1 = dot2(pq1.y, z2.y, dot2(pq1.x, z2.x, 0.f)) + dot2(pq1.w, z3.y, dot2(pq1.z, z3.x, 0.f));
        const float r0 = __builtin_amdgcn_rcpf(red_h(d0) + 1e-6f), r1 = __builtin_amdgcn_rcpf(red_h(d1) + 1e-6f);
        v4u o0, o1;
#pragma unroll
        for (int i = 0; i < 4; ++i) { o0[i] = pack2(B0[2 * i] * r0, B0[2 * i + 1] * r0); o1[i] = pack2(B0[8 + 2 * i] * r1, B0[9 + 2 * i] * r1); }
        st_ws(c, o0, lv, ob);
        st_ws(c, o1, lv + KB, ob);
        st_chk<false>(c, sum4(sum4(0u, o0), o1), 0u, ob, tagc);
      }
      stamp<1>(c);
      const int rtA = opq(rh * 2 + mh), clb = opq(cl * ACT_CL);
      auto res_send = [&](f32x16& A1, int q, unsigned xseq) INL {
        LAS char* xs = L + L_XCH + w * 4096 + lv;
        *(LAS f32x4*)(xs + q * 2048) = f32x4{A1[8 * q], A1[8 * q + 1], A1[8 * q + 2], A1[8 * q + 3]};
        *(LAS f32x4*)(xs + q * 2048 + 1024) = f32x4{A1[8 * q + 4], A1[8 * q + 5], A1[8 * q + 6], A1[8 * q + 7]};
        MEMBAR();
        if (q) *flg(c, F_XF + w) = xseq;
      };
      auto res_epilogue = [&](f32x16& A0, f32x16& A1, int xi, bool final_out, unsigned xseq) INL {
        LAS char* xs = L + L_XCH;
        const int lv = fresh_lane() * 16;
        f32x4 o0, o1, o2, o3;
#pragma nounroll
        for (unsigned sp = 0; sp < SPIN_CAP; ++sp) {
          const unsigned fl = *flg(c, F_XF + (w ^ 1));
          MEMBAR();
          o0 = *(LAS f32x4*)(xs + (w ^ 1) * 4096 + lv); o1 = *(LAS f32x4*)(xs + (w ^ 1) * 4096 + 1024 + lv);
          o2 = *(LAS f32x4*)(xs + (w ^ 1) * 4096 + 2048 + lv); o3 = *(LAS f32x4*)(xs + (w ^ 1) * 4096 + 3072 + lv);
          MEMBAR();
          if ((unsigned)__builtin_amdgcn_readfirstlane(fl) >= xseq) break;
        }
        xr0 = v4u{pack2(A0[0] + o0[0], A0[1] + o0[1]), pack2(A0[2] + o0[2], A0[3] + o0[3]), pack2(A0[4] + o1[0], A0[5] + o1[1]), pack2(A0[6] + o1[2], A0[7] + o1[3])};
        xr1 = v4u{pack2(A0[8] + o2[0], A0[9] + o2[1]), pack2(A0[10] + o2[2], A0[11] + o2[3]), pack2(A0[12] + o3[0], A0[13] + o3[1]), pack2(A0[14] + o3[2], A0[15] + o3[3])};
        if (final_out) {
          const int lo = fresh_lane();
          GAS char* ob = (GAS char*)out + (size_t)((cl * 4 + rtA) * 32 + (lo & 31)) * 1024 + cg * 128 + kh * 64 + (lo >> 5) * 16;
          *(GAS v4u*)ob = xr0;
          *(GAS v4u*)(ob + 32) = xr1;
        } else {
          const int xb = WS_X + clb + (((REUSE ? xi >> 1 : xi) * 4 + rtA) * 16 + cg * 2 + kh) * REC;
          st_ws(c, xr0, lv, xb);
          st_ws(c, xr1, lv + KB, xb);
          const float s = csum(xr0, 0.f) + csum(xr1, 0.f);
          float q0 = 0.f, q1 = 0.f;
#pragma unroll
          for (int i = 0; i < 4; ++i) { q0 = dot2(xr0[i], xr0[i], q0); q1 = dot2(xr1[i], xr1[i], q1); }
          const auto sw = __builtin_amdgcn_permlane32_swap(f2u(s), f2u(q0 + q1), false, false);
          st_chk<true>(c, sum4(sum4(0u, xr0), xr1), f2u(u2f((unsigned)sw[0]) + u2f((unsigned)sw[1])), xb, tagc);
        }
      };
      unsigned m0, m1;
      {
        const int ab = WS_O + clb + ((lb * 4 + rtA) * 16 + kh) * REC;
        wpre<1>(c, kb0, kb1);
        open_phase<2, false, XD0>(c, a, k, lv, ab, tagc);
        stamp<1>(c);
        gemm<32, 2, 1, -1, false, 2, 8, 0, 99, 1>(c, a, A0, A1, kb0, kb1, 0, 0, [&](auto F) INL {
          if (F.v == 24 && layer == 0) { if (CORD == 3) wait_lds<2, 0>(c, F_CONV + 2, 1u); else wait_lds<4, 0>(c, F_CONV, 1u); }
          if constexpr (F.v == 29 || F.v == 30) res_send(A1, F.v - 29, (unsigned)(layer * 0x200 + 1));
          req_step<F.v, OPENR, 8, 2, false>(c, a, k, lv, ab);
          check_step<F.v, 8, 2, false>(c, m0, m1, a, k, lv, ab, tagc);
        });
        stamp<1>(c);
        res_epilogue(A0, A1, layer * 2, false, (unsigned)(layer * 0x200 + 1));
      }
      *flg(c, F_XF + w) = (unsigned)(layer * 0x200 + 0x100);
      *(LAS v4u*)(L + L_KVS + w * 2304 + lv) = xr0;
      *(LAS v4u*)(L + L_KVS + w * 2304 + 1024 + lv) = xr1;
      MEMBAR();
      auto res_back = [&]() INL { xr0 = *(LAS v4u*)(L + L_KVS + w * 2304 + lv); xr1 = *(LAS v4u*)(L + L_KVS + w * 2304 + 1024 + lv); };
      wpre<0>(c, lv, lv);
      stamp<1>(c);
      const int rtB = w;
      v4u cs0;
      float sacc;
      auto ln_side = [&](auto F, int xb, float& var) INL -> bool {
        constexpr int f = F.v;
        if constexpr (f == 0) { sacc = u2f(k[0].y) + u2f(k[1].y); PIN(sacc); }
        if constexpr (PACE) {
          req_step<f, OPENR, 10, 1, true>(c, a, k, lv, xb);
          if constexpr (f >= 12 && f < 36 && (f & 3) < 3) ld_rec<1, true>(c, a, k, lv, xb, 10 + (f - 12) / 4, f & 3);
        } else req_step<f, OPENR, 16, 1, true>(c, a, k, lv, xb);
        check_step<f, 16, 1, true>(c, m0, m1, a, k, lv, xb, tagc);
        if constexpr ((f & 7) == 0 && f >= 8 && f <= 56) { sacc += u2f(k[(f / 4) & 15].y) + u2f(k[(f / 4 + 1) & 15].y); PIN(sacc); }
        if constexpr (f == 57) {
          const auto sw = __builtin_amdgcn_permlane32_swap(f2u(sacc), f2u(sacc), false, false);
          var = set_stats(u2f((unsigned)sw[0]), u2f((unsigned)sw[1]));
          PIN(mfrag);
          return true;
        }
        return false;
      };
      const int ab4 = WS_H + clb + ((lb * 4 + rtA) * 43 + kh) * REC;
      const int hb = WS_H + clb + (lb * 4 + rtB) * 43 * REC;
      float r2;
      v4u ho;
      unsigned hs;
      float h0, h1;
      auto hside = [&](auto F, auto VIA, f32x16& G, f32x16& U, int gi) INL {
        constexpr int f = F.v, sl = (f - 1) / 2;
        if constexpr (f >= 1 && f < 17) {
          if constexpr (f & 1) { h0 = relu(G[2 * sl]) * r2; PIN(h0); h1 = relu(G[2 * sl + 1]) * r2; PIN(h1); }
          else {
            h0 *= U[2 * sl]; PIN(h0); h1 *= U[2 * sl + 1]; PIN(h1);
            ho[sl & 3] = pack2(h0, h1); PIN(ho);
          }
        }
        {
          if constexpr (f == 9 || f == 17) { st_ws(c, ho, lv + (f == 17) * KB, hb + hpos(gi) * REC); hs = sum4(f == 9 ? 0u : hs, ho); PIN(hs); }
          if constexpr (f == 18) st_chk<false>(c, hs, 0u, hb + hpos(gi) * REC, tagc);
        }
      };
      auto corr = [&](auto F, int at, f32x16& G, f32x16& U, int gi) INL {
        if (F.v == at - 6) cs0 = cs_frag(3 + 2 * gi);
        if (F.v == at - 3) { G = mfma(cs0, mfrag, G); SB(); cs0 = cs_frag(4 + 2 * gi); }
        if (F.v == at) U = mfma(cs0, mfrag, U);
      };
      const int xb3 = WS_X + clb + ((REUSE ? layer : layer * 2) * 4 + rtB) * 16 * REC;
      open_phase<1, true, XD1>(c, a, k, lv, xb3, tagc);
      stamp<1>(c); stamp<2>(c);
      gemm4<128, 2, 0, 0, false, 3, RELD, 0, 99, 0, 128, -1>(c, a, A0, A1, B0, B1, lv, lv, lv, lv, [&](auto F) INL {
        constexpr int f = F.v, u = f / 8;
        if constexpr (f == 0) { sacc = u2f(k[0].y) + u2f(k[1].y); PIN(sacc); }
        req_pace<f, DPER, OPENR, 16, 1, true>(c, a, k, lv, xb3);
        sfor<8>([&](auto CH) INL {
          if constexpr (CH.v > 0 && dual_first(CH.v) == u + 1) check_step<8 * (CH.v - 1) + f % 8, 16, 1, true>(c, m0, m1, a, k, lv, xb3, tagc);
          if constexpr (CH.v > 0 && f == 8 * dual_first(CH.v)) { sacc += u2f(k[2 * CH.v].y) + u2f(k[2 * CH.v + 1].y); PIN(sacc); }
        });
        if constexpr (f == 89) {
          const auto sw = __builtin_amdgcn_permlane32_swap(f2u(sacc), f2u(sacc), false, false);
          r2 = __builtin_amdgcn_rcpf(set_stats(u2f((unsigned)sw[0]), u2f((unsigned)sw[1])));
          PIN(mfrag); PIN(r2);
        }
        if (f == 86 && layer == 0) wait_lds<1, 0>(c, F_CS3R, 1u);
        corr(F, 95, A0, A1, 0);
        if constexpr (f >= 96) hside(IC<f - 96>{}, IC<0>{}, A0, A1, 0);
        corr(F, 123, B0, B1, 1);
      });
      stamp<2>(c);
      const int tb0 = fresh_lane() * 16 + kh * 4096, tb1 = tb0;
      constexpr int NPRE = (64 - S0F) / HPER;
      static_assert(NPRE >= 8 && NPRE <= 16, "pace");
      if (ng == 3) {
        gemm<64, 2, 0, 4, false, 3, 0, 0, 99>(c, a, A0, A1, lv, lv, tb0, tb1, [&](auto F) INL {
          constexpr int f = F.v;
          hside(F, IC<0>{}, B0, B1, 1);
          corr(F, 47, A0, A1, 2);
          if constexpr (f == 19) res_back();
          if constexpr (f >= 23 && f < 31) { res_init(B0, f - 23); PIN(B0); }
          req_pace<f - S0F, HPER, 0, NPRE, 2, false>(c, a, k, lv, ab4);
          if constexpr (f >= 56) check_step<f - 64, 22, 2, false>(c, m0, m1, a, k, lv, ab4, tagc);
        });
      }
      if (opq(ng) == 2) {
        const unsigned tg0 = (unsigned)__builtin_amdgcn_s_memrealtime();
        sfor<20>([&](auto F) INL { hside(F, IC<0>{}, B0, B1, 1); });
        res_back();
#pragma unroll
        for (int i = 0; i < 8; ++i) res_init(B0, i);
        while ((unsigned)__builtin_amdgcn_s_memrealtime() - tg0 < NG2S) __builtin_amdgcn_s_sleep(2);
        sfor<2 * NPRE>([&](auto F) INL { req_step<F.v, 0, NPRE, 2, false>(c, a, k, lv, ab4); SB(); });
        wpre<4>(c, tb0, tb1);
        settle<2, 2, false>(c, rec_bad<false>(a, k, 0, tagc) | rec_bad<false>(a, k, 1, tagc), a, k, lv, ab4, 0, tagc);
        while ((unsigned)__builtin_amdgcn_s_memrealtime() - tg0 < NG2T) __builtin_amdgcn_s_sleep(2);
      }
      stamp<1>(c); stamp<2>(c); stamp<4>(c); stamp<6>(c);
      {
        auto side4 = [&](auto F) INL {
          constexpr int f = F.v;
          if constexpr (f < 10) { hside(IC<2 * f>{}, IC<0>{}, A0, A1, 2 * ng - 4); hside(IC<2 * f + 1>{}, IC<0>{}, A0, A1, 2 * ng - 4); }
          if (f == 10 && EVD && rh == 0) __builtin_amdgcn_s_sleep(EVD);
          req_pace<f, HPER, NPRE, 16, 2, false>(c, a, k, lv, ab4);
          req_pace<f - S2H, H2PER, 16, 22, 2, false>(c, a, k, lv, ab4);
          if constexpr (f < 72) check_step<f, 22, 2, false>(c, m0, m1, a, k, lv, ab4, tagc);
          if constexpr (f == 74) settle<1, 2, false>(c, rec_bad<false>(a, k, 20, tagc), a, k, lv, ab4, 20, tagc);
          if constexpr (f == 78) {
            if (kh == 0) settle<1, 2, false>(c, rec_bad<false>(a, k, 21, tagc), a, k, lv, ab4, 21, tagc);
            else { a[10] = v4u{0u, 0u, 0u, 0u}; a[11] = v4u{0u, 0u, 0u, 0u}; }
          }
          if constexpr (f == 85 || f == 86) res_send(B1, f - 85, (unsigned)(layer * 0x200 + 0x101));
          if ((f & 7) == 7) stamp<4>(c);
          if (f == 15 || f == 39 || f == 55 || f == 65 || f == 83) stamp<6>(c);
        };
        gemm<88, 2, 4, -1, false, 2, 32, 3, P4CAP, 1>(c, a, B0, B1, tb0, tb1, 0, 0, side4);
        stamp<1>(c); stamp<2>(c);
        tagc += 0x632BE5AAu;
        res_epilogue(B0, B1, 1, layer == 1, (unsigned)(layer * 0x200 + 0x101));
      }
      if (layer == 1) break;
      wpre<0>(c, lv, lv);
      stamp<1>(c);
      const int xb1 = WS_X + clb + ((REUSE ? 0 : 4) + rtB) * 16 * REC;
      open_phase<1, true, XD2>(c, a, k, lv, xb1, tagc);
      stamp<1>(c);
      gemm<64, 2, 0, -1, true, 3, RELA, 0, 99>(c, a, A0, A1, lv, lv, 0, 0, [&](auto F) INL {
        constexpr int f = F.v;
        float var;
        if (ln_side(F, xb1, var)) { rstd = __builtin_amdgcn_rsqf(var); rs_put(); }
        if (f == 56) cs0 = cs_frag(0);
        if (f == 60) { A0 = mfma(mfrag, cs0, A0); SB(); cs0 = cs_frag(1); }
        if (f == 63) A1 = mfma(mfrag, cs0, A1);
        if (f >= 59 && f < 63) rs_get(f - 59);
      });
      tagc += 0x632BE5AAu;
    }
  } else if (wave < 6) {
    const int lw = wave - 4;
    LANEVARS
    c.lane = lane;
    volatile LAS unsigned* full = flg(c, F_FULL + lw);
    __builtin_amdgcn_s_setprio(3);
    load_half0(0);
    if (CODEPF && lw == 1) {
      glds((const char*)(pc0 & ~(u64)255) + j * (CODEPF / 32), min(lane * 32, CODEPF / 32 - 32), L_RING + 7 * SLOT + 12288);
    } else {
      asm volatile("" ::: "memory");
    }
    __builtin_amdgcn_s_setprio(2);
    load_half0(1);
    if (LAG1) { VMWAIT(8); *full = 1u; }
    load_half0(2);
    if (!LAG1) { VMWAIT(16); *full = 1u; }
    stamp<3>(c);
    if (LAG1 == 2) { VMWAIT(8); *full = 2u; }
    load_half0(3);
    auto touch = [&]() INL {
      const unsigned tv = min(lane, 42) * 128, td = L_RING + (6 + lw) * SLOT + (lw ? 13312 : 3072);
      glds((const char*)(lw ? wu : wg) + b * 5504, tv, td);
      if (lw) glds((const char*)wd + b * 5504, tv, td);
      else glds((const char*)wo + b * 2048, (lane & 15) * 128, td);
      *flg(c, F_QGO + lw) = 1u;
    };
    VMWAIT(16); *full = 2u;
    if (TORD == 0) {
      touch();
      VMWAIT(10); *full = 3u;
      VMWAIT(2); *full = 4u;
      __builtin_amdgcn_s_setprio(1);
      load_half0(4);
      stamp<3>(c);
      load_half0(5);
      VMWAIT(8); *full = 5u;
      VMWAIT(0); *full = 6u;
    } else {
      VMWAIT(8); *full = 3u;
      __builtin_amdgcn_s_setprio(1);
      load_half0(4);
      VMWAIT(8); *full = 4u;
      stamp<3>(c);
      load_half0(5);
      touch();
      VMWAIT(10); *full = 5u;
      VMWAIT(2); *full = 6u;
      VMWAIT(0);
    }
    stamp<1>(c); stamp<3>(c);
    int seq = 6, pub = 6;
    v4u wc[48];
    auto stream = [&](int seg, int i0, int i1) INL {
      const int ln = fresh_lane(), lv = ln * 16;
      const unsigned vo2 = (ln >> 3) * 1024 + (((ln & 7) ^ (((ln >> 4) & 1) << 2)) * 16);
      const int src = (seg == 0 ? WS_WT1 + m * 96 * KB : WS_WT3 + g0 * 64 * KB);
#pragma unroll 1
      for (int i = i0; i < i1; ++i) {
        if (seq >= NSLOT) {
          const unsigned need = (unsigned)(seq - NSLOT + 1);
          bool first = true;
#pragma nounroll
          for (unsigned sp = 0; sp < SPIN_CAP; ++sp) {
            v4u f = *(volatile LAS v4u*)flg(c, F_FREE);
            if ((unsigned)__builtin_amdgcn_readfirstlane(umin(umin(f.x, f.y), umin(f.z, f.w))) >= need) break;
            if (first && pub < seq) { VMWAIT(0); pub = seq; *full = (unsigned)pub; }
            first = false;
            __builtin_amdgcn_s_sleep(LSLP);
          }
          MEMBAR();
        }
        const unsigned dst = L_RING + (seq & (NSLOT - 1)) * SLOT + lw * 8192;
        const int hs = i * 2 + lw, hr = (seg == 2 && hs < 16) ? (int)((DUAL >> (4 * hs)) & 15) : hs;
        if (seg == 1) {
          const char* sp = (const char*)wo + (size_t)hr * 65536 + cg * 128;
#pragma unroll
          for (int e = 0; e < 8; ++e) glds(sp + e * 8192, vo2, dst + e * 1024);
        } else if (seg == 0 && i >= 4) {
          glds4(ws + src + 65536 + ((hs * 2 + c.rot) & 7) * 4096, lv, dst);
          glds4(ws + src + 65536 + ((hs * 2 + 1 + c.rot) & 7) * 4096, lv, dst + 4096);
        } else {
          glds4(ws + src + hr * 8192, lv, dst);
          glds4(ws + src + hr * 8192 + 4096, lv, dst + 4096);
        }
        ++seq;
        if (seq - pub > LDEP) {
          if (LDEP == 1) VMWAIT(8); else if (LDEP == 2) VMWAIT(16); else VMWAIT(24);
          pub = seq - LDEP; *full = (unsigned)pub;
        }
      }
    };
    auto drain = [&]() INL { VMWAIT(0); if (pub < seq) { pub = seq; *full = (unsigned)seq; } };
    if (CORD == 2) cache_load(wc, IC<6>{}, IC<0>{}, IC<6>{});
    if (CORD == 3) cache_load(wc, IC<6>{}, IC<0>{}, IC<CE>{});
    wait_lds<2, 1>(c, F_JOB, 1u);
    stream(1, 0, 4);
    drain();
    if (CORD == 2) cache_conv(wc, IC<6>{});
    stamp<1>(c); stamp<3>(c);
    if (CORD == 0) cache_load(wc, IC<6>{}, IC<0>{}, IC<6>{});
    if (lw == 0) { poll_tags<32>(c, WS_F1 + m * 2048, *(volatile LAS v2u*)flg(c, F_TAG)); *flg(c, F_PTOK) = 1u; }
    else wait_lds<1, 1>(c, F_PTOK, 1u);
    stamp<1>(c); stamp<3>(c);
    stream(2, 0, 4);
    if (CORD == 1) cache_load(wc, IC<6>{}, IC<0>{}, IC<6>{});
    if (CORD < 2) { drain(); cache_conv(wc, IC<6>{}); }
    if (CORD == 3) {
      cache_load(wc, IC<6>{}, IC<CE>{}, IC<6>{});
      stream(2, 4, 8);
      drain();
      wait_lds<4, 1>(c, F_XF, 0x100u);
      cache_conv(wc, IC<6>{});
    }
    stream(2, CORD == 3 ? 8 : 4, 4 * ng);
    drain();
    stamp<1>(c);
    cache_put(wc, IC<6>{}, seq);
    seq += 11; pub = seq;
    stamp<1>(c);
    stream(0, 0, 6);
    stream(1, 0, 4);
    stamp<1>(c);
    stream(2, 0, 4 * ng);
    drain();
    stamp<1>(c);
    cache_put(wc, IC<6>{}, seq);
  } else {
    const int hh = wave - 6;
    LANEVARS
    c.lane = lane;
    const int pts = L_RING + (6 + hh) * SLOT;
    __builtin_amdgcn_s_setprio(3);
    {
      const unsigned vo1 = (lane >> 2) * 3072 + (lane & 3) * 16;
      const char* j0 = (const char*)wqkv + (size_t)(cl * 32) * 3072 + m * 64;
      glds(j0 + (1 + hh) * 1024, vo1, pts); glds(j0 + (1 + hh) * 1024 + 16 * 3072, vo1, pts + 1024);
      glds(j0 + hh * 16 * 3072, vo1, pts + 2048);
    }
    const int srow = cl * 128 + m * 8 + hh * 4 + (lane >> 4);
    v4u xs[4];
    v2u e0;
    {
      const GAS char* xp = (const GAS char*)x + (size_t)srow * 1024 + (lane & 15) * 16;
      const int zo = opqv(0);
      asm volatile("global_load_dwordx4 %0, %5, off\n\tglobal_load_dwordx4 %1, %5, off offset:256\n\tglobal_load_dwordx4 %2, %5, off offset:512\n\t"
                   "global_load_dwordx4 %3, %5, off offset:768\n\tglobal_load_dwordx2 %4, %6, %7 sc1"
                   : "=&v"(xs[0]), "=&v"(xs[1]), "=&v"(xs[2]), "=&v"(xs[3]), "=&v"(e0) : "v"(xp), "v"(zo), "s"(ws + WS_E) : "memory");
    }
    asm volatile("s_waitcnt vmcnt(0)" : "+v"(xs[0]), "+v"(xs[1]), "+v"(xs[2]), "+v"(xs[3]), "+v"(e0)::"memory");
    const u64 tag = mix64(((u64)(unsigned)__builtin_amdgcn_readfirstlane(e0.y) << 32) | (unsigned)__builtin_amdgcn_readfirstlane(e0.x));
    const v2u tag2 = {(unsigned)tag, (unsigned)(tag >> 32)};
    if (hh == 0) *(volatile LAS v2u*)flg(c, F_TAG) = tag2;
    stamp<1>(c); stamp<3>(c);
    {
      const unsigned tb = ((unsigned)(mix64(tag) >> 32) >> ((lane & 3) * 8)) & 0xffu;
      const v4u f0 = tr_frag(L + pts + (hh ? trS : trN)), f1 = tr_frag(L + pts + 1024 + (hh ? trS : trN)), f2 = tr_frag(L + pts + 2048 + trN);
      const float s0 = red_h(csum(f1, csum(f0, 0.f))), s2 = red_h(csum(f2, 0.f));
      __builtin_amdgcn_raw_buffer_store_b32((f2u(h ? s2 : s0) & 0xffffff00u) | tb, c.rs, l31 * 4 + h * 4096, WS_C1P + m * 8 * KB + cl * 256 + hh * 128, 16);
      float sa = 0.f, sb = 0.f, qa = 0.f, qb = 0.f;
#pragma unroll
      for (int i = 0; i < 4; ++i) {
        sa = dot2(xs[i].y, ONES2, dot2(xs[i].x, ONES2, sa)); sb = dot2(xs[i].w, ONES2, dot2(xs[i].z, ONES2, sb));
        qa = dot2(xs[i].y, xs[i].y, dot2(xs[i].x, xs[i].x, qa)); qb = dot2(xs[i].w, xs[i].w, dot2(xs[i].z, xs[i].z, qb));
      }
      sa += sb; qa += qb;
      sa += dpp<0x128>(sa); qa += dpp<0x128>(qa); sa += dpp<0x124>(sa); qa += dpp<0x124>(qa);
      sa += dpp<0x122>(sa); qa += dpp<0x122>(qa); sa += dpp<0x121>(sa); qa += dpp<0x121>(qa);
      if ((lane & 15) == 0) st_ws(c, v4u{f2u(sa), tag2.x, f2u(qa), tag2.y}, srow * 16, WS_S0P);
      if (KVWARM) {
        const unsigned nt = ~(hh ? tag2.y : tag2.x);
        const v4u W = {nt, nt >> 8, nt >> 16, nt >> 24};
        const int kvb = WS_KVP + ((hh * 16 + m) * 16 + cl) * KVR;
        st_ws(c, W, lv, kvb); st_ws(c, W, lv + KB, kvb);
        if (lane < 8) st_ws(c, W, lv + 2 * KB, kvb);
      }
      VMWAIT(0);
      if (CSDELAY) __builtin_amdgcn_s_sleep(CSDELAY);
      stamp<3>(c);
      {
        float t;
#pragma nounroll
        for (unsigned sp = 0; sp < SPIN_CAP; ++sp) {
          unsigned p[16];
#pragma unroll
          for (int i = 0; i < 16; ++i) p[i] = __builtin_amdgcn_raw_buffer_load_b32(c.rs, lane * 4 + i * 256, WS_C1P + m * 8 * KB + hh * 4096, 16);
          SB();
          unsigned chk = 0;
          t = 0.f;
#pragma unroll
          for (int i = 0; i < 16; ++i) { p[i] ^= tb; chk |= p[i]; t += u2f(p[i]); }
          if (!__any((chk & 0xffu) != 0u)) break;
          MEMBAR();
        }
        if (hh) t = red_h(t);
        if (hh == 0 || h == 0) *(LAS v4u*)(L + L_CS + hh * 1024 + lv) = cs_operand(t);
        MEMBAR();
        *flg(c, F_CS1R + hh) = 1u;
      }
      stamp<3>(c);
      __builtin_amdgcn_s_setprio(1);
      wait_lds<2, 1>(c, F_QGO, 1u);
      const int pt3 = pts + 4096;
      const unsigned vo3 = (lane >> 2) * 2752 + (lane & 3) * 16;
      const char* w3 = (const char*)(hh ? wu : wg) + (size_t)(cl * 32) * 2752;
#pragma unroll
      for (int i = 0; i < 3; ++i) {
        const int aa = g0 + min(i, ng - 1);
        glds(w3 + aa * 64, vo3, pt3 + i * 2048);
        glds(w3 + aa * 64 + 16 * 2752, vo3, pt3 + i * 2048 + 1024);
      }
      st_ws(c, f0, lv, WS_WT1 + m * 96 * KB + (cl * 4 + hh) * KB);
      st_ws(c, f1, lv, WS_WT1 + m * 96 * KB + (cl * 4 + 2 + hh) * KB);
      st_ws(c, f2, lv, WS_WT1 + m * 96 * KB + (64 + cl * 2 + hh) * KB);
      if (WARM0) {
        const v4u Z = {0u, 0u, 0u, 0u};
#pragma unroll
        for (int e = 0; e < 4; ++e) {
          const int off = cl * ACT_CL + (e < 2 ? WS_O + ((2 * hh + e) * 16 + m) * REC : WS_X + ((rh * 2 + hh) * 16 + cg * 2 + (e & 1)) * REC);
          st_ws(c, Z, lv, off); st_ws(c, Z, lv + KB, off);
          if (h == 0) st_ws(c, Z, lv + 2 * KB, off);
        }
      }
      VMWAIT(0);
#pragma unroll
      for (int i = 0; i < 3; ++i) {
        const int aa = g0 + min(i, ng - 1);
        const v4u g0f = tr_frag(L + pt3 + i * 2048 + trS), g1f = tr_frag(L + pt3 + i * 2048 + 1024 + trS);
        st_ws(c, g0f, lv, WS_WT3 + aa * 64 * KB + (cl * 4 + hh) * KB);
        st_ws(c, g1f, lv, WS_WT3 + aa * 64 * KB + (cl * 4 + 2 + hh) * KB);
        const float s = red_h(csum(g1f, csum(g0f, 0.f)));
        if (h == 0) __builtin_amdgcn_raw_buffer_store_b32(f2u(s), c.rs, l31 * 4, WS_CSP + aa * 4 * KB + cl * 256 + hh * 128, 16);
      }
      MEMBAR();
      *flg(c, F_JOB + hh) = 1u;
    }
    VMWAIT(0);
    if (lane == 0) __builtin_amdgcn_raw_buffer_store_b64(tag2, c.rs, 0, WS_F1 + ((m * 16 + cl) * 2 + hh) * 64, 16);
    stamp<1>(c); stamp<3>(c);
    v4u wc[40];
    cache_load(wc, IC<5>{}, IC<0>{}, IC<5>{});
    cache_conv(wc, IC<5>{});
    if (hh == 1) {
      wait_lds<1, 2>(c, F_PTOK, 1u);
#pragma unroll 1
      for (int i = 0; i < ng; ++i) {
        float t = 0.f;
#pragma unroll
        for (int q = 0; q < 16; ++q) t += u2f(__builtin_amdgcn_raw_buffer_load_b32(c.rs, lane * 4 + q * 256, WS_CSP + (g0 + i) * 4 * KB, 16));
        *(LAS v4u*)(L + L_CS + (3 + 2 * i) * 512 + lv) = cs_operand(t);
      }
      MEMBAR();
      *flg(c, F_CS3R) = 2u;
      MEMBAR();
    }
    stamp<1>(c);
    __builtin_amdgcn_s_setprio(1);
    cache_put(wc, IC<5>{}, 10 + 4 * ng);
    stamp<1>(c);
    if (b == 0 && wave == 6 && lane == 0) __hip_atomic_store((GAS u64*)(ws + WS_E), tag, RLX_AGENT);
    stamp<1>(c);
    __builtin_amdgcn_s_setprio(1);
    cache_put(wc, IC<5>{}, 31 + 8 * ng);
  }
}

extern "C" void kernel_launch(void* const* d_in, const int* in_sizes, int n_in, void* d_out, int out_size, void* d_ws, size_t ws_size,
                              hipStream_t stream) {
  fused_fwd<<<256, 512, 0, stream>>>((const u16*)d_in[0], (const u16*)d_in[1], (const u16*)d_in[2], (const u16*)d_in[4], (const u16*)d_in[5],
                                     (const u16*)d_in[6], (u16*)d_out, (char*)d_ws);
}
```

```cpp
#include <hip/hip_runtime.h>
#include <stdint.h>
#include <utility>

#define GAS __attribute__((address_space(1)))
#define LAS __attribute__((address_space(3)))
#define RLX_AGENT __ATOMIC_RELAXED, __HIP_MEMORY_SCOPE_AGENT
typedef unsigned v4u __attribute__((ext_vector_type(4)));
typedef unsigned v2u __attribute__((ext_vector_type(2)));
typedef float f32x2 __attribute__((ext_vector_type(2)));
typedef float f32x4 __attribute__((ext_vector_type(4)));
typedef float f32x16 __attribute__((ext_vector_type(16)));
typedef short s16x8 __attribute__((ext_vector_type(8)));
typedef short s16x4 __attribute__((ext_vector_type(4)));
typedef __bf16 bf16x2 __attribute__((ext_vector_type(2)));
typedef unsigned short u16;
typedef unsigned long long u64;

constexpr int XD = 12;
constexpr int YOFF = 14;
constexpr int KVDELAY = 18;
constexpr int KVDP = 14;
constexpr int BACKOFF = 4;
constexpr int NG2S = 90;
constexpr int NG2T = 150;
constexpr int S2H = 44;
constexpr int XPRE = 6;
constexpr int CODEPF = 59392;

constexpr int KB = 1024;
constexpr int WS_E = 0;
constexpr int WS_F1 = 4 * KB;
constexpr int WS_WT1 = 128 * KB;
constexpr int WS_WT3 = WS_WT1 + 16 * 96 * KB;
constexpr int WS_CSP = WS_WT3 + 43 * 64 * KB;
constexpr int WS_S0P = WS_CSP + 43 * 4 * KB;
constexpr int WS_C1P = WS_S0P + 32 * KB;
constexpr int KVR = 2176;
constexpr int WS_KVP = WS_C1P + 16 * 8 * KB;
constexpr int REC = 2560;
constexpr int WS_ACT = 6 * KB * KB, ACT_CL = KB * KB;
constexpr int WS_O = WS_ACT;
constexpr int WS_X = WS_O + 4 * 16 * REC;
constexpr int WS_H = WS_X + 2 * 4 * 16 * REC;
static_assert(WS_KVP + 512 * KVR <= WS_ACT && WS_H + 4 * 43 * REC <= WS_ACT + ACT_CL, "ws");
constexpr int WS_END = WS_ACT + 16 * ACT_CL;

constexpr int SLOT = 16384, NSLOT = 8;
constexpr int L_FLG = 0;
constexpr int L_ZF = L_FLG + 128;
constexpr int L_CS = L_ZF + 64;
constexpr int L_KVF = L_CS + 9 * 512;
constexpr int L_KVS = L_KVF + 1024 + 1152;
constexpr int L_XCH = 16384;
constexpr int L_RING = 32768;
constexpr int L_END = L_RING + NSLOT * SLOT;
static_assert(L_KVS + 4 * 2304 <= L_XCH && L_END == 163840 && (L_KVS & 15) == 0 && (L_CS & 15) == 0, "lds");
enum { F_FULL = 0, F_TAG = 2, F_FREE = 4, F_JOB = 8, F_KFL = 12, F_QGO = 10, F_CONV = 24, F_CS3R = 31, F_PTOK = 30, F_KVR = 16, F_XF = 20, F_CS1R = 28 };
constexpr unsigned SPIN_CAP = 1u << 17;

template <int V> struct IC { static constexpr int v = V; __device__ constexpr operator int() const { return V; } };
template <class F, int... I> __device__ __forceinline__ void sfor_(F&& fn, std::integer_sequence<int, I...>) { (fn(IC<I>{}), ...); }
template <int N, class F> __device__ __forceinline__ void sfor(F&& fn) { sfor_(fn, std::make_integer_sequence<int, N>{}); }
#define INL __attribute__((always_inline))
#define MEMBAR() asm volatile("" ::: "memory")
#define SB() __builtin_amdgcn_sched_barrier(0)
#define VMWAIT(n) asm volatile("s_waitcnt vmcnt(" #n ")" ::: "memory")
#define PIN(x) asm volatile("" : "+v"(x))

__device__ __forceinline__ unsigned umin(unsigned a, unsigned b) { return a < b ? a : b; }
__device__ __forceinline__ int gsplit(int i) { return (43 * i) >> 4; }
__device__ __forceinline__ f32x16 mfma(v4u a, v4u b, f32x16 c) {
  return __builtin_amdgcn_mfma_f32_32x32x16_bf16(__builtin_bit_cast(s16x8, a), __builtin_bit_cast(s16x8, b), c, 0, 0, 0);
}
__device__ __forceinline__ f32x16 zero16() { return f32x16{0.f, 0.f, 0.f, 0.f, 0.f, 0.f, 0.f, 0.f, 0.f, 0.f, 0.f, 0.f, 0.f, 0.f, 0.f, 0.f}; }
__device__ __forceinline__ unsigned pack2(float a, float b) {
  bf16x2 v = {(__bf16)a, (__bf16)b};
  return __builtin_bit_cast(unsigned, v);
}
__device__ __forceinline__ float u2f(unsigned u) { return __builtin_bit_cast(float, u); }
__device__ __forceinline__ unsigned f2u(float f) { return __builtin_bit_cast(unsigned, f); }
__device__ __forceinline__ float blo(unsigned u) { return u2f(u << 16); }
__device__ __forceinline__ float bhi(unsigned u) { return u2f(u & 0xffff0000u); }
__device__ __forceinline__ float relu(float x) { return u2f((unsigned)max((int)f2u(x), 0)); }
__device__ __forceinline__ float dot2(unsigned a, unsigned b, float acc) {
  return __builtin_amdgcn_fdot2_f32_bf16(__builtin_bit_cast(bf16x2, a), __builtin_bit_cast(bf16x2, b), acc, false);
}
constexpr unsigned ONES2 = 0x3f803f80u;
constexpr float LOG2E = 1.44269504f;
__device__ __forceinline__ float red_h(float v) {
  auto s = __builtin_amdgcn_permlane32_swap(f2u(v), f2u(v), false, false);
  return u2f((unsigned)s[0]) + u2f((unsigned)s[1]);
}
__device__ __forceinline__ float red_g(float v) {
  auto r = __builtin_amdgcn_permlane16_swap(f2u(v), f2u(v), false, false);
  return red_h(u2f((unsigned)r[0]) + u2f((unsigned)r[1]));
}
template <int CTRL> __device__ __forceinline__ float dpp(float v) { return u2f(__builtin_amdgcn_update_dpp(0, (int)f2u(v), CTRL, 0xf, 0xf, false)); }
__device__ __forceinline__ float exp2c(float x) { return __builtin_amdgcn_fmed3f(__builtin_amdgcn_exp2f(x), 0.f, 1.f); }
__device__ __forceinline__ void split2(float v, unsigned& hi, unsigned& lo) {
  hi = pack2(v, 0.f) & 0xffffu;
  lo = pack2(v - blo(hi), 0.f) & 0xffffu;
}
template <int D = 256> __device__ __forceinline__ v4u tr_frag(LAS char* p) {
  v2u lo = __builtin_bit_cast(v2u, __builtin_amdgcn_ds_read_tr16_b64_v4i16((LAS s16x4*)p));
  v2u hi = __builtin_bit_cast(v2u, __builtin_amdgcn_ds_read_tr16_b64_v4i16((LAS s16x4*)(p + D)));
  return v4u{lo.x, lo.y, hi.x, hi.y};
}
__device__ __forceinline__ void glds(const void* sbase, unsigned voff, unsigned ldst) {
  asm volatile("s_mov_b32 m0, %2\n\ts_nop 0\n\tglobal_load_lds_dwordx4 %0, %1" ::"v"(voff), "s"(sbase), "s"(ldst) : "memory");
}
__device__ __forceinline__ void glds4(const void* sbase, unsigned voff, unsigned ldst) {
  asm volatile(
      "s_mov_b32 m0, %2\n\ts_nop 0\n\tglobal_load_lds_dwordx4 %0, %1 sc1\n\tglobal_load_lds_dwordx4 %0, %1 offset:1024 sc1\n\t"
      "global_load_lds_dwordx4 %0, %1 offset:2048 sc1\n\tglobal_load_lds_dwordx4 %0, %1 offset:3072 sc1" ::"v"(voff), "s"(sbase), "s"(ldst)
      : "memory");
}

struct Ctx {
  LAS char* L;
  __amdgpu_buffer_rsrc_t rs;
  int lane, wave;
  int seq, known;
  int vb0, vb1, nb0, nb1;
  int fa;
  v2u fpre;
  v4u w0, w1, w2, w3;
  template <int I> __device__ __forceinline__ v4u& wf() { if constexpr (I == 0) return w0; else if constexpr (I == 1) return w1; else if constexpr (I == 2) return w2; else return w3; }
};
__device__ __forceinline__ volatile LAS unsigned* flg(Ctx& c, int i) { return (volatile LAS unsigned*)(c.L + L_FLG) + i; }
template <int N, int SLP> __device__ __forceinline__ void wait_lds(Ctx& c, int i, unsigned val) {
#pragma nounroll
  for (unsigned sp = 0; sp < SPIN_CAP; ++sp) {
    unsigned v;
    if (N == 1) v = *flg(c, i);
    else if (N == 2) { v2u f = *(volatile LAS v2u*)flg(c, i); v = umin(f.x, f.y); }
    else { v4u f = *(volatile LAS v4u*)flg(c, i); v = umin(umin(f.x, f.y), umin(f.z, f.w)); }
    if ((unsigned)__builtin_amdgcn_readfirstlane(v) >= val) break;
    if (SLP) __builtin_amdgcn_s_sleep(SLP);
  }
  MEMBAR();
}
template <int N> __device__ __forceinline__ void poll_tags(Ctx& c, int base, v2u tag) {
#pragma nounroll
  for (unsigned sp = 0; sp < SPIN_CAP; ++sp) {
    const v2u v = __builtin_amdgcn_raw_buffer_load_b64(c.rs, (c.lane & (N - 1)) * 64, base, 16);
    if (__all(v.x == tag.x && v.y == tag.y)) break;
    __builtin_amdgcn_s_sleep(4);
    MEMBAR();
  }
  MEMBAR();
}
__device__ __forceinline__ v4u ld_ws(Ctx& c, int voff, int soff) { return __builtin_amdgcn_raw_buffer_load_b128(c.rs, voff, soff, 16); }
__device__ __forceinline__ void st_ws(Ctx& c, v4u v, int voff, int soff) {
  __builtin_amdgcn_raw_buffer_store_b128(v, c.rs, voff, soff, 16);
  asm volatile("s_nop 1" ::"v"(v) : "memory");
}
__device__ __forceinline__ int fresh_lane() { int l; asm volatile("v_mbcnt_lo_u32_b32 %0, -1, 0\n\tv_mbcnt_hi_u32_b32 %0, -1, %0" : "=v"(l)); __builtin_assume(l >= 0 && l < 64); return l; }
__device__ __forceinline__ int opq(int v) { asm volatile("" : "+s"(v)); return v; }
__device__ __forceinline__ int opqv(int v) { asm volatile("" : "+v"(v)); return v; }

constexpr int fps(int mode) { return (mode == 1 || mode == 4) ? 8 : 16; }
constexpr bool two(int mode) { return mode == 1 || mode == 2; }
__device__ __forceinline__ void slot_wait(Ctx& c, int sq) {
  if (__builtin_expect(c.known <= sq, 0)) {
#pragma nounroll
    for (unsigned sp = 0; sp < SPIN_CAP; ++sp) {
      v2u f = *(volatile LAS v2u*)flg(c, F_FULL);
      c.known = __builtin_amdgcn_readfirstlane(umin(f.x, f.y));
      if (c.known > sq) break;
    }
  }
  MEMBAR();
}
template <int MODE> __device__ __forceinline__ void slot_base(Ctx& c, int sq, int lb0, int lb1) {
  const int sb = (sq & (NSLOT - 1)) * SLOT;
  c.nb0 = lb0 + sb; PIN(c.nb0);
  if (two(MODE)) { c.nb1 = lb1 + sb; PIN(c.nb1); }
}
template <int MODE> __device__ __forceinline__ void slot_next(Ctx& c) {
  c.nb0 = (c.vb0 + SLOT) & (NSLOT * SLOT - 1); PIN(c.nb0);
  if (two(MODE)) { c.nb1 = (c.vb1 + SLOT) & (NSLOT * SLOT - 1); PIN(c.nb1); }
}
template <int MODE, int F, bool OPEN = (F % fps(MODE) == 0)> __device__ __forceinline__ v4u wread(Ctx& c) {
  constexpr int i = F % fps(MODE);
  if constexpr (OPEN) { c.vb0 = c.nb0; if (two(MODE)) c.vb1 = c.nb1; }
  LAS char* R = c.L + L_RING;
  if (MODE == 0) return *(LAS v4u*)(R + c.vb0 + i * KB);
  if (MODE == 1) return tr_frag<512>(R + ((i & 1) ? c.vb1 : c.vb0) + ((i >> 1) & 1) * 2 * KB + (i >> 2) * 8 * KB);
  if (MODE == 4) return *(LAS v4u*)(R + c.vb0 + (i & 3) * KB + (i >> 2) * 8 * KB);
  if (MODE == 2) return tr_frag(R + ((i & 1) ? c.vb1 : c.vb0) + i * KB);
  return tr_frag(R + c.vb0 + i * KB);
}
__device__ __forceinline__ void ring_publish(Ctx& c) { MEMBAR(); *(volatile LAS unsigned*)(c.L + c.fa) = (unsigned)c.seq; MEMBAR(); }
template <int MODE> __device__ __forceinline__ void wpre(Ctx& c, int lb0, int lb1) {
  slot_wait(c, c.seq);
  slot_base<MODE>(c, c.seq, lb0, lb1);
  sfor<4>([&](auto F) INL { c.wf<F.v>() = wread<MODE, F.v>(c); });
  if (fps(MODE) == 8) c.fpre = *(volatile LAS v2u*)flg(c, F_FULL);
}
struct NoSide { template <class T> __device__ __forceinline__ void operator()(T) const {} };
constexpr int fmap(int f, int nf, int fpsl, int tf) {
  if (tf < 0 || f < nf - fpsl || f >= nf) return f;
  const int i = f - (nf - fpsl), hf = fpsl / 2;
  return nf - fpsl + 2 * (i % hf) + (i < hf ? tf : 1 - tf);
}
constexpr u64 DUAL = 0xFEDC76B54A329180ull;
constexpr int dual_first(int ch) { for (int u = 0; u < 16; ++u) if (((DUAL >> (4 * u)) & 15) == (u64)ch) return u; return -1; }
template <int NF, int TPS, int MODE, int NEXT, bool XA, int ZERO, int RELF, int EARLY, int CAP, int TF, class SIDE>
__device__ __forceinline__ void gemm4(Ctx& c, v4u (&a)[32], f32x16& c0, f32x16& c1, f32x16& c2, f32x16& c3, int lb0, int lb1, int nb0, int nb1, SIDE side) {
  constexpr int FPS = fps(MODE);
  static_assert(NF % FPS == 0, "whole slots");
  sfor<NF>([&](auto F) INL {
    constexpr int f = F.v, un = NF == 128 ? (int)((DUAL >> (4 * (f / 8))) & 15) : 0;
    constexpr int fm = fmap(f, NF, FPS, TF);
    constexpr int s = NF == 128 ? 4 * (un & 7) + (f % 8) / 2 : fm / TPS, t = fm % TPS, fn = f + 4, i = fn % FPS;
    const v4u x = a[s & 31], wv = c.wf<f % 4>();
    f32x16& acc = un >= 8 ? (t ? c3 : c2) : (t ? c1 : c0);
    const f32x16 cin = (s == 0 && ((ZERO >> t) & 1)) ? zero16() : acc;
    acc = XA ? mfma(x, wv, cin) : mfma(wv, x, cin);
    SB();
    if constexpr (fn < NF) {
      c.wf<f % 4>() = wread<MODE, fmap(fn, NF, FPS, TF), i == 0>(c);
      constexpr bool lastslot = fn / FPS == NF / FPS - 1;
      if constexpr (!lastslot || NEXT >= 0) {
        constexpr int sl = fn / FPS, ahead = FPS == 8 && sl + 2 < NF / FPS ? 2 : 1;
        if constexpr (FPS == 16 || !(sl & 1)) {
          if constexpr (i == FPS - 7) c.fpre = *(volatile LAS v2u*)flg(c, F_FULL);
          if constexpr (i == FPS - 4) { c.fpre.x = umin(c.fpre.x, c.fpre.y); PIN(c.fpre); }
          if constexpr (i == FPS - 3) { c.known = __builtin_amdgcn_readfirstlane(c.fpre.x); asm volatile("" : "+s"(c.known)); }
          if constexpr (i == FPS - 1) slot_wait(c, c.seq + ahead);
        }
        if constexpr (i == FPS - 2) {
          if constexpr (lastslot && NEXT != MODE) slot_base<(NEXT < 0 ? 0 : NEXT)>(c, c.seq + 1, nb0, nb1); else slot_next<MODE>(c);
        }
      }
      if constexpr (i == FPS - 1) c.seq++;
    } else if constexpr (NEXT >= 0) {
      c.wf<f % 4>() = wread<(NEXT < 0 ? 0 : NEXT), fn - NF>(c);
      if constexpr (fps(NEXT) == 8 && fn - NF == 1) c.fpre = *(volatile LAS v2u*)flg(c, F_FULL);
    }
    if constexpr (fn <= NF && i == 0) { if constexpr ((f >= RELF || fn / FPS - 1 < EARLY) && fn / FPS - 1 < CAP) ring_publish(c); }
    else if constexpr (f == RELF && RELF > 0) ring_publish(c);
    side(F);
    SB();
  });
}
template <int NF, int TPS, int MODE, int NEXT, bool XA, int ZERO, int RELF, int EARLY, int CAP, int TF = -1, class SIDE>
__device__ __forceinline__ void gemm(Ctx& c, v4u (&a)[32], f32x16& c0, f32x16& c1, int lb0, int lb1, int nb0, int nb1, SIDE side) {
  gemm4<NF, TPS, MODE, NEXT, XA, ZERO, RELF, EARLY, CAP, TF>(c, a, c0, c1, c0, c1, lb0, lb1, nb0, nb1, side);
}
__device__ __forceinline__ unsigned sum4(unsigned m, v4u v) { return ((m + v.x + v.y) + v.z + v.w); }
template <bool EX> __device__ __forceinline__ void ld_chk(Ctx& c, v2u& k, int sb) {
  if (EX) k = __builtin_amdgcn_raw_buffer_load_b64(c.rs, c.lane * 8 + 2 * KB, sb, 16);
  else k.x = __builtin_amdgcn_raw_buffer_load_b32(c.rs, c.lane * 4 + 2 * KB, sb, 16);
}
template <int STR, bool EX> __device__ __forceinline__ void ld_rec(Ctx& c, v4u (&a)[32], v2u (&k)[16], int lv, int sb, int p, int e) {
  const int so = sb + STR * p * REC;
  if (e < 2) a[(2 * p + e) & 31] = ld_ws(c, lv + e * KB, so);
  else ld_chk<EX>(c, k[p & 15], so);
}
template <bool EX> __device__ __forceinline__ unsigned rec_bad(v4u (&a)[32], v2u (&k)[16], int p, unsigned tag) {
  return sum4(sum4(EX ? k[p & 15].x + k[p & 15].y : k[p & 15].x, a[(2 * p) & 31]), a[(2 * p + 1) & 31]) ^ tag;
}
template <int N, int STR, bool EX> __device__ __forceinline__ void settle(Ctx& c, unsigned bad, v4u (&a)[32], v2u (&k)[16], int lv, int sb, int p0, unsigned tag) {
  if (__builtin_expect(__any(bad != 0u), 0)) {
    unsigned sp = 0;
#pragma nounroll
    do {
      __builtin_amdgcn_s_sleep(BACKOFF);
      MEMBAR();
      bad = 0;
#pragma unroll
      for (int i = 0; i < 3 * N; ++i) ld_rec<STR, EX>(c, a, k, lv, sb, p0 + i / 3, i % 3);
#pragma unroll
      for (int i = 0; i < N; ++i) bad |= rec_bad<EX>(a, k, p0 + i, tag);
    } while (__any(bad != 0u) && ++sp < SPIN_CAP);
  }
}
template <int F, int NP, int STR, bool EX>
__device__ __forceinline__ void check_step(Ctx& c, unsigned& m0, unsigned& m1, v4u (&a)[32], v2u (&k)[16], int lv, int sb, unsigned tag) {
  constexpr int p = 2 * ((F + 8) / 8), r = (F + 8) % 8;
  if constexpr (p < NP) {
    if constexpr (r == 2) { m0 = sum4(EX ? k[p & 15].x + k[p & 15].y : k[p & 15].x, a[(2 * p) & 31]); PIN(m0); }
    if constexpr (r == 3) { m0 = sum4(m0, a[(2 * p + 1) & 31]); PIN(m0); }
    if constexpr (r == 4) { m1 = sum4(EX ? k[(p + 1) & 15].x + k[(p + 1) & 15].y : k[(p + 1) & 15].x, a[(2 * p + 2) & 31]); PIN(m1); }
    if constexpr (r == 5) { m1 = sum4(m1, a[(2 * p + 3) & 31]); PIN(m1); }
    if constexpr (r == 6) { m0 = (m0 ^ tag) | (m1 ^ tag); PIN(m0); }
    if constexpr (r == 7) settle<2, STR, EX>(c, m0, a, k, lv, sb, p, tag);
  }
}
template <int STR, bool EX> __device__ __forceinline__ void open_phase(Ctx& c, v4u (&a)[32], v2u (&k)[16], int lv, int sb, unsigned tag) {
  VMWAIT(0);
  __builtin_amdgcn_s_sleep(XD);
#pragma unroll
  for (int i = 0; i < 12; ++i) ld_rec<STR, EX>(c, a, k, lv, sb, i / 3, i % 3);
  SB();
  settle<2, STR, EX>(c, rec_bad<EX>(a, k, 0, tag) | rec_bad<EX>(a, k, 1, tag), a, k, lv, sb, 0, tag);
}
template <int F, int P0, int P1, int STR, bool EX> __device__ __forceinline__ void req_step(Ctx& c, v4u (&a)[32], v2u (&k)[16], int lv, int sb) {
  if constexpr (F >= 0 && P0 + F / 2 < P1) {
    ld_rec<STR, EX>(c, a, k, lv, sb, P0 + F / 2, F & 1);
    if constexpr (F & 1) ld_rec<STR, EX>(c, a, k, lv, sb, P0 + F / 2, 2);
  }
}
template <int G, int PER, int P0, int P1, int STR, bool EX> __device__ __forceinline__ void req_pace(Ctx& c, v4u (&a)[32], v2u (&k)[16], int lv, int sb) {
  if constexpr (G >= 0 && P0 + G / PER < P1) {
    constexpr int r = G % PER, D = PER / 3;
    if constexpr (r % D == 0 && r / D < 3) ld_rec<STR, EX>(c, a, k, lv, sb, P0 + G / PER, r / D);
  }
}
template <bool EX> __device__ __forceinline__ void st_chk(Ctx& c, unsigned sum, unsigned ex, int sb, unsigned tag) {
  if (EX) __builtin_amdgcn_raw_buffer_store_b64(v2u{tag - sum - ex, ex}, c.rs, c.lane * 8 + 2 * KB, sb, 16);
  else __builtin_amdgcn_raw_buffer_store_b32(tag - sum, c.rs, c.lane * 4 + 2 * KB, sb, 16);
}
__device__ __forceinline__ u64 mix64(u64 z) {
  z += 0x9E3779B97F4A7C15ull;
  z = (z ^ (z >> 30)) * 0xBF58476D1CE4E5B9ull;
  z = (z ^ (z >> 27)) * 0x94D049BB133111EBull;
  return z ^ (z >> 31);
}
__device__ __forceinline__ float csum(v4u f, float s) { return dot2(f.w, ONES2, dot2(f.z, ONES2, dot2(f.y, ONES2, dot2(f.x, ONES2, s)))); }
__device__ __forceinline__ v4u cs_operand(float s) { unsigned hi, lo; split2(s, hi, lo); return v4u{hi | (hi << 16), lo, 0u, 0u}; }

__global__ __launch_bounds__(512, 2) void fused_fwd(const u16* __restrict__ x, const u16* __restrict__ wqkv, const u16* __restrict__ wo,
                                                    const u16* __restrict__ wg, const u16* __restrict__ wu, const u16* __restrict__ wd,
                                                    u16* __restrict__ out, char* __restrict__ ws) {
  __shared__ __attribute__((aligned(16))) char smem[L_END];
  u64 pc0;
  asm volatile("s_getpc_b64 %0" : "=s"(pc0));
  Ctx c;
  c.L = (LAS char*)smem;
  c.rs = __builtin_amdgcn_make_buffer_rsrc((void*)ws, 0, WS_END, 0x00020000);
  const int wave = __builtin_amdgcn_readfirstlane(threadIdx.x >> 6);
  const int b = blockIdx.x, xc = b & 7, j = b >> 3;
  const int cl = (xc >> 1) * 4 + (j >> 3), m = (xc & 1) * 8 + (j & 7);
  const int rh = m >> 3, cg = m & 7;
  const int g0 = gsplit(m), ng = gsplit(m + 1) - g0;
  c.lane = 0; c.wave = wave; c.seq = 0; c.known = 0; c.vb0 = 0; c.vb1 = 0; c.nb0 = 0; c.nb1 = 0; c.fa = 0; c.fpre = v2u{0u, 0u};
  const int rot = (2 * cl + m) & 7;
  LAS char* L = c.L;
  if (threadIdx.x < 32) *flg(c, threadIdx.x) = 0u;
  asm volatile("s_waitcnt lgkmcnt(0)\n\ts_barrier" ::: "memory");
#define LANEVARS                                                                                                              \
  const int lane = fresh_lane(), h = lane >> 5, l31 = lane & 31, lv = lane * 16;                                               \
  const int trN = (8 * h + ((lane >> 2) & 3)) * 64 + ((lane >> 4) & 1) * 32 + (lane & 3) * 8;                                   \
  const int trS = (8 * h + ((lane >> 2) & 3)) * 64 + ((lane >> 4) & 1) * 32 + (((lane & 1) << 1) | ((lane >> 1) & 1)) * 8;       \
  (void)trN; (void)trS; (void)l31; (void)lv;
  const int pp = wave & 1;
  auto load_half0 = [&](int sl) INL {
    const int lane = fresh_lane();
    const unsigned vo1 = (lane >> 2) * 3072 + (lane & 3) * 16;
    const char* wb = (const char*)wqkv + m * 64 + (sl < 4 ? (1 + pp) * 1024 : 0);
#pragma unroll
    for (int p = 0; p < 8; ++p) {
      const int s = sl < 4 ? sl * 8 + p : (sl - 4) * 16 + 2 * p + pp;
      glds(wb + (size_t)(((s + 4 * rot) & 31) * 16) * 3072, vo1, L_RING + sl * SLOT + p * 2048 + pp * 1024);
    }
  };
  auto cache_load = [&](auto& wc, auto NK) INL {
    const int lane = fresh_lane();
    const __amdgpu_buffer_rsrc_t rd = __builtin_amdgcn_make_buffer_rsrc((void*)wd, 0, 1376 * 1024, 0x00020000);
    const int vo2 = (lane >> 3) * 1024 + (((lane & 7) ^ (((lane >> 4) & 1) << 2)) * 16);
#pragma unroll
    for (int k = 0; k < NK.v; ++k) {
#pragma unroll
      for (int q = 0; q < 2; ++q) {
        const int hs = (NK.v == 5 ? 2 * k : k < 5 ? 2 * k + 1 : 10) * 2 + pp;
        const int p = min(hs * 2 + q, 42);
        const int m3 = min(p - 32 + 1 + ((p - 32) >> 1), 15);
        const int aa = p < 32 ? gsplit(p & 15) + (p >> 4) : gsplit(m3) + 2;
#pragma unroll
        for (int e = 0; e < 4; ++e) wc[k * 8 + q * 4 + e] = __builtin_amdgcn_raw_buffer_load_b128(rd, vo2, aa * 32768 + cg * 128 + e * 8192, 0);
        SB();
        if (NK.v == 5) __builtin_amdgcn_s_sleep(2);
      }
    }
  };
  auto cache_conv = [&](auto& wc, auto NK) INL {
    const int lane = fresh_lane(), h = lane >> 5;
    const int tk = (8 * h + ((lane >> 2) & 3)) * 128 + ((lane >> 4) & 1) * 32 + (((lane & 1) << 1) | ((lane >> 1) & 1)) * 8 + ((lane >> 3) & 1) * 64;
    LAS char* d = L + L_XCH + (wave - 4) * 4096;
    wait_lds<4, 2>(c, F_FREE, 4u);
#pragma unroll
    for (int q = 0; q < 2 * NK.v; ++q) {
#pragma unroll
      for (int e = 0; e < 4; ++e) *(LAS v4u*)(d + lane * 16 + e * KB) = wc[q * 4 + e];
      MEMBAR(); SB();
#pragma unroll
      for (int e = 0; e < 4; ++e) wc[q * 4 + e] = tr_frag<512>(d + (e >> 1) * 2048 + (tk ^ (((e ^ q) & 1) * 64)));
      MEMBAR(); SB();
    }
    *flg(c, F_CONV + wave - 4) = 1u;
    MEMBAR();
  };
  auto cache_put = [&](auto& wc, auto NK, int base) INL {
    const int lane = fresh_lane();
#pragma unroll
    for (int k = 0; k < NK.v; ++k) {
      const int sq = base + (NK.v == 5 ? 2 * k : k < 5 ? 2 * k + 1 : 10);
      wait_lds<4, 4>(c, F_FREE, (unsigned)(sq - NSLOT + 1));
      LAS char* d = L + L_RING + (sq & (NSLOT - 1)) * SLOT + pp * 8192;
#pragma unroll
      for (int e = 0; e < 8; ++e) {
        *(LAS v4u*)(d + lane * 16 + e * KB) = wc[k * 8 + e];
        asm volatile("s_nop 3" ::: "memory");
      }
      wait_lds<1, 2>(c, F_FULL + pp, (unsigned)sq);
      *flg(c, F_FULL + pp) = (unsigned)(sq + 1);
      MEMBAR();
    }
  };

  if (wave < 4) {
    __builtin_amdgcn_s_setprio(2);
    const int w = wave, mh = w >> 1, kh = w & 1;
    c.fa = opqv(L_FLG + (F_FREE + wave) * 4);
    asm volatile("" : "+s"(c.rs));
    v4u a[32], xr0, xr1;
    v2u k[16];
    f32x16 A0, A1, B0, B1;
    LANEVARS
    c.lane = lane;
    float rstd;
    unsigned tagx, tagy, tagc;
    v4u mfrag;
    auto set_stats = [&](float sum, float ssq) INL -> float {
      const float s = sum * (1.f / 512), q = ssq * (1.f / 512);
      unsigned hi, lo;
      split2(-s, hi, lo);
      mfrag = h == 0 ? v4u{hi | (lo << 16), hi, 0u, 0u} : v4u{0u, 0u, 0u, 0u};
      return fmaxf(q - s * s, 0.f) + 1e-5f;
    };
    auto cs_frag = [&](int tile) INL { return *(LAS v4u*)(L + L_CS + tile * 512 + l31 * 16); };
    auto rs_put = [&]() INL { const int lane = fresh_lane(); *(LAS float*)(L + L_KVS + w * 2304 + lane * 4) = h ? rstd * LOG2E : rstd; MEMBAR(); };
    auto rs_get = [&](int q) INL {
      const int h = fresh_lane() >> 5;
      const f32x4 r = *(LAS f32x4*)(L + L_KVS + w * 2304 + (8 * q + 4 * h) * 4), rl = *(LAS f32x4*)(L + L_KVS + w * 2304 + 128 + (8 * q + 4 * h) * 4);
#pragma unroll
      for (int i = 0; i < 4; ++i) { B0[4 * q + i] = r[i]; B1[4 * q + i] = rl[i]; }
    };
    {
      const __amdgpu_buffer_rsrc_t rx = __builtin_amdgcn_make_buffer_rsrc((void*)x, 0, 2048 * 1024, 0x00020000);
      const int xv = (lane >> 3) * 1024 + (lane & 7) * 16, xs = (cl * 128 + w * 32) * 1024;
      auto ldx = [&](int i) INL { a[i] = __builtin_amdgcn_raw_buffer_load_b128(rx, xv, xs + (i & 3) * 8192 + (((i >> 2) + rot) & 7) * 128, 0); };
      __builtin_amdgcn_s_setprio(3);
#pragma unroll
      for (int i = 0; i < 8; ++i) ldx(i);
      __builtin_amdgcn_s_setprio(2);
#pragma unroll
      for (int i = 8; i < 16; ++i) ldx(i);
      SB();
      LAS char* sc = L + L_XCH + w * 4096;
      const int r8 = lane >> 3;
      const int wo_ = r8 * 128 + (((lane & 7) ^ (r8 >> 1)) * 16);
      const int ro = l31 * 128 + ((h ^ ((l31 >> 1) & 7)) * 16);
      auto xstep = [&](int kp, int e) INL {
        MEMBAR();
        if (e < 4) *(LAS v4u*)(sc + ((wo_ + e * 1024) ^ ((e & 1) * 64))) = a[kp * 4 + e];
        else a[kp * 4 + e - 4] = *(LAS v4u*)(sc + (ro ^ ((e - 4) * 32)));
        MEMBAR();
      };
#pragma unroll
      for (int kp = 0; kp < XPRE; ++kp) {
#pragma unroll
        for (int e = 0; e < 8; ++e) xstep(kp, e);
        if (kp < 2) {
#pragma unroll
          for (int i = 16 + 8 * kp; i < 24 + 8 * kp; ++i) ldx(i);
        }
      }
      wpre<2>(c, trN, trS);
      v4u s0, ca, cb;
      const int s0b = WS_S0P + (cl * 128 + w * 32) * 16;
      gemm<64, 2, 2, -1, true, 3, 0, 0, 99>(c, a, A0, A1, trN, trS, 0, 0, [&](auto F) INL {
        constexpr int f = F.v;
        if ((f >> 3) + XPRE < 8) xstep((f >> 3) + XPRE, f & 7);
        if (f == 0) s0 = ld_ws(c, l31 * 16, s0b);
        if (f == 2) {
      const int xq = l31 * 1024 + h * 16, xqs = (cl * 128 + mh * 64 + rh * 32) * 1024 + cg * 128 + kh * 64;
      xr0 = __builtin_amdgcn_raw_buffer_load_b128(rx, xq, xqs, 0);
      xr1 = __builtin_amdgcn_raw_buffer_load_b128(rx, xq + 32, xqs, 0);
        }
        if (f == 40) {
          wait_lds<2, 0>(c, F_CS1R, 1u);
          const v2u tg = *(volatile LAS v2u*)flg(c, F_TAG);
          tagx = __builtin_amdgcn_readfirstlane(tg.x); tagy = __builtin_amdgcn_readfirstlane(tg.y); tagc = (tagx * 0x9E3779B1u + tagy) | 1u;
          if (__any(s0.y != tagx || s0.w != tagy)) {
            unsigned sp = 0;
#pragma nounroll
            do { MEMBAR(); s0 = ld_ws(c, l31 * 16, s0b); } while (__any(s0.y != tagx || s0.w != tagy) && ++sp < SPIN_CAP);
          }
          rstd = __builtin_amdgcn_rsqf(set_stats(u2f(s0.x), u2f(s0.z)));
          rs_put();
          ca = cs_frag(0); cb = cs_frag(1);
        }
        if (f == 45) { A0 = mfma(mfrag, ca, A0); A1 = mfma(mfrag, cb, A1); }
        if (f >= 54 && f < 62 && !(f & 1)) rs_get((f - 54) >> 1);
      });
    }
#pragma unroll 1
    for (int layer = 0; layer < 2; ++layer) {
      bool publisher;
      {
        LANEVARS
#pragma unroll
        for (int i = 0; i < 16; ++i) B1[i] *= A0[i];
        SB();
#pragma unroll
        for (int i = 0; i < 16; ++i) { B1[i] = exp2c(B1[i]); A0[i] = fmaf(A0[i], B0[i], 1.f); A1[i] *= B0[i]; }
        SB();
        v4u pk0, pk1, vv0, vv1;
#pragma unroll
        for (int i = 0; i < 16; ++i) A0[i] = fmaxf(A0[i], B1[i]) + 1e-6f;
#pragma unroll
        for (int i = 0; i < 4; ++i) {
          pk0[i] = pack2(A0[2 * i], A0[2 * i + 1]); vv0[i] = pack2(A1[2 * i], A1[2 * i + 1]);
          pk1[i] = pack2(A0[8 + 2 * i], A0[9 + 2 * i]); vv1[i] = pack2(A1[8 + 2 * i], A1[9 + 2 * i]);
        }
        f32x16 kv = mfma(pk0, vv0, zero16());
        kv = mfma(pk1, vv1, kv);
        const float z = red_h(csum(pk0, 0.f) + csum(pk1, 0.f));
        LAS char* ks = L + L_KVS + w * 2304;
        if (h == 0) *(LAS float*)(ks + 2048 + l31 * 4) = z;
        if (lane & 16) {
          *(LAS f32x4*)(ks + lv) = f32x4{kv[8], kv[9], kv[10], kv[11]};
          *(LAS f32x4*)(ks + 1024 + lv) = f32x4{kv[12], kv[13], kv[14], kv[15]};
          MEMBAR();
        } else {
          MEMBAR();
          *(LAS f32x4*)(ks + lv) = f32x4{kv[0], kv[1], kv[2], kv[3]};
          *(LAS f32x4*)(ks + 1024 + lv) = f32x4{kv[4], kv[5], kv[6], kv[7]};
        }
        MEMBAR();
        *flg(c, F_KFL + w) = (unsigned)(layer + 1);
        const v4u kf = *(volatile LAS v4u*)flg(c, F_KFL);
        MEMBAR();
        f32x4 p0[4], p1[4];
        f32x2 pz[4];
#pragma unroll
        for (int ww = 0; ww < 4; ++ww) {
          p0[ww] = *(LAS f32x4*)(L + L_KVS + ww * 2304 + lv);
          p1[ww] = *(LAS f32x4*)(L + L_KVS + ww * 2304 + 1024 + lv);
          pz[ww] = *(LAS f32x2*)(L + L_KVS + ww * 2304 + 2048 + (lane & 15) * 8);
        }
        MEMBAR();
        SB();
        publisher = (unsigned)__builtin_amdgcn_readfirstlane(umin(umin(kf.x, kf.y), umin(kf.z, kf.w))) > (unsigned)layer;
        if (publisher) {
          const f32x4 k0 = (p0[0] + p0[1]) + (p0[2] + p0[3]), k1 = (p1[0] + p1[1]) + (p1[2] + p1[3]);
          const f32x2 zz = (pz[0] + pz[1]) + (pz[2] + pz[3]);
          const unsigned tg = layer ? tagy : tagx;
          const unsigned tl = tg >> ((lane & 1) * 16), t0 = tl & 0xffu, t1 = (tl >> 8) & 0xffu;
          const int kvb = WS_KVP + ((layer * 16 + m) * 16 + cl) * KVR;
          auto st2 = [&](float u, float v, int vo) INL {
            __builtin_amdgcn_raw_buffer_store_b64(v2u{(f2u(u) & 0xffffff00u) | t0, (f2u(v) & 0xffffff00u) | t1}, c.rs, vo, kvb, 16);
          };
          st2(k0[0], k0[1], lane * 8); st2(k0[2], k0[3], lane * 8 + 512); st2(k1[0], k1[1], lane * 8 + 1024); st2(k1[2], k1[3], lane * 8 + 1536);
          if (lane < 16) st2(zz[0], zz[1], lane * 8 + 2048);
        }
        MEMBAR();
      }
      B0 = mfma(cs_frag(2), mfrag, zero16());
      if (layer == 0) { LANEVARS wpre<3>(c, trN, trN); gemm<32, 1, 3, -1, false, 0, 0, 0, 99>(c, a, B0, B0, trN, trN, 0, 0, NoSide()); }
      else { wpre<0>(c, lv, lv); gemm<32, 1, 0, -1, false, 0, 0, 0, 99>(c, a, B0, B0, lv, lv, 0, 0, NoSide()); }
      const int lq = fresh_lane();
      const int kb0 = (8 * (lq >> 5) + ((lq >> 2) & 3)) * 128 + ((lq >> 4) & 1) * 32 + (((lq & 1) << 1) | ((lq >> 1) & 1)) * 8 + kh * 4096 + ((kh ^ (lq >> 3)) & 1) * 64;
      const int kb1 = kb0 ^ 64;
      auto res_init = [&](f32x16& R, int i) INL { const unsigned u = i < 4 ? xr0[i & 3] : xr1[i & 3]; R[2 * i] = blo(u); R[2 * i + 1] = bhi(u); };
      {
        LANEVARS
        v4u pq0, pq1;
        const float rl = rstd * LOG2E;
#pragma unroll
        for (int i = 0; i < 16; ++i) B1[i] = exp2c(B0[i] * rl);
#pragma unroll
        for (int i = 0; i < 16; ++i) B0[i] = fmaxf(fmaf(B0[i], rstd, 1.f), B1[i]) + 1e-6f;
#pragma unroll
        for (int i = 0; i < 4; ++i) { pq0[i] = pack2(B0[2 * i], B0[2 * i + 1]); pq1[i] = pack2(B0[8 + 2 * i], B0[9 + 2 * i]); }
#pragma unroll
        for (int i = 0; i < 8; ++i) res_init(A0, i);
        PIN(pq0); PIN(pq1); PIN(A0);
        const int ob = opq(WS_O + cl * ACT_CL + (w * 16 + m) * REC);
        {
          const int lane = fresh_lane();
          const unsigned tg = layer ? tagy : tagx;
          const unsigned tl = tg >> ((lane & 1) * 16), t0 = tl & 0xffu, t1 = (tl >> 8) & 0xffu;
          const int kvb = WS_KVP + (layer * 16 + m) * 16 * KVR;
          float s0 = 0.f, s1 = 0.f;
          v2u zp;
          unsigned zpk;
          if (publisher) __builtin_amdgcn_s_sleep(KVDP); else __builtin_amdgcn_s_sleep(KVDELAY);
#pragma nounroll
          for (unsigned sp = 0; sp < SPIN_CAP; ++sp) {
            v2u p[16];
            zp = __builtin_amdgcn_raw_buffer_load_b64(c.rs, (lane >> 2) * KVR + (lane & 3) * 8 + 2048, kvb + w * 32, 16);
#pragma unroll
            for (int i = 0; i < 16; ++i) p[i] = __builtin_amdgcn_raw_buffer_load_b64(c.rs, lane * 8, kvb + w * 512 + i * KVR, 16);
            SB();
            zp.x ^= t0; zp.y ^= t1;
            unsigned chk = zp.x | zp.y;
            float za = u2f(zp.x), zb = u2f(zp.y);
            za += dpp<0x128>(za); zb += dpp<0x128>(zb); za += dpp<0x124>(za); zb += dpp<0x124>(zb);
            zpk = pack2(red_g(za), red_g(zb));
            PIN(zpk);
#pragma unroll
            for (int i = 0; i < 16; ++i) {
              p[i].x ^= t0; p[i].y ^= t1; chk |= p[i].x | p[i].y;
              s0 = i ? s0 + u2f(p[i].x) : u2f(p[i].x); s1 = i ? s1 + u2f(p[i].y) : u2f(p[i].y);
              PIN(s0); PIN(s1);
            }
            if (!__any((chk & 0xffu) != 0u)) break;
            __builtin_amdgcn_s_sleep(BACKOFF);
            MEMBAR();
          }
          *(LAS unsigned*)(L + L_KVF + lv + w * 4) = pack2(s0, s1);
          if (lane < 4) *(LAS unsigned*)(L + L_ZF + w * 16 + lane * 4) = zpk;
          MEMBAR();
          *flg(c, F_KVR + w) = (unsigned)(layer + 1);
        }
        v4u kvf;
        v2u z0, z1, z2, z3;
#pragma nounroll
        for (unsigned sp = 0; sp < SPIN_CAP; ++sp) {
          const v4u fl = *(volatile LAS v4u*)flg(c, F_KVR);
          MEMBAR();
          kvf = *(LAS v4u*)(L + L_KVF + lv);
          z0 = *(LAS v2u*)(L + L_ZF + h * 8); z1 = *(LAS v2u*)(L + L_ZF + 16 + h * 8);
          z2 = *(LAS v2u*)(L + L_ZF + 32 + h * 8); z3 = *(LAS v2u*)(L + L_ZF + 48 + h * 8);
          MEMBAR();
          if ((unsigned)__builtin_amdgcn_readfirstlane(umin(umin(fl.x, fl.y), umin(fl.z, fl.w))) > (unsigned)layer) break;
        }
        const bool up = (lane & 16) != 0;
        const v4u zz = {0u, 0u, 0u, 0u};
        B0 = mfma(up ? zz : kvf, pq0, zero16());
        B0 = mfma(up ? kvf : zz, pq1, B0);
        const float d0 = dot2(pq0.y, z0.y, dot2(pq0.x, z0.x, 0.f)) + dot2(pq0.w, z1.y, dot2(pq0.z, z1.x, 0.f));
        const float d1 = dot2(pq1.y, z2.y, dot2(pq1.x, z2.x, 0.f)) + dot2(pq1.w, z3.y, dot2(pq1.z, z3.x, 0.f));
        const float r0 = __builtin_amdgcn_rcpf(red_h(d0) + 1e-6f), r1 = __builtin_amdgcn_rcpf(red_h(d1) + 1e-6f);
        v4u o0, o1;
#pragma unroll
        for (int i = 0; i < 4; ++i) { o0[i] = pack2(B0[2 * i] * r0, B0[2 * i + 1] * r0); o1[i] = pack2(B0[8 + 2 * i] * r1, B0[9 + 2 * i] * r1); }
        st_ws(c, o0, lv, ob);
        st_ws(c, o1, lv + KB, ob);
        st_chk<false>(c, sum4(sum4(0u, o0), o1), 0u, ob, tagc);
      }
      const int rtA = opq(mh * 2 + rh), clb = opq(cl * ACT_CL);
      auto res_send = [&](f32x16& A1, int q, unsigned xseq) INL {
        LAS char* xs = L + L_XCH + w * 4096 + lv;
        *(LAS f32x4*)(xs + q * 2048) = f32x4{A1[8 * q], A1[8 * q + 1], A1[8 * q + 2], A1[8 * q + 3]};
        *(LAS f32x4*)(xs + q * 2048 + 1024) = f32x4{A1[8 * q + 4], A1[8 * q + 5], A1[8 * q + 6], A1[8 * q + 7]};
        MEMBAR();
        if (q) *flg(c, F_XF + w) = xseq;
      };
      auto res_epilogue = [&](f32x16& A0, int xi, bool final_out, unsigned xseq) INL {
        LAS char* xs = L + L_XCH;
        const int lv = fresh_lane() * 16;
        f32x4 o0, o1, o2, o3;
#pragma nounroll
        for (unsigned sp = 0; sp < SPIN_CAP; ++sp) {
          const unsigned fl = *flg(c, F_XF + (w ^ 1));
          MEMBAR();
          o0 = *(LAS f32x4*)(xs + (w ^ 1) * 4096 + lv); o1 = *(LAS f32x4*)(xs + (w ^ 1) * 4096 + 1024 + lv);
          o2 = *(LAS f32x4*)(xs + (w ^ 1) * 4096 + 2048 + lv); o3 = *(LAS f32x4*)(xs + (w ^ 1) * 4096 + 3072 + lv);
          MEMBAR();
          if ((unsigned)__builtin_amdgcn_readfirstlane(fl) >= xseq) break;
        }
        xr0 = v4u{pack2(A0[0] + o0[0], A0[1] + o0[1]), pack2(A0[2] + o0[2], A0[3] + o0[3]), pack2(A0[4] + o1[0], A0[5] + o1[1]), pack2(A0[6] + o1[2], A0[7] + o1[3])};
        xr1 = v4u{pack2(A0[8] + o2[0], A0[9] + o2[1]), pack2(A0[10] + o2[2], A0[11] + o2[3]), pack2(A0[12] + o3[0], A0[13] + o3[1]), pack2(A0[14] + o3[2], A0[15] + o3[3])};
        if (final_out) {
          const int lo = fresh_lane();
          GAS char* ob = (GAS char*)out + (size_t)((cl * 4 + rtA) * 32 + (lo & 31)) * 1024 + cg * 128 + kh * 64 + (lo >> 5) * 16;
          *(GAS v4u*)ob = xr0;
          *(GAS v4u*)(ob + 32) = xr1;
        } else {
          const int xb = WS_X + clb + ((xi * 4 + rtA) * 16 + cg * 2 + kh) * REC;
          st_ws(c, xr0, lv, xb);
          st_ws(c, xr1, lv + KB, xb);
          const float s = csum(xr0, 0.f) + csum(xr1, 0.f);
          float q0 = 0.f, q1 = 0.f;
#pragma unroll
          for (int i = 0; i < 4; ++i) { q0 = dot2(xr0[i], xr0[i], q0); q1 = dot2(xr1[i], xr1[i], q1); }
          const auto sw = __builtin_amdgcn_permlane32_swap(f2u(s), f2u(q0 + q1), false, false);
          st_chk<true>(c, sum4(sum4(0u, xr0), xr1), f2u(u2f((unsigned)sw[0]) + u2f((unsigned)sw[1])), xb, tagc);
        }
      };
      unsigned m0, m1;
      {
        const int ab = WS_O + clb + (rtA * 16 + kh) * REC;
        wpre<1>(c, kb0, kb1);
        if (mh) __builtin_amdgcn_s_sleep(YOFF);
        open_phase<2, false>(c, a, k, lv, ab, tagc);
        gemm<32, 2, 1, -1, false, 2, 8, 0, 99, 1>(c, a, A0, A1, kb0, kb1, 0, 0, [&](auto F) INL {
          if (F.v == 24 && layer == 0) wait_lds<2, 0>(c, F_CONV + 2, 1u);
          if constexpr (F.v == 29 || F.v == 30) res_send(A1, F.v - 29, (unsigned)(layer * 0x200 + 1));
          req_step<F.v, 4, 8, 2, false>(c, a, k, lv, ab);
          check_step<F.v, 8, 2, false>(c, m0, m1, a, k, lv, ab, tagc);
        });
        res_epilogue(A0, layer, false, (unsigned)(layer * 0x200 + 1));
      }
      *flg(c, F_XF + w) = (unsigned)(layer * 0x200 + 0x100);
      *(LAS v4u*)(L + L_KVS + w * 2304 + lv) = xr0;
      *(LAS v4u*)(L + L_KVS + w * 2304 + 1024 + lv) = xr1;
      MEMBAR();
      auto res_back = [&]() INL { xr0 = *(LAS v4u*)(L + L_KVS + w * 2304 + lv); xr1 = *(LAS v4u*)(L + L_KVS + w * 2304 + 1024 + lv); };
      wpre<0>(c, lv, lv);
      v4u cs0;
      float sacc;
      const int ab4 = WS_H + clb + (rtA * 43 + kh) * REC;
      const int hb = WS_H + clb + w * 43 * REC;
      float r2;
      v4u ho;
      unsigned hs;
      float h0, h1;
      auto hside = [&](auto F, f32x16& G, f32x16& U, int dst) INL {
        constexpr int f = F.v, sl = (f - 1) / 2;
        if constexpr (f >= 1 && f < 17) {
          if constexpr (f & 1) { h0 = relu(G[2 * sl]) * r2; PIN(h0); h1 = relu(G[2 * sl + 1]) * r2; PIN(h1); }
          else {
            h0 *= U[2 * sl]; PIN(h0); h1 *= U[2 * sl + 1]; PIN(h1);
            ho[sl & 3] = pack2(h0, h1); PIN(ho);
          }
        }
        if constexpr (f == 9 || f == 17) { st_ws(c, ho, lv + (f == 17) * KB, dst); hs = sum4(f == 9 ? 0u : hs, ho); PIN(hs); }
        if constexpr (f == 18) st_chk<false>(c, hs, 0u, dst, tagc);
      };
      auto corr = [&](auto F, int at, f32x16& G, f32x16& U, int gi) INL {
        if (F.v == at - 6) cs0 = cs_frag(3 + 2 * gi);
        if (F.v == at - 3) { G = mfma(cs0, mfrag, G); SB(); cs0 = cs_frag(4 + 2 * gi); }
        if (F.v == at) U = mfma(cs0, mfrag, U);
      };
      const int xb3 = WS_X + clb + (layer * 4 + w) * 16 * REC;
      open_phase<1, true>(c, a, k, lv, xb3, tagc);
      gemm4<128, 2, 0, 0, false, 3, 62, 0, 99, -1>(c, a, A0, A1, B0, B1, lv, lv, lv, lv, [&](auto F) INL {
        constexpr int f = F.v, u = f / 8;
        if constexpr (f == 0) { sacc = u2f(k[0].y) + u2f(k[1].y); PIN(sacc); }
        req_pace<f, 6, 4, 16, 1, true>(c, a, k, lv, xb3);
        sfor<8>([&](auto CH) INL {
          if constexpr (CH.v > 0 && dual_first(CH.v) == u + 1) check_step<8 * (CH.v - 1) + f % 8, 16, 1, true>(c, m0, m1, a, k, lv, xb3, tagc);
          if constexpr (CH.v > 0 && f == 8 * dual_first(CH.v)) { sacc += u2f(k[2 * CH.v].y) + u2f(k[2 * CH.v + 1].y); PIN(sacc); }
        });
        if constexpr (f == 89) {
          const auto sw = __builtin_amdgcn_permlane32_swap(f2u(sacc), f2u(sacc), false, false);
          r2 = __builtin_amdgcn_rcpf(set_stats(u2f((unsigned)sw[0]), u2f((unsigned)sw[1])));
          PIN(mfrag); PIN(r2);
        }
        if (f == 86 && layer == 0) wait_lds<1, 0>(c, F_CS3R, 1u);
        corr(F, 95, A0, A1, 0);
        if constexpr (f >= 96) hside(IC<f - 96>{}, A0, A1, hb + m * REC);
        corr(F, 123, B0, B1, 1);
      });
      const int tb0 = fresh_lane() * 16 + kh * 4096;
      if (ng == 3) {
        gemm<64, 2, 0, 4, false, 3, 0, 0, 99>(c, a, A0, A1, lv, lv, tb0, tb0, [&](auto F) INL {
          constexpr int f = F.v;
          hside(F, B0, B1, hb + (16 + m) * REC);
          corr(F, 47, A0, A1, 2);
          if constexpr (f == 19) res_back();
          if constexpr (f >= 23 && f < 31) { res_init(B0, f - 23); PIN(B0); }
          req_pace<f - 32, 4, 0, 8, 2, false>(c, a, k, lv, ab4);
          if constexpr (f >= 56) check_step<f - 64, 22, 2, false>(c, m0, m1, a, k, lv, ab4, tagc);
        });
      }
      if (opq(ng) == 2) {
        const unsigned tg0 = (unsigned)__builtin_amdgcn_s_memrealtime();
        sfor<20>([&](auto F) INL { hside(F, B0, B1, hb + (16 + m) * REC); });
        res_back();
#pragma unroll
        for (int i = 0; i < 8; ++i) res_init(B0, i);
        while ((unsigned)__builtin_amdgcn_s_memrealtime() - tg0 < (unsigned)NG2S) __builtin_amdgcn_s_sleep(2);
        sfor<16>([&](auto F) INL { req_step<F.v, 0, 8, 2, false>(c, a, k, lv, ab4); SB(); });
        wpre<4>(c, tb0, tb0);
        settle<2, 2, false>(c, rec_bad<false>(a, k, 0, tagc) | rec_bad<false>(a, k, 1, tagc), a, k, lv, ab4, 0, tagc);
        while ((unsigned)__builtin_amdgcn_s_memrealtime() - tg0 < (unsigned)NG2T) __builtin_amdgcn_s_sleep(2);
      }
      {
        const int h2 = ng == 3 ? hb + (32 + g0 - 2 * m) * REC : WS_END;
        const int ab21 = kh ? WS_END : ab4;
        const unsigned t21 = kh ? 0u : tagc;
        auto side4 = [&](auto F) INL {
          constexpr int f = F.v;
          if constexpr (f < 10) { hside(IC<2 * f>{}, A0, A1, h2); hside(IC<2 * f + 1>{}, A0, A1, h2); }
          req_pace<f, 4, 8, 16, 2, false>(c, a, k, lv, ab4);
          req_pace<f - S2H, 3, 16, 21, 2, false>(c, a, k, lv, ab4);
          req_pace<f - S2H - 15, 3, 21, 22, 2, false>(c, a, k, lv, ab21);
          if constexpr (f < 72) check_step<f, 22, 2, false>(c, m0, m1, a, k, lv, ab4, tagc);
          if constexpr (f == 74) settle<1, 2, false>(c, rec_bad<false>(a, k, 20, tagc), a, k, lv, ab4, 20, tagc);
          if constexpr (f == 78) settle<1, 2, false>(c, rec_bad<false>(a, k, 21, t21), a, k, lv, ab21, 21, t21);
          if constexpr (f == 85 || f == 86) res_send(B1, f - 85, (unsigned)(layer * 0x200 + 0x101));
        };
        gemm<88, 2, 4, -1, false, 2, 32, 3, 7, 1>(c, a, B0, B1, tb0, tb0, 0, 0, side4);
        tagc += 0x632BE5AAu;
        res_epilogue(B0, 0, layer == 1, (unsigned)(layer * 0x200 + 0x101));
      }
      if (layer == 1) break;
      wpre<0>(c, lv, lv);
      const int xb1 = WS_X + clb + w * 16 * REC;
      open_phase<1, true>(c, a, k, lv, xb1, tagc);
      gemm<64, 2, 0, -1, true, 3, 34, 0, 99>(c, a, A0, A1, lv, lv, 0, 0, [&](auto F) INL {
        constexpr int f = F.v;
        if constexpr (f == 0) { sacc = u2f(k[0].y) + u2f(k[1].y); PIN(sacc); }
        req_step<f, 4, 10, 1, true>(c, a, k, lv, xb1);
        req_pace<f - 12, 4, 10, 16, 1, true>(c, a, k, lv, xb1);
        check_step<f, 16, 1, true>(c, m0, m1, a, k, lv, xb1, tagc);
        if constexpr ((f & 7) == 0 && f >= 8 && f <= 56) { sacc += u2f(k[f / 4].y) + u2f(k[f / 4 + 1].y); PIN(sacc); }
        if constexpr (f == 57) {
          const auto sw = __builtin_amdgcn_permlane32_swap(f2u(sacc), f2u(sacc), false, false);
          rstd = __builtin_amdgcn_rsqf(set_stats(u2f((unsigned)sw[0]), u2f((unsigned)sw[1])));
          PIN(mfrag);
          rs_put();
        }
        if (f == 56) cs0 = cs_frag(0);
        if (f == 60) { A0 = mfma(mfrag, cs0, A0); SB(); cs0 = cs_frag(1); }
        if (f == 63) A1 = mfma(mfrag, cs0, A1);
        if (f >= 59 && f < 63) rs_get(f - 59);
      });
      tagc += 0x632BE5AAu;
    }
  } else if (wave < 6) {
    const int lw = wave - 4;
    LANEVARS
    c.lane = lane;
    volatile LAS unsigned* full = flg(c, F_FULL + lw);
    __builtin_amdgcn_s_setprio(3);
    load_half0(0);
    if (lw == 1) glds((const char*)(pc0 & ~(u64)255) + j * (CODEPF / 32), min(lane * 32, CODEPF / 32 - 32), L_RING + 7 * SLOT + 12288);
    __builtin_amdgcn_s_setprio(2);
    load_half0(1);
    VMWAIT(8); *full = 1u;
    load_half0(2);
    load_half0(3);
    VMWAIT(16); *full = 2u;
    {
      const unsigned tv = min(lane, 42) * 128, td = L_RING + (6 + lw) * SLOT + (lw ? 13312 : 3072);
      glds((const char*)(lw ? wu : wg) + b * 5504, tv, td);
      if (lw) glds((const char*)wd + b * 5504, tv, td);
      else glds((const char*)wo + b * 2048, (lane & 15) * 128, td);
      *flg(c, F_QGO + lw) = 1u;
    }
    VMWAIT(10); *full = 3u;
    VMWAIT(2); *full = 4u;
    __builtin_amdgcn_s_setprio(1);
    load_half0(4);
    load_half0(5);
    VMWAIT(8); *full = 5u;
    VMWAIT(0); *full = 6u;
    int seq = 6, pub = 6;
    v4u wc[48];
    auto stream = [&](int seg, int i0, int i1) INL {
      const int ln = fresh_lane(), lv = ln * 16;
      const unsigned vo2 = (ln >> 3) * 1024 + (((ln & 7) ^ (((ln >> 4) & 1) << 2)) * 16);
      const int src = (seg == 0 ? WS_WT1 + m * 96 * KB : WS_WT3 + g0 * 64 * KB);
#pragma unroll 1
      for (int i = i0; i < i1; ++i) {
        if (seq >= NSLOT) {
          const unsigned need = (unsigned)(seq - NSLOT + 1);
          bool first = true;
#pragma nounroll
          for (unsigned sp = 0; sp < SPIN_CAP; ++sp) {
            v4u f = *(volatile LAS v4u*)flg(c, F_FREE);
            if ((unsigned)__builtin_amdgcn_readfirstlane(umin(umin(f.x, f.y), umin(f.z, f.w))) >= need) break;
            if (first && pub < seq) { VMWAIT(0); pub = seq; *full = (unsigned)pub; }
            first = false;
            __builtin_amdgcn_s_sleep(4);
          }
          MEMBAR();
        }
        const unsigned dst = L_RING + (seq & (NSLOT - 1)) * SLOT + lw * 8192;
        const int hs = i * 2 + lw, hr = (seg == 2 && hs < 16) ? (int)((DUAL >> (4 * hs)) & 15) : hs;
        if (seg == 1) {
          const char* sp = (const char*)wo + (size_t)hr * 65536 + cg * 128;
#pragma unroll
          for (int e = 0; e < 8; ++e) glds(sp + e * 8192, vo2, dst + e * 1024);
        } else {
          glds4(ws + src + hr * 8192, lv, dst);
          glds4(ws + src + hr * 8192 + 4096, lv, dst + 4096);
        }
        ++seq;
        if (seq - pub > 2) { VMWAIT(16); pub = seq - 2; *full = (unsigned)pub; }
      }
    };
    auto drain = [&]() INL { VMWAIT(0); if (pub < seq) { pub = seq; *full = (unsigned)seq; } };
    wait_lds<2, 1>(c, F_JOB, 1u);
    stream(1, 0, 4);
    drain();
    if (lw == 0) { poll_tags<32>(c, WS_F1 + m * 2048, *(volatile LAS v2u*)flg(c, F_TAG)); *flg(c, F_PTOK) = 1u; }
    else wait_lds<1, 1>(c, F_PTOK, 1u);
    stream(2, 0, 4);
    VMWAIT(7);
    cache_load(wc, IC<6>{});
    stream(2, 4, 8);
    drain();
    wait_lds<4, 1>(c, F_XF, 0x100u);
    cache_conv(wc, IC<6>{});
    stream(2, 8, 4 * ng);
    drain();
    cache_put(wc, IC<6>{}, seq);
    seq += 11; pub = seq;
    stream(0, 0, 6);
    stream(1, 0, 4);
    stream(2, 0, 4 * ng);
    drain();
    cache_put(wc, IC<6>{}, seq);
  } else {
    const int hh = wave - 6;
    LANEVARS
    c.lane = lane;
    const int pts = L_RING + (6 + hh) * SLOT;
    __builtin_amdgcn_s_setprio(3);
    {
      const unsigned vo1 = (lane >> 2) * 3072 + (lane & 3) * 16;
      const char* j0 = (const char*)wqkv + (size_t)(cl * 32) * 3072 + m * 64;
      glds(j0 + (1 + hh) * 1024, vo1, pts); glds(j0 + (1 + hh) * 1024 + 16 * 3072, vo1, pts + 1024);
      glds(j0 + hh * 16 * 3072, vo1, pts + 2048);
    }
    const int srow = cl * 128 + m * 8 + hh * 4 + (lane >> 4);
    v4u xs[4];
#pragma unroll
    for (int i = 0; i < 4; ++i) xs[i] = *(const GAS v4u*)((const GAS char*)x + (size_t)srow * 1024 + i * 256 + (lane & 15) * 16);
    const v2u e0 = __builtin_amdgcn_raw_buffer_load_b64(c.rs, 0, WS_E, 16);
    VMWAIT(0);
    const u64 tag = mix64(((u64)(unsigned)__builtin_amdgcn_readfirstlane(e0.y) << 32) | (unsigned)__builtin_amdgcn_readfirstlane(e0.x));
    const v2u tag2 = {(unsigned)tag, (unsigned)(tag >> 32)};
    if (hh == 0) *(volatile LAS v2u*)flg(c, F_TAG) = tag2;
    {
      const unsigned tb = ((unsigned)(mix64(tag) >> 32) >> ((lane & 3) * 8)) & 0xffu;
      const v4u f0 = tr_frag(L + pts + (hh ? trS : trN)), f1 = tr_frag(L + pts + 1024 + (hh ? trS : trN)), f2 = tr_frag(L + pts + 2048 + trN);
      const float s0 = red_h(csum(f1, csum(f0, 0.f))), s2 = red_h(csum(f2, 0.f));
      __builtin_amdgcn_raw_buffer_store_b32((f2u(h ? s2 : s0) & 0xffffff00u) | tb, c.rs, l31 * 4 + h * 4096, WS_C1P + m * 8 * KB + cl * 256 + hh * 128, 16);
      float sa = 0.f, sb = 0.f, qa = 0.f, qb = 0.f;
#pragma unroll
      for (int i = 0; i < 4; ++i) {
        sa = dot2(xs[i].y, ONES2, dot2(xs[i].x, ONES2, sa)); sb = dot2(xs[i].w, ONES2, dot2(xs[i].z, ONES2, sb));
        qa = dot2(xs[i].y, xs[i].y, dot2(xs[i].x, xs[i].x, qa)); qb = dot2(xs[i].w, xs[i].w, dot2(xs[i].z, xs[i].z, qb));
      }
      sa += sb; qa += qb;
      sa += dpp<0x128>(sa); qa += dpp<0x128>(qa); sa += dpp<0x124>(sa); qa += dpp<0x124>(qa);
      sa += dpp<0x122>(sa); qa += dpp<0x122>(qa); sa += dpp<0x121>(sa); qa += dpp<0x121>(qa);
      if ((lane & 15) == 0) st_ws(c, v4u{f2u(sa), tag2.x, f2u(qa), tag2.y}, srow * 16, WS_S0P);
      {
        const unsigned nt = ~(hh ? tag2.y : tag2.x);
        const v4u W = {nt, nt >> 8, nt >> 16, nt >> 24};
        const int kvb = WS_KVP + ((hh * 16 + m) * 16 + cl) * KVR;
        st_ws(c, W, lv, kvb); st_ws(c, W, lv + KB, kvb);
        if (lane < 8) st_ws(c, W, lv + 2 * KB, kvb);
      }
      VMWAIT(0);
      __builtin_amdgcn_s_sleep(8);
      {
        float t;
#pragma nounroll
        for (unsigned sp = 0; sp < SPIN_CAP; ++sp) {
          unsigned p[16];
#pragma unroll
          for (int i = 0; i < 16; ++i) p[i] = __builtin_amdgcn_raw_buffer_load_b32(c.rs, lane * 4 + i * 256, WS_C1P + m * 8 * KB + hh * 4096, 16);
          SB();
          unsigned chk = 0;
          t = 0.f;
#pragma unroll
          for (int i = 0; i < 16; ++i) { p[i] ^= tb; chk |= p[i]; t += u2f(p[i]); }
          if (!__any((chk & 0xffu) != 0u)) break;
          MEMBAR();
        }
        if (hh) t = red_h(t);
        if (hh == 0 || h == 0) *(LAS v4u*)(L + L_CS + hh * 1024 + lv) = cs_operand(t);
        MEMBAR();
        *flg(c, F_CS1R + hh) = 1u;
      }
      __builtin_amdgcn_s_setprio(1);
      wait_lds<2, 1>(c, F_QGO, 1u);
      const int pt3 = pts + 4096;
      const unsigned vo3 = (lane >> 2) * 2752 + (lane & 3) * 16;
      const char* w3 = (const char*)(hh ? wu : wg) + (size_t)(cl * 32) * 2752;
#pragma unroll
      for (int i = 0; i < 3; ++i) {
        const int aa = g0 + min(i, ng - 1);
        glds(w3 + aa * 64, vo3, pt3 + i * 2048);
        glds(w3 + aa * 64 + 16 * 2752, vo3, pt3 + i * 2048 + 1024);
      }
      st_ws(c, f0, lv, WS_WT1 + m * 96 * KB + (cl * 4 + hh) * KB);
      st_ws(c, f1, lv, WS_WT1 + m * 96 * KB + (cl * 4 + 2 + hh) * KB);
      st_ws(c, f2, lv, WS_WT1 + m * 96 * KB + (64 + cl * 2 + hh) * KB);
      VMWAIT(0);
#pragma unroll
      for (int i = 0; i < 3; ++i) {
        const int aa = g0 + min(i, ng - 1);
        const v4u g0f = tr_frag(L + pt3 + i * 2048 + trS), g1f = tr_frag(L + pt3 + i * 2048 + 1024 + trS);
        st_ws(c, g0f, lv, WS_WT3 + aa * 64 * KB + (cl * 4 + hh) * KB);
        st_ws(c, g1f, lv, WS_WT3 + aa * 64 * KB + (cl * 4 + 2 + hh) * KB);
        const float s = red_h(csum(g1f, csum(g0f, 0.f)));
        if (h == 0) __builtin_amdgcn_raw_buffer_store_b32(f2u(s), c.rs, l31 * 4, WS_CSP + aa * 4 * KB + cl * 256 + hh * 128, 16);
      }
      MEMBAR();
      *flg(c, F_JOB + hh) = 1u;
    }
    VMWAIT(0);
    if (lane == 0) __builtin_amdgcn_raw_buffer_store_b64(tag2, c.rs, 0, WS_F1 + ((m * 16 + cl) * 2 + hh) * 64, 16);
    v4u wc[40];
    cache_load(wc, IC<5>{});
    cache_conv(wc, IC<5>{});
    if (hh == 1) {
      wait_lds<1, 2>(c, F_PTOK, 1u);
#pragma unroll 1
      for (int i = 0; i < ng; ++i) {
        float t = 0.f;
#pragma unroll
        for (int q = 0; q < 16; ++q) t += u2f(__builtin_amdgcn_raw_buffer_load_b32(c.rs, lane * 4 + q * 256, WS_CSP + (g0 + i) * 4 * KB, 16));
        *(LAS v4u*)(L + L_CS + (3 + 2 * i) * 512 + lv) = cs_operand(t);
      }
      MEMBAR();
      *flg(c, F_CS3R) = 1u;
      MEMBAR();
    }
    __builtin_amdgcn_s_setprio(1);
    cache_put(wc, IC<5>{}, 10 + 4 * ng);
    if (b == 0 && wave == 6 && lane == 0) __hip_atomic_store((GAS u64*)(ws + WS_E), tag, RLX_AGENT);
    __builtin_amdgcn_s_setprio(1);
    cache_put(wc, IC<5>{}, 31 + 8 * ng);
  }
}

extern "C" void kernel_launch(void* const* d_in, const int* in_sizes, int n_in, void* d_out, int out_size, void* d_ws, size_t ws_size,
                              hipStream_t stream) {
  fused_fwd<<<256, 512, 0, stream>>>((const u16*)d_in[0], (const u16*)d_in[1], (const u16*)d_in[2], (const u16*)d_in[4], (const u16*)d_in[5],
                                     (const u16*)d_in[6], (u16*)d_out, (char*)d_ws);
}
```

```cpp
#include <hip/hip_runtime.h>
#include <stdint.h>
#include <utility>

#define GAS __attribute__((address_space(1)))
#define LAS __attribute__((address_space(3)))
#define RLX_AGENT __ATOMIC_RELAXED, __HIP_MEMORY_SCOPE_AGENT
typedef unsigned v4u __attribute__((ext_vector_type(4)));
typedef unsigned v2u __attribute__((ext_vector_type(2)));
typedef float f32x2 __attribute__((ext_vector_type(2)));
typedef float f32x4 __attribute__((ext_vector_type(4)));
typedef float f32x16 __attribute__((ext_vector_type(16)));
typedef short s16x8 __attribute__((ext_vector_type(8)));
typedef short s16x4 __attribute__((ext_vector_type(4)));
typedef __bf16 bf16x2 __attribute__((ext_vector_type(2)));
typedef unsigned short u16;
typedef unsigned long long u64;

constexpr int XD = 12;
constexpr int YOFF = 14;
constexpr int KVDELAY = 18;
constexpr int KVDP = 14;
constexpr int BACKOFF = 4;
constexpr int NG2S = 90;
constexpr int NG2T = 150;
constexpr int S2H = 44;
constexpr int XPRE = 5;
constexpr int CODEPF = 59392;

constexpr int KB = 1024;
constexpr int WS_E = 0;
constexpr int WS_F1 = 4 * KB;
constexpr int WS_WT1 = 128 * KB;
constexpr int WS_WT3 = WS_WT1 + 16 * 96 * KB;
constexpr int WS_CSP = WS_WT3 + 43 * 64 * KB;
constexpr int WS_S0P = WS_CSP + 43 * 4 * KB;
constexpr int WS_C1P = WS_S0P + 32 * KB;
constexpr int KVR = 2176;
constexpr int WS_KVP = WS_C1P + 16 * 8 * KB;
constexpr int REC = 2560;
constexpr int WS_ACT = 6 * KB * KB, ACT_CL = KB * KB;
constexpr int WS_O = WS_ACT;
constexpr int WS_X = WS_O + 4 * 16 * REC;
constexpr int WS_H = WS_X + 2 * 4 * 16 * REC;
static_assert(WS_KVP + 512 * KVR <= WS_ACT && WS_H + 4 * 43 * REC <= WS_ACT + ACT_CL, "ws");
constexpr int WS_END = WS_ACT + 16 * ACT_CL;

constexpr int SLOT = 16384, NSLOT = 8;
constexpr int L_FLG = 0;
constexpr int L_ZF = L_FLG + 128;
constexpr int L_CS = L_ZF + 64;
constexpr int L_KVF = L_CS + 9 * 512;
constexpr int L_KVS = L_KVF + 1024 + 1152;
constexpr int L_XCH = 16384;
constexpr int L_RING = 32768;
constexpr int L_END = L_RING + NSLOT * SLOT;
static_assert(L_KVS + 4 * 2304 <= L_XCH && L_END == 163840 && (L_KVS & 15) == 0 && (L_CS & 15) == 0, "lds");
enum { F_FULL = 0, F_TAG = 2, F_FREE = 4, F_JOB = 8, F_KFL = 12, F_QGO = 10, F_CONV = 24, F_CS3R = 31, F_PTOK = 30, F_KVR = 16, F_XF = 20, F_CS1R = 28 };
constexpr unsigned SPIN_CAP = 1u << 17;

template <int V> struct IC { static constexpr int v = V; __device__ constexpr operator int() const { return V; } };
template <class F, int... I> __device__ __forceinline__ void sfor_(F&& fn, std::integer_sequence<int, I...>) { (fn(IC<I>{}), ...); }
template <int N, class F> __device__ __forceinline__ void sfor(F&& fn) { sfor_(fn, std::make_integer_sequence<int, N>{}); }
#define INL __attribute__((always_inline))
#define MEMBAR() asm volatile("" ::: "memory")
#define SB() __builtin_amdgcn_sched_barrier(0)
#define VMWAIT(n) asm volatile("s_waitcnt vmcnt(" #n ")" ::: "memory")
#define PIN(x) asm volatile("" : "+v"(x))

__device__ __forceinline__ unsigned umin(unsigned a, unsigned b) { return a < b ? a : b; }
__device__ __forceinline__ int gsplit(int i) { return (43 * i) >> 4; }
__device__ __forceinline__ f32x16 mfma(v4u a, v4u b, f32x16 c) {
  return __builtin_amdgcn_mfma_f32_32x32x16_bf16(__builtin_bit_cast(s16x8, a), __builtin_bit_cast(s16x8, b), c, 0, 0, 0);
}
__device__ __forceinline__ f32x16 zero16() { return f32x16{0.f, 0.f, 0.f, 0.f, 0.f, 0.f, 0.f, 0.f, 0.f, 0.f, 0.f, 0.f, 0.f, 0.f, 0.f, 0.f}; }
__device__ __forceinline__ unsigned pack2(float a, float b) {
  bf16x2 v = {(__bf16)a, (__bf16)b};
  return __builtin_bit_cast(unsigned, v);
}
__device__ __forceinline__ float u2f(unsigned u) { return __builtin_bit_cast(float, u); }
__device__ __forceinline__ unsigned f2u(float f) { return __builtin_bit_cast(unsigned, f); }
__device__ __forceinline__ float blo(unsigned u) { return u2f(u << 16); }
__device__ __forceinline__ float bhi(unsigned u) { return u2f(u & 0xffff0000u); }
__device__ __forceinline__ float relu(float x) { return u2f((unsigned)max((int)f2u(x), 0)); }
__device__ __forceinline__ float dot2(unsigned a, unsigned b, float acc) {
  return __builtin_amdgcn_fdot2_f32_bf16(__builtin_bit_cast(bf16x2, a), __builtin_bit_cast(bf16x2, b), acc, false);
}
constexpr unsigned ONES2 = 0x3f803f80u;
constexpr float LOG2E = 1.44269504f;
__device__ __forceinline__ float red_h(float v) {
  auto s = __builtin_amdgcn_permlane32_swap(f2u(v), f2u(v), false, false);
  return u2f((unsigned)s[0]) + u2f((unsigned)s[1]);
}
__device__ __forceinline__ float red_g(float v) {
  auto r = __builtin_amdgcn_permlane16_swap(f2u(v), f2u(v), false, false);
  return red_h(u2f((unsigned)r[0]) + u2f((unsigned)r[1]));
}
template <int CTRL> __device__ __forceinline__ float dpp(float v) { return u2f(__builtin_amdgcn_update_dpp(0, (int)f2u(v), CTRL, 0xf, 0xf, false)); }
__device__ __forceinline__ float exp2c(float x) { return __builtin_amdgcn_fmed3f(__builtin_amdgcn_exp2f(x), 0.f, 1.f); }
__device__ __forceinline__ void split2(float v, unsigned& hi, unsigned& lo) {
  hi = pack2(v, 0.f) & 0xffffu;
  lo = pack2(v - blo(hi), 0.f) & 0xffffu;
}
template <int D = 256> __device__ __forceinline__ v4u tr_frag(LAS char* p) {
  v2u lo = __builtin_bit_cast(v2u, __builtin_amdgcn_ds_read_tr16_b64_v4i16((LAS s16x4*)p));
  v2u hi = __builtin_bit_cast(v2u, __builtin_amdgcn_ds_read_tr16_b64_v4i16((LAS s16x4*)(p + D)));
  return v4u{lo.x, lo.y, hi.x, hi.y};
}
__device__ __forceinline__ void glds(const void* sbase, unsigned voff, unsigned ldst) {
  asm volatile("s_mov_b32 m0, %2\n\ts_nop 0\n\tglobal_load_lds_dwordx4 %0, %1" ::"v"(voff), "s"(sbase), "s"(ldst) : "memory");
}
__device__ __forceinline__ void glds4(const void* sbase, unsigned voff, unsigned ldst) {
  asm volatile(
      "s_mov_b32 m0, %2\n\ts_nop 0\n\tglobal_load_lds_dwordx4 %0, %1 sc1\n\tglobal_load_lds_dwordx4 %0, %1 offset:1024 sc1\n\t"
      "global_load_lds_dwordx4 %0, %1 offset:2048 sc1\n\tglobal_load_lds_dwordx4 %0, %1 offset:3072 sc1" ::"v"(voff), "s"(sbase), "s"(ldst)
      : "memory");
}

struct Ctx {
  LAS char* L;
  __amdgpu_buffer_rsrc_t rs;
  int lane, wave;
  int seq, known;
  int vb0, vb1, nb0, nb1;
  int fa;
  v2u fpre;
  v4u w0, w1, w2, w3;
  template <int I> __device__ __forceinline__ v4u& wf() { if constexpr (I == 0) return w0; else if constexpr (I == 1) return w1; else if constexpr (I == 2) return w2; else return w3; }
};
__device__ __forceinline__ volatile LAS unsigned* flg(Ctx& c, int i) { return (volatile LAS unsigned*)(c.L + L_FLG) + i; }
template <int N, int SLP> __device__ __forceinline__ void wait_lds(Ctx& c, int i, unsigned val) {
#pragma nounroll
  for (unsigned sp = 0; sp < SPIN_CAP; ++sp) {
    unsigned v;
    if (N == 1) v = *flg(c, i);
    else if (N == 2) { v2u f = *(volatile LAS v2u*)flg(c, i); v = umin(f.x, f.y); }
    else { v4u f = *(volatile LAS v4u*)flg(c, i); v = umin(umin(f.x, f.y), umin(f.z, f.w)); }
    if ((unsigned)__builtin_amdgcn_readfirstlane(v) >= val) break;
    if (SLP) __builtin_amdgcn_s_sleep(SLP);
  }
  MEMBAR();
}
template <int N> __device__ __forceinline__ void poll_tags(Ctx& c, int base, v2u tag) {
#pragma nounroll
  for (unsigned sp = 0; sp < SPIN_CAP; ++sp) {
    const v2u v = __builtin_amdgcn_raw_buffer_load_b64(c.rs, (c.lane & (N - 1)) * 64, base, 16);
    if (__all(v.x == tag.x && v.y == tag.y)) break;
    __builtin_amdgcn_s_sleep(4);
    MEMBAR();
  }
  MEMBAR();
}
__device__ __forceinline__ v4u ld_ws(Ctx& c, int voff, int soff) { return __builtin_amdgcn_raw_buffer_load_b128(c.rs, voff, soff, 16); }
__device__ __forceinline__ void st_ws(Ctx& c, v4u v, int voff, int soff) {
  __builtin_amdgcn_raw_buffer_store_b128(v, c.rs, voff, soff, 16);
  asm volatile("s_nop 1" ::"v"(v) : "memory");
}
__device__ __forceinline__ int fresh_lane() { int l; asm volatile("v_mbcnt_lo_u32_b32 %0, -1, 0\n\tv_mbcnt_hi_u32_b32 %0, -1, %0" : "=v"(l)); __builtin_assume(l >= 0 && l < 64); return l; }
__device__ __forceinline__ int opq(int v) { asm volatile("" : "+s"(v)); return v; }
__device__ __forceinline__ int opqv(int v) { asm volatile("" : "+v"(v)); return v; }

constexpr int fps(int mode) { return (mode == 1 || mode == 4) ? 8 : 16; }
constexpr bool two(int mode) { return mode == 1 || mode == 2; }
__device__ __forceinline__ void slot_wait(Ctx& c, int sq) {
  if (__builtin_expect(c.known <= sq, 0)) {
#pragma nounroll
    for (unsigned sp = 0; sp < SPIN_CAP; ++sp) {
      v2u f = *(volatile LAS v2u*)flg(c, F_FULL);
      c.known = __builtin_amdgcn_readfirstlane(umin(f.x, f.y));
      if (c.known > sq) break;
    }
  }
  MEMBAR();
}
template <int MODE> __device__ __forceinline__ void slot_base(Ctx& c, int sq, int lb0, int lb1) {
  const int sb = (sq & (NSLOT - 1)) * SLOT;
  c.nb0 = lb0 + sb; PIN(c.nb0);
  if (two(MODE)) { c.nb1 = lb1 + sb; PIN(c.nb1); }
}
template <int MODE> __device__ __forceinline__ void slot_next(Ctx& c) {
  c.nb0 = (c.vb0 + SLOT) & (NSLOT * SLOT - 1); PIN(c.nb0);
  if (two(MODE)) { c.nb1 = (c.vb1 + SLOT) & (NSLOT * SLOT - 1); PIN(c.nb1); }
}
template <int MODE, int F, bool OPEN = (F % fps(MODE) == 0)> __device__ __forceinline__ v4u wread(Ctx& c) {
  constexpr int i = F % fps(MODE);
  if constexpr (OPEN) { c.vb0 = c.nb0; if (two(MODE)) c.vb1 = c.nb1; }
  LAS char* R = c.L + L_RING;
  if (MODE == 0) return *(LAS v4u*)(R + c.vb0 + i * KB);
  if (MODE == 1) return tr_frag<512>(R + ((i & 1) ? c.vb1 : c.vb0) + ((i >> 1) & 1) * 2 * KB + (i >> 2) * 8 * KB);
  if (MODE == 4) return *(LAS v4u*)(R + c.vb0 + (i & 3) * KB + (i >> 2) * 8 * KB);
  if (MODE == 2) return tr_frag(R + ((i & 1) ? c.vb1 : c.vb0) + i * KB);
  return tr_frag(R + c.vb0 + i * KB);
}
__device__ __forceinline__ void ring_publish(Ctx& c) { MEMBAR(); *(volatile LAS unsigned*)(c.L + c.fa) = (unsigned)c.seq; MEMBAR(); }
template <int MODE> __device__ __forceinline__ void wpre(Ctx& c, int lb0, int lb1) {
  slot_wait(c, c.seq);
  slot_base<MODE>(c, c.seq, lb0, lb1);
  sfor<4>([&](auto F) INL { c.wf<F.v>() = wread<MODE, F.v>(c); });
  if (fps(MODE) == 8) c.fpre = *(volatile LAS v2u*)flg(c, F_FULL);
}
struct NoSide { template <class T> __device__ __forceinline__ void operator()(T) const {} };
constexpr int fmap(int f, int nf, int fpsl, int tf) {
  if (tf < 0 || f < nf - fpsl || f >= nf) return f;
  const int i = f - (nf - fpsl), hf = fpsl / 2;
  return nf - fpsl + 2 * (i % hf) + (i < hf ? tf : 1 - tf);
}
constexpr u64 DUAL = 0xFEDC76B54A329180ull;
constexpr int dual_first(int ch) { for (int u = 0; u < 16; ++u) if (((DUAL >> (4 * u)) & 15) == (u64)ch) return u; return -1; }
template <int NF, int TPS, int MODE, int NEXT, bool XA, int ZERO, int RELF, int EARLY, int CAP, int TF, class SIDE>
__device__ __forceinline__ void gemm4(Ctx& c, v4u (&a)[32], f32x16& c0, f32x16& c1, f32x16& c2, f32x16& c3, int lb0, int lb1, int nb0, int nb1, SIDE side) {
  constexpr int FPS = fps(MODE);
  static_assert(NF % FPS == 0, "whole slots");
  sfor<NF>([&](auto F) INL {
    constexpr int f = F.v, un = NF == 128 ? (int)((DUAL >> (4 * (f / 8))) & 15) : 0;
    constexpr int fm = fmap(f, NF, FPS, TF);
    constexpr int s = NF == 128 ? 4 * (un & 7) + (f % 8) / 2 : fm / TPS, t = fm % TPS, fn = f + 4, i = fn % FPS;
    const v4u x = a[s & 31], wv = c.wf<f % 4>();
    f32x16& acc = un >= 8 ? (t ? c3 : c2) : (t ? c1 : c0);
    const f32x16 cin = (s == 0 && ((ZERO >> t) & 1)) ? zero16() : acc;
    acc = XA ? mfma(x, wv, cin) : mfma(wv, x, cin);
    PIN(acc);
    SB();
    if constexpr (fn < NF) {
      c.wf<f % 4>() = wread<MODE, fmap(fn, NF, FPS, TF), i == 0>(c);
      constexpr bool lastslot = fn / FPS == NF / FPS - 1;
      if constexpr (!lastslot || NEXT >= 0) {
        constexpr int sl = fn / FPS, ahead = FPS == 8 && sl + 2 < NF / FPS ? 2 : 1;
        if constexpr (FPS == 16 || !(sl & 1)) {
          if constexpr (i == (FPS == 8 ? 2 : FPS - 7)) c.fpre = *(volatile LAS v2u*)flg(c, F_FULL);
          if constexpr (i == FPS - 4) { c.fpre.x = umin(c.fpre.x, c.fpre.y); PIN(c.fpre); }
          if constexpr (i == FPS - 3) { c.known = __builtin_amdgcn_readfirstlane(c.fpre.x); asm volatile("" : "+s"(c.known)); }
          if constexpr (i == FPS - 1) slot_wait(c, c.seq + ahead);
        }
        if constexpr (i == FPS - 3) {
          if constexpr (lastslot && NEXT != MODE) slot_base<(NEXT < 0 ? 0 : NEXT)>(c, c.seq + 1, nb0, nb1); else slot_next<MODE>(c);
        }
      }
      if constexpr (i == FPS - 1) c.seq++;
    } else if constexpr (NEXT >= 0) {
      c.wf<f % 4>() = wread<(NEXT < 0 ? 0 : NEXT), fn - NF>(c);
      if constexpr (fps(NEXT) == 8 && fn - NF == 1) c.fpre = *(volatile LAS v2u*)flg(c, F_FULL);
    }
    if constexpr (fn >= FPS && fn <= NF + 2 && i == 2) { if constexpr ((f >= RELF || fn / FPS - 1 < EARLY) && fn / FPS - 1 < CAP) ring_publish(c); }
    else if constexpr (f == RELF && RELF > 0) ring_publish(c);
    side(F);
    SB();
  });
}
template <int NF, int TPS, int MODE, int NEXT, bool XA, int ZERO, int RELF, int EARLY, int CAP, int TF = -1, class SIDE>
__device__ __forceinline__ void gemm(Ctx& c, v4u (&a)[32], f32x16& c0, f32x16& c1, int lb0, int lb1, int nb0, int nb1, SIDE side) {
  gemm4<NF, TPS, MODE, NEXT, XA, ZERO, RELF, EARLY, CAP, TF>(c, a, c0, c1, c0, c1, lb0, lb1, nb0, nb1, side);
}
__device__ __forceinline__ unsigned sum4(unsigned m, v4u v) { return ((m + v.x + v.y) + v.z + v.w); }
template <bool EX> __device__ __forceinline__ void ld_chk(Ctx& c, v2u& k, int sb) {
  if (EX) k = __builtin_amdgcn_raw_buffer_load_b64(c.rs, c.lane * 8 + 2 * KB, sb, 16);
  else k.x = __builtin_amdgcn_raw_buffer_load_b32(c.rs, c.lane * 4 + 2 * KB, sb, 16);
}
template <int STR, bool EX> __device__ __forceinline__ void ld_rec(Ctx& c, v4u (&a)[32], v2u (&k)[16], int lv, int sb, int p, int e) {
  const int so = sb + STR * p * REC;
  if (e < 2) a[(2 * p + e) & 31] = ld_ws(c, lv + e * KB, so);
  else ld_chk<EX>(c, k[p & 15], so);
}
template <bool EX> __device__ __forceinline__ unsigned rec_bad(v4u (&a)[32], v2u (&k)[16], int p, unsigned tag) {
  return sum4(sum4(EX ? k[p & 15].x + k[p & 15].y : k[p & 15].x, a[(2 * p) & 31]), a[(2 * p + 1) & 31]) ^ tag;
}
template <int N, int STR, bool EX> __device__ __forceinline__ void settle(Ctx& c, unsigned bad, v4u (&a)[32], v2u (&k)[16], int lv, int sb, int p0, unsigned tag) {
  if (__builtin_expect(__any(bad != 0u), 0)) {
    unsigned sp = 0;
#pragma nounroll
    do {
      __builtin_amdgcn_s_sleep(BACKOFF);
      MEMBAR();
      bad = 0;
#pragma unroll
      for (int i = 0; i < 3 * N; ++i) ld_rec<STR, EX>(c, a, k, lv, sb, p0 + i / 3, i % 3);
#pragma unroll
      for (int i = 0; i < N; ++i) bad |= rec_bad<EX>(a, k, p0 + i, tag);
    } while (__any(bad != 0u) && ++sp < SPIN_CAP);
  }
}
template <int F, int NP, int STR, bool EX>
__device__ __forceinline__ void check_step(Ctx& c, unsigned& m0, unsigned& m1, v4u (&a)[32], v2u (&k)[16], int lv, int sb, unsigned tag) {
  constexpr int p = 2 * ((F + 8) / 8), r = (F + 8) % 8;
  if constexpr (p < NP) {
    if constexpr (r == 2) { m0 = sum4(EX ? k[p & 15].x + k[p & 15].y : k[p & 15].x, a[(2 * p) & 31]); PIN(m0); }
    if constexpr (r == 3) { m0 = sum4(m0, a[(2 * p + 1) & 31]); PIN(m0); }
    if constexpr (r == 4) { m1 = sum4(EX ? k[(p + 1) & 15].x + k[(p + 1) & 15].y : k[(p + 1) & 15].x, a[(2 * p + 2) & 31]); PIN(m1); }
    if constexpr (r == 5) { m1 = sum4(m1, a[(2 * p + 3) & 31]); PIN(m1); }
    if constexpr (r == 6) { m0 = (m0 ^ tag) | (m1 ^ tag); PIN(m0); }
    if constexpr (r == 7) settle<2, STR, EX>(c, m0, a, k, lv, sb, p, tag);
  }
}
template <int STR, bool EX> __device__ __forceinline__ void open_phase(Ctx& c, v4u (&a)[32], v2u (&k)[16], int lv, int sb, unsigned tag) {
  VMWAIT(0);
  __builtin_amdgcn_s_sleep(XD);
#pragma unroll
  for (int i = 0; i < 12; ++i) ld_rec<STR, EX>(c, a, k, lv, sb, i / 3, i % 3);
  SB();
  settle<2, STR, EX>(c, rec_bad<EX>(a, k, 0, tag) | rec_bad<EX>(a, k, 1, tag), a, k, lv, sb, 0, tag);
}
template <int F, int P0, int P1, int STR, bool EX> __device__ __forceinline__ void req_step(Ctx& c, v4u (&a)[32], v2u (&k)[16], int lv, int sb) {
  if constexpr (F >= 0 && P0 + F / 2 < P1) {
    ld_rec<STR, EX>(c, a, k, lv, sb, P0 + F / 2, F & 1);
    if constexpr (F & 1) ld_rec<STR, EX>(c, a, k, lv, sb, P0 + F / 2, 2);
  }
}
template <int G, int PER, int P0, int P1, int STR, bool EX> __device__ __forceinline__ void req_pace(Ctx& c, v4u (&a)[32], v2u (&k)[16], int lv, int sb) {
  if constexpr (G >= 0 && P0 + G / PER < P1) {
    constexpr int r = G % PER - (PER == 4 ? (G / 4) & 1 : 0), D = PER / 3;
    if constexpr (r >= 0 && r % D == 0 && r / D < 3) ld_rec<STR, EX>(c, a, k, lv, sb, P0 + G / PER, r / D);
  }
}
template <bool EX> __device__ __forceinline__ void st_chk(Ctx& c, unsigned sum, unsigned ex, int sb, unsigned tag) {
  if (EX) __builtin_amdgcn_raw_buffer_store_b64(v2u{tag - sum - ex, ex}, c.rs, c.lane * 8 + 2 * KB, sb, 16);
  else __builtin_amdgcn_raw_buffer_store_b32(tag - sum, c.rs, c.lane * 4 + 2 * KB, sb, 16);
}
__device__ __forceinline__ u64 mix64(u64 z) {
  z += 0x9E3779B97F4A7C15ull;
  z = (z ^ (z >> 30)) * 0xBF58476D1CE4E5B9ull;
  z = (z ^ (z >> 27)) * 0x94D049BB133111EBull;
  return z ^ (z >> 31);
}
__device__ __forceinline__ float csum(v4u f, float s) { return dot2(f.w, ONES2, dot2(f.z, ONES2, dot2(f.y, ONES2, dot2(f.x, ONES2, s)))); }
__device__ __forceinline__ v4u cs_operand(float s) { unsigned hi, lo; split2(s, hi, lo); return v4u{hi | (hi << 16), lo, 0u, 0u}; }

__global__ __launch_bounds__(512, 2) void fused_fwd(const u16* __restrict__ x, const u16* __restrict__ wqkv, const u16* __restrict__ wo,
                                                    const u16* __restrict__ wg, const u16* __restrict__ wu, const u16* __restrict__ wd,
                                                    u16* __restrict__ out, char* __restrict__ ws) {
  __shared__ __attribute__((aligned(16))) char smem[L_END];
  u64 pc0;
  asm volatile("s_getpc_b64 %0" : "=s"(pc0));
  Ctx c;
  c.L = (LAS char*)smem;
  c.rs = __builtin_amdgcn_make_buffer_rsrc((void*)ws, 0, WS_END, 0x00020000);
  const int wave = __builtin_amdgcn_readfirstlane(threadIdx.x >> 6);
  const int b = blockIdx.x, xc = b & 7, j = b >> 3;
  const int cl = (xc >> 1) * 4 + (j >> 3), m = (xc & 1) * 8 + (j & 7);
  const int rh = m >> 3, cg = m & 7;
  const int g0 = gsplit(m), ng = gsplit(m + 1) - g0;
  c.lane = 0; c.wave = wave; c.seq = 0; c.known = 0; c.vb0 = 0; c.vb1 = 0; c.nb0 = 0; c.nb1 = 0; c.fa = 0; c.fpre = v2u{0u, 0u};
  const int rot = (2 * cl + m) & 7;
  LAS char* L = c.L;
  if (threadIdx.x < 32) *flg(c, threadIdx.x) = 0u;
  asm volatile("s_waitcnt lgkmcnt(0)\n\ts_barrier" ::: "memory");
#define LANEVARS                                                                                                              \
  const int lane = fresh_lane(), h = lane >> 5, l31 = lane & 31, lv = lane * 16;                                               \
  const int trN = (8 * h + ((lane >> 2) & 3)) * 64 + ((lane >> 4) & 1) * 32 + (lane & 3) * 8;                                   \
  const int trS = (8 * h + ((lane >> 2) & 3)) * 64 + ((lane >> 4) & 1) * 32 + (((lane & 1) << 1) | ((lane >> 1) & 1)) * 8;       \
  (void)trN; (void)trS; (void)l31; (void)lv;
  const int pp = wave & 1;
  auto load_half0 = [&](int sl) INL {
    const int lane = fresh_lane();
    const unsigned vo1 = (lane >> 2) * 3072 + (lane & 3) * 16;
    const char* wb = (const char*)wqkv + m * 64 + (sl < 4 ? (1 + pp) * 1024 : 0);
#pragma unroll
    for (int p = 0; p < 8; ++p) {
      const int s = sl < 4 ? sl * 8 + p : (sl - 4) * 16 + 2 * p + pp;
      glds(wb + (size_t)(((s + 4 * rot) & 31) * 16) * 3072, vo1, L_RING + sl * SLOT + p * 2048 + pp * 1024);
    }
  };
  auto cache_load = [&](auto& wc, auto NK) INL {
    const int lane = fresh_lane();
    const __amdgpu_buffer_rsrc_t rd = __builtin_amdgcn_make_buffer_rsrc((void*)wd, 0, 1376 * 1024, 0x00020000);
    const int vo2 = (lane >> 3) * 1024 + (((lane & 7) ^ (((lane >> 4) & 1) << 2)) * 16);
#pragma unroll
    for (int k = 0; k < NK.v; ++k) {
#pragma unroll
      for (int q = 0; q < 2; ++q) {
        const int hs = (NK.v == 5 ? 2 * k : k < 5 ? 2 * k + 1 : 10) * 2 + pp;
        const int p = min(hs * 2 + q, 42);
        const int m3 = min(p - 32 + 1 + ((p - 32) >> 1), 15);
        const int aa = p < 32 ? gsplit(p & 15) + (p >> 4) : gsplit(m3) + 2;
#pragma unroll
        for (int e = 0; e < 4; ++e) wc[k * 8 + q * 4 + e] = __builtin_amdgcn_raw_buffer_load_b128(rd, vo2, aa * 32768 + cg * 128 + e * 8192, 0);
        SB();
        if (NK.v == 5) __builtin_amdgcn_s_sleep(2);
      }
    }
  };
  auto cache_conv = [&](auto& wc, auto NK) INL {
    const int lane = fresh_lane(), h = lane >> 5;
    const int tk = (8 * h + ((lane >> 2) & 3)) * 128 + ((lane >> 4) & 1) * 32 + (((lane & 1) << 1) | ((lane >> 1) & 1)) * 8 + ((lane >> 3) & 1) * 64;
    LAS char* d = L + L_XCH + (wave - 4) * 4096;
    wait_lds<4, 2>(c, F_FREE, 4u);
#pragma unroll
    for (int q = 0; q < 2 * NK.v; ++q) {
#pragma unroll
      for (int e = 0; e < 4; ++e) *(LAS v4u*)(d + lane * 16 + e * KB) = wc[q * 4 + e];
      MEMBAR(); SB();
#pragma unroll
      for (int e = 0; e < 4; ++e) wc[q * 4 + e] = tr_frag<512>(d + (e >> 1) * 2048 + (tk ^ (((e ^ q) & 1) * 64)));
      MEMBAR(); SB();
    }
    *flg(c, F_CONV + wave - 4) = 1u;
    MEMBAR();
  };
  auto cache_put = [&](auto& wc, auto NK, int base) INL {
    const int lane = fresh_lane();
#pragma unroll
    for (int k = 0; k < NK.v; ++k) {
      const int sq = base + (NK.v == 5 ? 2 * k : k < 5 ? 2 * k + 1 : 10);
      wait_lds<4, 4>(c, F_FREE, (unsigned)(sq - NSLOT + 1));
      LAS char* d = L + L_RING + (sq & (NSLOT - 1)) * SLOT + pp * 8192;
#pragma unroll
      for (int e = 0; e < 8; ++e) {
        *(LAS v4u*)(d + lane * 16 + e * KB) = wc[k * 8 + e];
        asm volatile("s_nop 3" ::: "memory");
      }
      wait_lds<1, 2>(c, F_FULL + pp, (unsigned)sq);
      *flg(c, F_FULL + pp) = (unsigned)(sq + 1);
      MEMBAR();
    }
  };

  if (wave < 4) {
    __builtin_amdgcn_s_setprio(2);
    const int w = wave, mh = w >> 1, kh = w & 1;
    c.fa = opqv(L_FLG + (F_FREE + wave) * 4);
    asm volatile("" : "+s"(c.rs));
    v4u a[32], xr0, xr1;
    v2u k[16];
    f32x16 A0, A1, B0, B1;
    LANEVARS
    c.lane = lane;
    float rstd;
    unsigned tagx, tagy, tagc;
    v4u mfrag;
    auto set_stats = [&](float sum, float ssq) INL -> float {
      const float s = sum * (1.f / 512), q = ssq * (1.f / 512);
      unsigned hi, lo;
      split2(-s, hi, lo);
      mfrag = h == 0 ? v4u{hi | (lo << 16), hi, 0u, 0u} : v4u{0u, 0u, 0u, 0u};
      return fmaxf(q - s * s, 0.f) + 1e-5f;
    };
    auto cs_frag = [&](int tile) INL { return *(LAS v4u*)(L + L_CS + tile * 512 + l31 * 16); };
    auto rs_put = [&]() INL { const int lane = fresh_lane(); *(LAS float*)(L + L_KVS + w * 2304 + lane * 4) = h ? rstd * LOG2E : rstd; MEMBAR(); };
    auto rs_get = [&](int q) INL {
      const int h = fresh_lane() >> 5;
      const f32x4 r = *(LAS f32x4*)(L + L_KVS + w * 2304 + (8 * q + 4 * h) * 4), rl = *(LAS f32x4*)(L + L_KVS + w * 2304 + 128 + (8 * q + 4 * h) * 4);
#pragma unroll
      for (int i = 0; i < 4; ++i) { B0[4 * q + i] = r[i]; B1[4 * q + i] = rl[i]; }
    };
    {
      const __amdgpu_buffer_rsrc_t rx = __builtin_amdgcn_make_buffer_rsrc((void*)x, 0, 2048 * 1024, 0x00020000);
      const int r8 = lane >> 3;
      const unsigned xv0 = r8 * 1024 + (((lane & 7) ^ (r8 >> 1)) * 16), xv1 = xv0 ^ 64;
      const char* xb = (const char*)x + (size_t)(cl * 128 + w * 32) * 1024;
      const int ro = l31 * 128 + ((h ^ ((l31 >> 1) & 7)) * 16);
      auto xbuf = [&](int kp) INL { return (kp % 3 == 1 ? L_XCH : L_RING + (4 + (kp % 3) / 2) * SLOT) + w * 4096; };
      auto xreq = [&](int kp) INL {
#pragma unroll
        for (int q = 0; q < 4; ++q) glds(xb + q * 8192 + ((kp + rot) & 7) * 128, (q & 1) ? xv1 : xv0, xbuf(kp) + q * 1024);
      };
      auto xget = [&](int kp) INL {
        if (kp < 6) VMWAIT(8); else if (kp == 6) VMWAIT(4); else VMWAIT(0);
#pragma unroll
        for (int q = 0; q < 4; ++q) a[kp * 4 + q] = *(LAS v4u*)(L + xbuf(kp) + (ro ^ (q * 32)));
        if (kp + 3 < 8) {
          asm volatile("s_waitcnt lgkmcnt(0)" : "+v"(a[kp * 4]), "+v"(a[kp * 4 + 1]), "+v"(a[kp * 4 + 2]), "+v"(a[kp * 4 + 3])::"memory");
          xreq(kp + 3);
        }
        MEMBAR();
      };
      __builtin_amdgcn_s_setprio(3);
      xreq(0); xreq(1); xreq(2);
      __builtin_amdgcn_s_setprio(2);

#pragma unroll
      for (int kp = 0; kp < XPRE; ++kp) xget(kp);
      wpre<2>(c, trN, trS);
      v4u s0, ca, cb;
      const int s0b = WS_S0P + (cl * 128 + w * 32) * 16;
      gemm<64, 2, 2, -1, true, 3, 0, 0, 99>(c, a, A0, A1, trN, trS, 0, 0, [&](auto F) INL {
        constexpr int f = F.v;
        if ((f & 3) == 1 && (f >> 2) + XPRE < 8) xget((f >> 2) + XPRE);
        if (f == 4 * (8 - XPRE)) s0 = ld_ws(c, l31 * 16, s0b);
        if (f == 4 * (8 - XPRE) + 2) {
          const int xq = l31 * 1024 + h * 16, xqs = (cl * 128 + mh * 64 + rh * 32) * 1024 + cg * 128 + kh * 64;
          xr0 = __builtin_amdgcn_raw_buffer_load_b128(rx, xq, xqs, 0);
          xr1 = __builtin_amdgcn_raw_buffer_load_b128(rx, xq + 32, xqs, 0);
        }
        if (f == 40) {
          wait_lds<2, 0>(c, F_CS1R, 1u);
          const v2u tg = *(volatile LAS v2u*)flg(c, F_TAG);
          tagx = __builtin_amdgcn_readfirstlane(tg.x); tagy = __builtin_amdgcn_readfirstlane(tg.y); tagc = (tagx * 0x9E3779B1u + tagy) | 1u;
          if (__any(s0.y != tagx || s0.w != tagy)) {
            unsigned sp = 0;
#pragma nounroll
            do { MEMBAR(); s0 = ld_ws(c, l31 * 16, s0b); } while (__any(s0.y != tagx || s0.w != tagy) && ++sp < SPIN_CAP);
          }
          rstd = __builtin_amdgcn_rsqf(set_stats(u2f(s0.x), u2f(s0.z)));
          rs_put();
          ca = cs_frag(0); cb = cs_frag(1);
        }
        if (f == 45) { A0 = mfma(mfrag, ca, A0); A1 = mfma(mfrag, cb, A1); }
        if (f >= 54 && f < 62 && !(f & 1)) rs_get((f - 54) >> 1);
      });
    }
#pragma unroll 1
    for (int layer = 0; layer < 2; ++layer) {
      bool publisher;
      {
        LANEVARS
#pragma unroll
        for (int i = 0; i < 16; ++i) B1[i] *= A0[i];
        SB();
#pragma unroll
        for (int i = 0; i < 16; ++i) { B1[i] = exp2c(B1[i]); A0[i] = fmaf(A0[i], B0[i], 1.f); A1[i] *= B0[i]; }
        SB();
        v4u pk0, pk1, vv0, vv1;
#pragma unroll
        for (int i = 0; i < 16; ++i) A0[i] = fmaxf(A0[i], B1[i]) + 1e-6f;
#pragma unroll
        for (int i = 0; i < 4; ++i) {
          pk0[i] = pack2(A0[2 * i], A0[2 * i + 1]); vv0[i] = pack2(A1[2 * i], A1[2 * i + 1]);
          pk1[i] = pack2(A0[8 + 2 * i], A0[9 + 2 * i]); vv1[i] = pack2(A1[8 + 2 * i], A1[9 + 2 * i]);
        }
        f32x16 kv = mfma(pk0, vv0, zero16());
        kv = mfma(pk1, vv1, kv);
        const float z = red_h(csum(pk0, 0.f) + csum(pk1, 0.f));
        LAS char* ks = L + L_KVS + w * 2304;
        if (h == 0) *(LAS float*)(ks + 2048 + l31 * 4) = z;
        if (lane & 16) {
          *(LAS f32x4*)(ks + lv) = f32x4{kv[8], kv[9], kv[10], kv[11]};
          *(LAS f32x4*)(ks + 1024 + lv) = f32x4{kv[12], kv[13], kv[14], kv[15]};
          MEMBAR();
        } else {
          MEMBAR();
          *(LAS f32x4*)(ks + lv) = f32x4{kv[0], kv[1], kv[2], kv[3]};
          *(LAS f32x4*)(ks + 1024 + lv) = f32x4{kv[4], kv[5], kv[6], kv[7]};
        }
        MEMBAR();
        *flg(c, F_KFL + w) = (unsigned)(layer + 1);
        const v4u kf = *(volatile LAS v4u*)flg(c, F_KFL);
        MEMBAR();
        f32x4 p0[4], p1[4];
        f32x2 pz[4];
#pragma unroll
        for (int ww = 0; ww < 4; ++ww) {
          p0[ww] = *(LAS f32x4*)(L + L_KVS + ww * 2304 + lv);
          p1[ww] = *(LAS f32x4*)(L + L_KVS + ww * 2304 + 1024 + lv);
          pz[ww] = *(LAS f32x2*)(L + L_KVS + ww * 2304 + 2048 + (lane & 15) * 8);
        }
        MEMBAR();
        SB();
        publisher = (unsigned)__builtin_amdgcn_readfirstlane(umin(umin(kf.x, kf.y), umin(kf.z, kf.w))) > (unsigned)layer;
        if (publisher) {
          const f32x4 k0 = (p0[0] + p0[1]) + (p0[2] + p0[3]), k1 = (p1[0] + p1[1]) + (p1[2] + p1[3]);
          const f32x2 zz = (pz[0] + pz[1]) + (pz[2] + pz[3]);
          const unsigned tg = layer ? tagy : tagx;
          const unsigned tl = tg >> ((lane & 1) * 16), t0 = tl & 0xffu, t1 = (tl >> 8) & 0xffu;
          const int kvb = WS_KVP + ((layer * 16 + m) * 16 + cl) * KVR;
          auto st2 = [&](float u, float v, int vo) INL {
            __builtin_amdgcn_raw_buffer_store_b64(v2u{(f2u(u) & 0xffffff00u) | t0, (f2u(v) & 0xffffff00u) | t1}, c.rs, vo, kvb, 16);
          };
          st2(k0[0], k0[1], lane * 8); st2(k0[2], k0[3], lane * 8 + 512); st2(k1[0], k1[1], lane * 8 + 1024); st2(k1[2], k1[3], lane * 8 + 1536);
          if (lane < 16) st2(zz[0], zz[1], lane * 8 + 2048);
        }
        MEMBAR();
      }
      B0 = mfma(cs_frag(2), mfrag, zero16());
      if (layer == 0) { LANEVARS wpre<3>(c, trN, trN); gemm<32, 1, 3, -1, false, 0, 0, 0, 99>(c, a, B0, B0, trN, trN, 0, 0, NoSide()); }
      else { wpre<0>(c, lv, lv); gemm<32, 1, 0, -1, false, 0, 0, 0, 99>(c, a, B0, B0, lv, lv, 0, 0, NoSide()); }
      const int lq = fresh_lane();
      const int kb0 = (8 * (lq >> 5) + ((lq >> 2) & 3)) * 128 + ((lq >> 4) & 1) * 32 + (((lq & 1) << 1) | ((lq >> 1) & 1)) * 8 + kh * 4096 + ((kh ^ (lq >> 3)) & 1) * 64;
      const int kb1 = kb0 ^ 64;
      auto res_init = [&](f32x16& R, int i) INL { const unsigned u = i < 4 ? xr0[i & 3] : xr1[i & 3]; R[2 * i] = blo(u); R[2 * i + 1] = bhi(u); };
      {
        LANEVARS
        v4u pq0, pq1;
        const float rl = rstd * LOG2E;
#pragma unroll
        for (int i = 0; i < 16; ++i) B1[i] = exp2c(B0[i] * rl);
#pragma unroll
        for (int i = 0; i < 16; ++i) B0[i] = fmaxf(fmaf(B0[i], rstd, 1.f), B1[i]) + 1e-6f;
#pragma unroll
        for (int i = 0; i < 4; ++i) { pq0[i] = pack2(B0[2 * i], B0[2 * i + 1]); pq1[i] = pack2(B0[8 + 2 * i], B0[9 + 2 * i]); }
#pragma unroll
        for (int i = 0; i < 8; ++i) res_init(A0, i);
        PIN(pq0); PIN(pq1); PIN(A0);
        const int ob = opq(WS_O + cl * ACT_CL + (w * 16 + m) * REC);
        {
          const int lane = fresh_lane();
          const unsigned tg = layer ? tagy : tagx;
          const unsigned tl = tg >> ((lane & 1) * 16), t0 = tl & 0xffu, t1 = (tl >> 8) & 0xffu;
          const int kvb = WS_KVP + (layer * 16 + m) * 16 * KVR;
          float s0 = 0.f, s1 = 0.f;
          v2u zp;
          unsigned zpk;
          if (publisher) __builtin_amdgcn_s_sleep(KVDP); else __builtin_amdgcn_s_sleep(KVDELAY);
#pragma nounroll
          for (unsigned sp = 0; sp < SPIN_CAP; ++sp) {
            v2u p[16];
            zp = __builtin_amdgcn_raw_buffer_load_b64(c.rs, (lane >> 2) * KVR + (lane & 3) * 8 + 2048, kvb + w * 32, 16);
#pragma unroll
            for (int i = 0; i < 16; ++i) p[i] = __builtin_amdgcn_raw_buffer_load_b64(c.rs, lane * 8, kvb + w * 512 + i * KVR, 16);
            SB();
            zp.x ^= t0; zp.y ^= t1;
            unsigned chk = zp.x | zp.y;
            float za = u2f(zp.x), zb = u2f(zp.y);
            za += dpp<0x128>(za); zb += dpp<0x128>(zb); za += dpp<0x124>(za); zb += dpp<0x124>(zb);
            zpk = pack2(red_g(za), red_g(zb));
            PIN(zpk);
#pragma unroll
            for (int i = 0; i < 16; ++i) {
              p[i].x ^= t0; p[i].y ^= t1; chk |= p[i].x | p[i].y;
              s0 = i ? s0 + u2f(p[i].x) : u2f(p[i].x); s1 = i ? s1 + u2f(p[i].y) : u2f(p[i].y);
              PIN(s0); PIN(s1);
            }
            if (!__any((chk & 0xffu) != 0u)) break;
            __builtin_amdgcn_s_sleep(BACKOFF);
            MEMBAR();
          }
          *(LAS unsigned*)(L + L_KVF + lv + w * 4) = pack2(s0, s1);
          if (lane < 4) *(LAS unsigned*)(L + L_ZF + w * 16 + lane * 4) = zpk;
          MEMBAR();
          *flg(c, F_KVR + w) = (unsigned)(layer + 1);
        }
        v4u kvf;
        v2u z0, z1, z2, z3;
#pragma nounroll
        for (unsigned sp = 0; sp < SPIN_CAP; ++sp) {
          const v4u fl = *(volatile LAS v4u*)flg(c, F_KVR);
          MEMBAR();
          kvf = *(LAS v4u*)(L + L_KVF + lv);
          z0 = *(LAS v2u*)(L + L_ZF + h * 8); z1 = *(LAS v2u*)(L + L_ZF + 16 + h * 8);
          z2 = *(LAS v2u*)(L + L_ZF + 32 + h * 8); z3 = *(LAS v2u*)(L + L_ZF + 48 + h * 8);
          MEMBAR();
          if ((unsigned)__builtin_amdgcn_readfirstlane(umin(umin(fl.x, fl.y), umin(fl.z, fl.w))) > (unsigned)layer) break;
        }
        const bool up = (lane & 16) != 0;
        const v4u zz = {0u, 0u, 0u, 0u};
        B0 = mfma(up ? zz : kvf, pq0, zero16());
        B0 = mfma(up ? kvf : zz, pq1, B0);
        const float d0 = dot2(pq0.y, z0.y, dot2(pq0.x, z0.x, 0.f)) + dot2(pq0.w, z1.y, dot2(pq0.z, z1.x, 0.f));
        const float d1 = dot2(pq1.y, z2.y, dot2(pq1.x, z2.x, 0.f)) + dot2(pq1.w, z3.y, dot2(pq1.z, z3.x, 0.f));
        const float r0 = __builtin_amdgcn_rcpf(red_h(d0) + 1e-6f), r1 = __builtin_amdgcn_rcpf(red_h(d1) + 1e-6f);
        v4u o0, o1;
#pragma unroll
        for (int i = 0; i < 4; ++i) { o0[i] = pack2(B0[2 * i] * r0, B0[2 * i + 1] * r0); o1[i] = pack2(B0[8 + 2 * i] * r1, B0[9 + 2 * i] * r1); }
        st_ws(c, o0, lv, ob);
        st_ws(c, o1, lv + KB, ob);
        st_chk<false>(c, sum4(sum4(0u, o0), o1), 0u, ob, tagc);
      }
      const int rtA = opq(mh * 2 + rh), clb = opq(cl * ACT_CL);
      auto res_send = [&](f32x16& A1, int q, unsigned xseq) INL {
        LAS char* xs = L + L_XCH + w * 4096 + lv;
        *(LAS f32x4*)(xs + q * 2048) = f32x4{A1[8 * q], A1[8 * q + 1], A1[8 * q + 2], A1[8 * q + 3]};
        *(LAS f32x4*)(xs + q * 2048 + 1024) = f32x4{A1[8 * q + 4], A1[8 * q + 5], A1[8 * q + 6], A1[8 * q + 7]};
        MEMBAR();
        if (q) *flg(c, F_XF + w) = xseq;
      };
      auto res_epilogue = [&](f32x16& A0, int xi, bool final_out, unsigned xseq) INL {
        LAS char* xs = L + L_XCH;
        const int lv = fresh_lane() * 16;
        f32x4 o0, o1, o2, o3;
#pragma nounroll
        for (unsigned sp = 0; sp < SPIN_CAP; ++sp) {
          const unsigned fl = *flg(c, F_XF + (w ^ 1));
          MEMBAR();
          o0 = *(LAS f32x4*)(xs + (w ^ 1) * 4096 + lv); o1 = *(LAS f32x4*)(xs + (w ^ 1) * 4096 + 1024 + lv);
          o2 = *(LAS f32x4*)(xs + (w ^ 1) * 4096 + 2048 + lv); o3 = *(LAS f32x4*)(xs + (w ^ 1) * 4096 + 3072 + lv);
          MEMBAR();
          if ((unsigned)__builtin_amdgcn_readfirstlane(fl) >= xseq) break;
        }
        xr0 = v4u{pack2(A0[0] + o0[0], A0[1] + o0[1]), pack2(A0[2] + o0[2], A0[3] + o0[3]), pack2(A0[4] + o1[0], A0[5] + o1[1]), pack2(A0[6] + o1[2], A0[7] + o1[3])};
        xr1 = v4u{pack2(A0[8] + o2[0], A0[9] + o2[1]), pack2(A0[10] + o2[2], A0[11] + o2[3]), pack2(A0[12] + o3[0], A0[13] + o3[1]), pack2(A0[14] + o3[2], A0[15] + o3[3])};
        if (final_out) {
          const int lo = fresh_lane();
          GAS char* ob = (GAS char*)out + (size_t)((cl * 4 + rtA) * 32 + (lo & 31)) * 1024 + cg * 128 + kh * 64 + (lo >> 5) * 16;
          *(GAS v4u*)ob = xr0;
          *(GAS v4u*)(ob + 32) = xr1;
        } else {
          const int xb = WS_X + clb + ((xi * 4 + rtA) * 16 + cg * 2 + kh) * REC;
          st_ws(c, xr0, lv, xb);
          st_ws(c, xr1, lv + KB, xb);
          const float s = csum(xr0, 0.f) + csum(xr1, 0.f);
          float q0 = 0.f, q1 = 0.f;
#pragma unroll
          for (int i = 0; i < 4; ++i) { q0 = dot2(xr0[i], xr0[i], q0); q1 = dot2(xr1[i], xr1[i], q1); }
          const auto sw = __builtin_amdgcn_permlane32_swap(f2u(s), f2u(q0 + q1), false, false);
          st_chk<true>(c, sum4(sum4(0u, xr0), xr1), f2u(u2f((unsigned)sw[0]) + u2f((unsigned)sw[1])), xb, tagc);
        }
      };
      unsigned m0, m1;
      {
        const int ab = WS_O + clb + (rtA * 16 + kh) * REC;
        wpre<1>(c, kb0, kb1);
        if (mh) __builtin_amdgcn_s_sleep(YOFF);
        open_phase<2, false>(c, a, k, lv, ab, tagc);
        gemm<32, 2, 1, -1, false, 2, 8, 0, 99, 1>(c, a, A0, A1, kb0, kb1, 0, 0, [&](auto F) INL {
          if (F.v == 24 && layer == 0) wait_lds<2, 0>(c, F_CONV + 2, 1u);
          if constexpr (F.v == 29 || F.v == 30) res_send(A1, F.v - 29, (unsigned)(layer * 0x200 + 1));
          req_step<F.v, 4, 8, 2, false>(c, a, k, lv, ab);
          check_step<F.v, 8, 2, false>(c, m0, m1, a, k, lv, ab, tagc);
        });
        res_epilogue(A0, layer, false, (unsigned)(layer * 0x200 + 1));
      }
      *flg(c, F_XF + w) = (unsigned)(layer * 0x200 + 0x100);
      *(LAS v4u*)(L + L_KVS + w * 2304 + lv) = xr0;
      *(LAS v4u*)(L + L_KVS + w * 2304 + 1024 + lv) = xr1;
      MEMBAR();
      auto res_back = [&]() INL { xr0 = *(LAS v4u*)(L + L_KVS + w * 2304 + lv); xr1 = *(LAS v4u*)(L + L_KVS + w * 2304 + 1024 + lv); };
      wpre<0>(c, lv, lv);
      v4u cs0;
      float sacc;
      const int ab4 = WS_H + clb + (rtA * 43 + kh) * REC;
      const int hb = WS_H + clb + w * 43 * REC;
      float r2;
      v4u ho;
      unsigned hs;
      float h0, h1;
      auto hside = [&](auto F, f32x16& G, f32x16& U, int dst) INL {
        constexpr int f = F.v, sl = (f - 1) / 2;
        if constexpr (f >= 1 && f < 17) {
          if constexpr (f & 1) { h0 = relu(G[2 * sl]) * r2; PIN(h0); h1 = relu(G[2 * sl + 1]) * r2; PIN(h1); }
          else {
            h0 *= U[2 * sl]; PIN(h0); h1 *= U[2 * sl + 1]; PIN(h1);
            ho[sl & 3] = pack2(h0, h1); PIN(ho);
          }
        }
        if constexpr (f == 9 || f == 17) { st_ws(c, ho, lv + (f == 17) * KB, dst); hs = sum4(f == 9 ? 0u : hs, ho); PIN(hs); }
        if constexpr (f == 18) st_chk<false>(c, hs, 0u, dst, tagc);
      };
      auto corr = [&](auto F, int at, f32x16& G, f32x16& U, int gi) INL {
        if (F.v == at - 6) cs0 = cs_frag(3 + 2 * gi);
        if (F.v == at - 3) { G = mfma(cs0, mfrag, G); SB(); cs0 = cs_frag(4 + 2 * gi); }
        if (F.v == at) U = mfma(cs0, mfrag, U);
      };
      const int xb3 = WS_X + clb + (layer * 4 + w) * 16 * REC;
      open_phase<1, true>(c, a, k, lv, xb3, tagc);
      gemm4<128, 2, 0, 0, false, 3, 62, 0, 99, -1>(c, a, A0, A1, B0, B1, lv, lv, lv, lv, [&](auto F) INL {
        constexpr int f = F.v, u = f / 8;
        if constexpr (f == 0) { sacc = u2f(k[0].y) + u2f(k[1].y); PIN(sacc); }
        req_pace<f, 6, 4, 16, 1, true>(c, a, k, lv, xb3);
        sfor<8>([&](auto CH) INL {
          if constexpr (CH.v > 0 && dual_first(CH.v) == u + 1) check_step<8 * (CH.v - 1) + f % 8, 16, 1, true>(c, m0, m1, a, k, lv, xb3, tagc);
          if constexpr (CH.v > 0 && f == 8 * dual_first(CH.v)) { sacc += u2f(k[2 * CH.v].y) + u2f(k[2 * CH.v + 1].y); PIN(sacc); }
        });
        if constexpr (f == 89) {
          const auto sw = __builtin_amdgcn_permlane32_swap(f2u(sacc), f2u(sacc), false, false);
          r2 = __builtin_amdgcn_rcpf(set_stats(u2f((unsigned)sw[0]), u2f((unsigned)sw[1])));
          PIN(mfrag); PIN(r2);
        }
        if (f == 86 && layer == 0) wait_lds<1, 0>(c, F_CS3R, 1u);
        corr(F, 95, A0, A1, 0);
        if constexpr (f >= 96) hside(IC<f - 96>{}, A0, A1, hb + m * REC);
        corr(F, 123, B0, B1, 1);
      });
      const int tb0 = fresh_lane() * 16 + kh * 4096;
      if (ng == 3) {
        gemm<64, 2, 0, 4, false, 3, 0, 0, 99>(c, a, A0, A1, lv, lv, tb0, tb0, [&](auto F) INL {
          constexpr int f = F.v;
          hside(F, B0, B1, hb + (16 + m) * REC);
          corr(F, 47, A0, A1, 2);
          if constexpr (f == 19) res_back();
          if constexpr (f >= 23 && f < 31) { res_init(B0, f - 23); PIN(B0); }
          req_pace<f - 32, 4, 0, 8, 2, false>(c, a, k, lv, ab4);
          if constexpr (f >= 56) check_step<f - 64, 22, 2, false>(c, m0, m1, a, k, lv, ab4, tagc);
        });
      }
      if (opq(ng) == 2) {
        const unsigned tg0 = (unsigned)__builtin_amdgcn_s_memrealtime();
        sfor<20>([&](auto F) INL { hside(F, B0, B1, hb + (16 + m) * REC); });
        res_back();
#pragma unroll
        for (int i = 0; i < 8; ++i) res_init(B0, i);
        while ((unsigned)__builtin_amdgcn_s_memrealtime() - tg0 < (unsigned)NG2S) __builtin_amdgcn_s_sleep(2);
        sfor<16>([&](auto F) INL { req_step<F.v, 0, 8, 2, false>(c, a, k, lv, ab4); SB(); });
        wpre<4>(c, tb0, tb0);
        settle<2, 2, false>(c, rec_bad<false>(a, k, 0, tagc) | rec_bad<false>(a, k, 1, tagc), a, k, lv, ab4, 0, tagc);
        while ((unsigned)__builtin_amdgcn_s_memrealtime() - tg0 < (unsigned)NG2T) __builtin_amdgcn_s_sleep(2);
      }
      {
        const int h2 = ng == 3 ? hb + (32 + g0 - 2 * m) * REC : WS_END;
        const int ab21 = kh ? WS_END : ab4;
        const unsigned t21 = kh ? 0u : tagc;
        auto side4 = [&](auto F) INL {
          constexpr int f = F.v;
          if constexpr (f < 10) { hside(IC<2 * f>{}, A0, A1, h2); hside(IC<2 * f + 1>{}, A0, A1, h2); }
          req_pace<f, 4, 8, 16, 2, false>(c, a, k, lv, ab4);
          req_pace<f - S2H, 3, 16, 21, 2, false>(c, a, k, lv, ab4);
          req_pace<f - S2H - 15, 3, 21, 22, 2, false>(c, a, k, lv, ab21);
          if constexpr (f < 72) check_step<f, 22, 2, false>(c, m0, m1, a, k, lv, ab4, tagc);
          if constexpr (f == 74) settle<1, 2, false>(c, rec_bad<false>(a, k, 20, tagc), a, k, lv, ab4, 20, tagc);
          if constexpr (f == 78) settle<1, 2, false>(c, rec_bad<false>(a, k, 21, t21), a, k, lv, ab21, 21, t21);
          if constexpr (f == 85 || f == 86) res_send(B1, f - 85, (unsigned)(layer * 0x200 + 0x101));
        };
        gemm<88, 2, 4, -1, false, 2, 32, 3, 7, 1>(c, a, B0, B1, tb0, tb0, 0, 0, side4);
        tagc += 0x632BE5AAu;
        res_epilogue(B0, 0, layer == 1, (unsigned)(layer * 0x200 + 0x101));
      }
      if (layer == 1) break;
      wpre<0>(c, lv, lv);
      const int xb1 = WS_X + clb + w * 16 * REC;
      open_phase<1, true>(c, a, k, lv, xb1, tagc);
      gemm<64, 2, 0, -1, true, 3, 34, 0, 99>(c, a, A0, A1, lv, lv, 0, 0, [&](auto F) INL {
        constexpr int f = F.v;
        if constexpr (f == 0) { sacc = u2f(k[0].y) + u2f(k[1].y); PIN(sacc); }
        req_step<f, 4, 10, 1, true>(c, a, k, lv, xb1);
        req_pace<f - 12, 4, 10, 16, 1, true>(c, a, k, lv, xb1);
        check_step<f, 16, 1, true>(c, m0, m1, a, k, lv, xb1, tagc);
        if constexpr ((f & 7) == 0 && f >= 8 && f <= 56) { sacc += u2f(k[f / 4].y) + u2f(k[f / 4 + 1].y); PIN(sacc); }
        if constexpr (f == 57) {
          const auto sw = __builtin_amdgcn_permlane32_swap(f2u(sacc), f2u(sacc), false, false);
          rstd = __builtin_amdgcn_rsqf(set_stats(u2f((unsigned)sw[0]), u2f((unsigned)sw[1])));
          PIN(mfrag);
          rs_put();
        }
        if (f == 56) cs0 = cs_frag(0);
        if (f == 60) { A0 = mfma(mfrag, cs0, A0); SB(); cs0 = cs_frag(1); }
        if (f == 63) A1 = mfma(mfrag, cs0, A1);
        if (f >= 59 && f < 63) rs_get(f - 59);
      });
      tagc += 0x632BE5AAu;
    }
  } else if (wave < 6) {
    const int lw = wave - 4;
    LANEVARS
    c.lane = lane;
    volatile LAS unsigned* full = flg(c, F_FULL + lw);
    __builtin_amdgcn_s_setprio(3);
    load_half0(0);
    if (lw == 1) glds((const char*)(pc0 & ~(u64)255) + j * (CODEPF / 32), min(lane * 32, CODEPF / 32 - 32), L_RING + 7 * SLOT + 12288);
    __builtin_amdgcn_s_setprio(2);
    load_half0(1);
    VMWAIT(8); *full = 1u;
    load_half0(2);
    load_half0(3);
    VMWAIT(16); *full = 2u;
    {
      const unsigned tv = min(lane, 42) * 128, td = L_RING + (6 + lw) * SLOT + (lw ? 13312 : 3072);
      glds((const char*)(lw ? wu : wg) + b * 5504, tv, td);
      if (lw) glds((const char*)wd + b * 5504, tv, td);
      else glds((const char*)wo + b * 2048, (lane & 15) * 128, td);
      *flg(c, F_QGO + lw) = 1u;
    }
    VMWAIT(10); *full = 3u;
    VMWAIT(2); *full = 4u;
    __builtin_amdgcn_s_setprio(1);
    wait_lds<4, 1>(c, F_FREE, 1u);
    load_half0(4);
    load_half0(5);
    VMWAIT(8); *full = 5u;
    VMWAIT(0); *full = 6u;
    int seq = 6, pub = 6;
    v4u wc[48];
    auto stream = [&](int seg, int i0, int i1) INL {
      const int ln = fresh_lane(), lv = ln * 16;
      const unsigned vo2 = (ln >> 3) * 1024 + (((ln & 7) ^ (((ln >> 4) & 1) << 2)) * 16);
      const int src = (seg == 0 ? WS_WT1 + m * 96 * KB : WS_WT3 + g0 * 64 * KB);
#pragma unroll 1
      for (int i = i0; i < i1; ++i) {
        if (seq >= NSLOT) {
          const unsigned need = (unsigned)(seq - NSLOT + 1);
          bool first = true;
#pragma nounroll
          for (unsigned sp = 0; sp < SPIN_CAP; ++sp) {
            v4u f = *(volatile LAS v4u*)flg(c, F_FREE);
            if ((unsigned)__builtin_amdgcn_readfirstlane(umin(umin(f.x, f.y), umin(f.z, f.w))) >= need) break;
            if (first && pub < seq) { VMWAIT(0); pub = seq; *full = (unsigned)pub; }
            first = false;
            __builtin_amdgcn_s_sleep(4);
          }
          MEMBAR();
        }
        const unsigned dst = L_RING + (seq & (NSLOT - 1)) * SLOT + lw * 8192;
        const int hs = i * 2 + lw, hr = (seg == 2 && hs < 16) ? (int)((DUAL >> (4 * hs)) & 15) : hs;
        if (seg == 1) {
          const char* sp = (const char*)wo + (size_t)hr * 65536 + cg * 128;
#pragma unroll
          for (int e = 0; e < 8; ++e) glds(sp + e * 8192, vo2, dst + e * 1024);
        } else {
          glds4(ws + src + hr * 8192, lv, dst);
          glds4(ws + src + hr * 8192 + 4096, lv, dst + 4096);
        }
        ++seq;
        if (seq - pub > 2) { VMWAIT(16); pub = seq - 2; *full = (unsigned)pub; }
      }
    };
    auto drain = [&]() INL { VMWAIT(0); if (pub < seq) { pub = seq; *full = (unsigned)seq; } };
    wait_lds<2, 1>(c, F_JOB, 1u);
    stream(1, 0, 4);
    drain();
    if (lw == 0) { poll_tags<32>(c, WS_F1 + m * 2048, *(volatile LAS v2u*)flg(c, F_TAG)); *flg(c, F_PTOK) = 1u; }
    else wait_lds<1, 1>(c, F_PTOK, 1u);
    stream(2, 0, 4);
    VMWAIT(7);
    cache_load(wc, IC<6>{});
    stream(2, 4, 8);
    drain();
    wait_lds<4, 1>(c, F_XF, 0x100u);
    cache_conv(wc, IC<6>{});
    stream(2, 8, 4 * ng);
    drain();
    cache_put(wc, IC<6>{}, seq);
    seq += 11; pub = seq;
    stream(0, 0, 6);
    stream(1, 0, 4);
    stream(2, 0, 4 * ng);
    drain();
    cache_put(wc, IC<6>{}, seq);
  } else {
    const int hh = wave - 6;
    LANEVARS
    c.lane = lane;
    const int pts = L_RING + (6 + hh) * SLOT;
    __builtin_amdgcn_s_setprio(3);
    {
      const unsigned vo1 = (lane >> 2) * 3072 + (lane & 3) * 16;
      const char* j0 = (const char*)wqkv + (size_t)(cl * 32) * 3072 + m * 64;
      glds(j0 + (1 + hh) * 1024, vo1, pts); glds(j0 + (1 + hh) * 1024 + 16 * 3072, vo1, pts + 1024);
      glds(j0 + hh * 16 * 3072, vo1, pts + 2048);
    }
    const int srow = cl * 128 + m * 8 + hh * 4 + (lane >> 4);
    v4u xs[4];
#pragma unroll
    for (int i = 0; i < 4; ++i) xs[i] = *(const GAS v4u*)((const GAS char*)x + (size_t)srow * 1024 + i * 256 + (lane & 15) * 16);
    const v2u e0 = __builtin_amdgcn_raw_buffer_load_b64(c.rs, 0, WS_E, 16);
    VMWAIT(0);
    const u64 tag = mix64(((u64)(unsigned)__builtin_amdgcn_readfirstlane(e0.y) << 32) | (unsigned)__builtin_amdgcn_readfirstlane(e0.x));
    const v2u tag2 = {(unsigned)tag, (unsigned)(tag >> 32)};
    if (hh == 0) *(volatile LAS v2u*)flg(c, F_TAG) = tag2;
    {
      const unsigned tb = ((unsigned)(mix64(tag) >> 32) >> ((lane & 3) * 8)) & 0xffu;
      const v4u f0 = tr_frag(L + pts + (hh ? trS : trN)), f1 = tr_frag(L + pts + 1024 + (hh ? trS : trN)), f2 = tr_frag(L + pts + 2048 + trN);
      const float s0 = red_h(csum(f1, csum(f0, 0.f))), s2 = red_h(csum(f2, 0.f));
      __builtin_amdgcn_raw_buffer_store_b32((f2u(h ? s2 : s0) & 0xffffff00u) | tb, c.rs, l31 * 4 + h * 4096, WS_C1P + m * 8 * KB + cl * 256 + hh * 128, 16);
      float sa = 0.f, sb = 0.f, qa = 0.f, qb = 0.f;
#pragma unroll
      for (int i = 0; i < 4; ++i) {
        sa = dot2(xs[i].y, ONES2, dot2(xs[i].x, ONES2, sa)); sb = dot2(xs[i].w, ONES2, dot2(xs[i].z, ONES2, sb));
        qa = dot2(xs[i].y, xs[i].y, dot2(xs[i].x, xs[i].x, qa)); qb = dot2(xs[i].w, xs[i].w, dot2(xs[i].z, xs[i].z, qb));
      }
      sa += sb; qa += qb;
      sa += dpp<0x128>(sa); qa += dpp<0x128>(qa); sa += dpp<0x124>(sa); qa += dpp<0x124>(qa);
      sa += dpp<0x122>(sa); qa += dpp<0x122>(qa); sa += dpp<0x121>(sa); qa += dpp<0x121>(qa);
      if ((lane & 15) == 0) st_ws(c, v4u{f2u(sa), tag2.x, f2u(qa), tag2.y}, srow * 16, WS_S0P);
      {
        const unsigned nt = ~(hh ? tag2.y : tag2.x);
        const v4u W = {nt, nt >> 8, nt >> 16, nt >> 24};
        const int kvb = WS_KVP + ((hh * 16 + m) * 16 + cl) * KVR;
        st_ws(c, W, lv, kvb); st_ws(c, W, lv + KB, kvb);
        if (lane < 8) st_ws(c, W, lv + 2 * KB, kvb);
      }
      VMWAIT(0);
      __builtin_amdgcn_s_sleep(8);
      {
        float t;
#pragma nounroll
        for (unsigned sp = 0; sp < SPIN_CAP; ++sp) {
          unsigned p[16];
#pragma unroll
          for (int i = 0; i < 16; ++i) p[i] = __builtin_amdgcn_raw_buffer_load_b32(c.rs, lane * 4 + i * 256, WS_C1P + m * 8 * KB + hh * 4096, 16);
          SB();
          unsigned chk = 0;
          t = 0.f;
#pragma unroll
          for (int i = 0; i < 16; ++i) { p[i] ^= tb; chk |= p[i]; t += u2f(p[i]); }
          if (!__any((chk & 0xffu) != 0u)) break;
          MEMBAR();
        }
        if (hh) t = red_h(t);
        if (hh == 0 || h == 0) *(LAS v4u*)(L + L_CS + hh * 1024 + lv) = cs_operand(t);
        MEMBAR();
        *flg(c, F_CS1R + hh) = 1u;
      }
      __builtin_amdgcn_s_setprio(1);
      wait_lds<2, 1>(c, F_QGO, 1u);
      const int pt3 = pts + 4096;
      const unsigned vo3 = (lane >> 2) * 2752 + (lane & 3) * 16;
      const char* w3 = (const char*)(hh ? wu : wg) + (size_t)(cl * 32) * 2752;
#pragma unroll
      for (int i = 0; i < 3; ++i) {
        const int aa = g0 + min(i, ng - 1);
        glds(w3 + aa * 64, vo3, pt3 + i * 2048);
        glds(w3 + aa * 64 + 16 * 2752, vo3, pt3 + i * 2048 + 1024);
      }
      st_ws(c, f0, lv, WS_WT1 + m * 96 * KB + (cl * 4 + hh) * KB);
      st_ws(c, f1, lv, WS_WT1 + m * 96 * KB + (cl * 4 + 2 + hh) * KB);
      st_ws(c, f2, lv, WS_WT1 + m * 96 * KB + (64 + cl * 2 + hh) * KB);
      VMWAIT(0);
#pragma unroll
      for (int i = 0; i < 3; ++i) {
        const int aa = g0 + min(i, ng - 1);
        const v4u g0f = tr_frag(L + pt3 + i * 2048 + trS), g1f = tr_frag(L + pt3 + i * 2048 + 1024 + trS);
        st_ws(c, g0f, lv, WS_WT3 + aa * 64 * KB + (cl * 4 + hh) * KB);
        st_ws(c, g1f, lv, WS_WT3 + aa * 64 * KB + (cl * 4 + 2 + hh) * KB);
        const float s = red_h(csum(g1f, csum(g0f, 0.f)));
        if (h == 0) __builtin_amdgcn_raw_buffer_store_b32(f2u(s), c.rs, l31 * 4, WS_CSP + aa * 4 * KB + cl * 256 + hh * 128, 16);
      }
      MEMBAR();
      *flg(c, F_JOB + hh) = 1u;
    }
    VMWAIT(0);
    if (lane == 0) __builtin_amdgcn_raw_buffer_store_b64(tag2, c.rs, 0, WS_F1 + ((m * 16 + cl) * 2 + hh) * 64, 16);
    v4u wc[40];
    cache_load(wc, IC<5>{});
    cache_conv(wc, IC<5>{});
    if (hh == 1) {
      wait_lds<1, 2>(c, F_PTOK, 1u);
#pragma unroll 1
      for (int i = 0; i < ng; ++i) {
        float t = 0.f;
#pragma unroll
        for (int q = 0; q < 16; ++q) t += u2f(__builtin_amdgcn_raw_buffer_load_b32(c.rs, lane * 4 + q * 256, WS_CSP + (g0 + i) * 4 * KB, 16));
        *(LAS v4u*)(L + L_CS + (3 + 2 * i) * 512 + lv) = cs_operand(t);
      }
      MEMBAR();
      *flg(c, F_CS3R) = 1u;
      MEMBAR();
    }
    __builtin_amdgcn_s_setprio(1);
    cache_put(wc, IC<5>{}, 10 + 4 * ng);
    if (b == 0 && wave == 6 && lane == 0) __hip_atomic_store((GAS u64*)(ws + WS_E), tag, RLX_AGENT);
    __builtin_amdgcn_s_setprio(1);
    cache_put(wc, IC<5>{}, 31 + 8 * ng);
  }
}

extern "C" void kernel_launch(void* const* d_in, const int* in_sizes, int n_in, void* d_out, int out_size, void* d_ws, size_t ws_size,
                              hipStream_t stream) {
  fused_fwd<<<256, 512, 0, stream>>>((const u16*)d_in[0], (const u16*)d_in[1], (const u16*)d_in[2], (const u16*)d_in[4], (const u16*)d_in[5],
                                     (const u16*)d_in[6], (u16*)d_out, (char*)d_ws);
}
```

```cpp
#include <hip/hip_runtime.h>
#include <stdint.h>
#include <utility>

#define GAS __attribute__((address_space(1)))
#define LAS __attribute__((address_space(3)))
#define RLX_AGENT __ATOMIC_RELAXED, __HIP_MEMORY_SCOPE_AGENT
typedef unsigned v4u __attribute__((ext_vector_type(4)));
typedef unsigned v2u __attribute__((ext_vector_type(2)));
typedef float f32x2 __attribute__((ext_vector_type(2)));
typedef float f32x4 __attribute__((ext_vector_type(4)));
typedef float f32x16 __attribute__((ext_vector_type(16)));
typedef short s16x8 __attribute__((ext_vector_type(8)));
typedef short s16x4 __attribute__((ext_vector_type(4)));
typedef __bf16 bf16x2 __attribute__((ext_vector_type(2)));
typedef unsigned short u16;
typedef unsigned long long u64;

#define STAMP 0
#define CYC 0
#define K_TOUCH 1
#define K_CE 1
#define K_KVW 1
#define K_ROT 1
#define K_XD 12
constexpr int XD = K_XD;
#define K_YOFF 14
constexpr int YOFF = K_YOFF;
#define K_KVDELAY 18
constexpr int KVDELAY = K_KVDELAY;
#define K_KVDP 14
constexpr int KVDP = K_KVDP;
#define K_BACKOFF 4
constexpr int BACKOFF = K_BACKOFF;
#define K_NG2S 90
constexpr int NG2S = K_NG2S;
#define K_NG2T 150
constexpr int NG2T = K_NG2T;
#define K_S2H 44
constexpr int S2H = K_S2H;
#define K_XPRE 5
constexpr int XPRE = K_XPRE;
constexpr int CODEPF = 59392;

constexpr int KB = 1024;
constexpr int WS_E = 0;
constexpr int WS_F1 = 4 * KB;
constexpr int WS_WT1 = 128 * KB;
constexpr int WS_WT3 = WS_WT1 + 16 * 96 * KB;
constexpr int WS_CSP = WS_WT3 + 43 * 64 * KB;
constexpr int WS_S0P = WS_CSP + 43 * 4 * KB;
constexpr int WS_C1P = WS_S0P + 32 * KB;
constexpr int KVR = 2176;
constexpr int WS_KVP = WS_C1P + 16 * 8 * KB;
constexpr int REC = 2560;
constexpr int WS_ACT = 6 * KB * KB, ACT_CL = KB * KB;
constexpr int WS_O = WS_ACT;
constexpr int WS_X = WS_O + 4 * 16 * REC;
constexpr int WS_H = WS_X + 2 * 4 * 16 * REC;
static_assert(WS_KVP + 512 * KVR <= WS_ACT && WS_H + 4 * 43 * REC <= WS_ACT + ACT_CL, "ws");
constexpr int WS_DBG = WS_ACT + 16 * ACT_CL;
constexpr int WS_END = WS_DBG + 256 * 2 * KB;

constexpr int SLOT = 16384, NSLOT = 8;
constexpr int NSTAMP = 36;
constexpr int L_FLG = 0;
constexpr int L_ZF = L_FLG + 128;
constexpr int L_CS = L_ZF + 64;
constexpr int L_KVF = L_CS + 9 * 512;
constexpr int L_STAMP = L_KVF + 1024;
constexpr int L_KVS = L_STAMP + 1152;
constexpr int L_XCH = 16384;
constexpr int L_RING = 32768;
constexpr int L_END = L_RING + NSLOT * SLOT;
static_assert(L_KVS + 4 * 2304 <= L_XCH && L_END == 163840 && (L_KVS & 15) == 0 && (L_CS & 15) == 0, "lds");
enum { F_FULL = 0, F_TAG = 2, F_FREE = 4, F_JOB = 8, F_KFL = 12, F_QGO = 10, F_CONV = 24, F_CS3R = 31, F_PTOK = 30, F_KVR = 16, F_XF = 20, F_CS1R = 28 };
constexpr unsigned SPIN_CAP = 1u << 17;

template <int V> struct IC { static constexpr int v = V; __device__ constexpr operator int() const { return V; } };
template <class F, int... I> __device__ __forceinline__ void sfor_(F&& fn, std::integer_sequence<int, I...>) { (fn(IC<I>{}), ...); }
template <int N, class F> __device__ __forceinline__ void sfor(F&& fn) { sfor_(fn, std::make_integer_sequence<int, N>{}); }
#define INL __attribute__((always_inline))
#define MEMBAR() asm volatile("" ::: "memory")
#define SB() __builtin_amdgcn_sched_barrier(0)
#define VMWAIT(n) asm volatile("s_waitcnt vmcnt(" #n ")" ::: "memory")
#define PIN(x) asm volatile("" : "+v"(x))

__device__ __forceinline__ unsigned umin(unsigned a, unsigned b) { return a < b ? a : b; }
__device__ __forceinline__ int gsplit(int i) { return (43 * i) >> 4; }
__device__ __forceinline__ f32x16 mfma(v4u a, v4u b, f32x16 c) {
  return __builtin_amdgcn_mfma_f32_32x32x16_bf16(__builtin_bit_cast(s16x8, a), __builtin_bit_cast(s16x8, b), c, 0, 0, 0);
}
__device__ __forceinline__ f32x16 zero16() { return f32x16{0.f, 0.f, 0.f, 0.f, 0.f, 0.f, 0.f, 0.f, 0.f, 0.f, 0.f, 0.f, 0.f, 0.f, 0.f, 0.f}; }
__device__ __forceinline__ unsigned pack2(float a, float b) {
  bf16x2 v = {(__bf16)a, (__bf16)b};
  return __builtin_bit_cast(unsigned, v);
}
__device__ __forceinline__ float u2f(unsigned u) { return __builtin_bit_cast(float, u); }
__device__ __forceinline__ unsigned f2u(float f) { return __builtin_bit_cast(unsigned, f); }
__device__ __forceinline__ float blo(unsigned u) { return u2f(u << 16); }
__device__ __forceinline__ float bhi(unsigned u) { return u2f(u & 0xffff0000u); }
__device__ __forceinline__ float relu(float x) { return u2f((unsigned)max((int)f2u(x), 0)); }
__device__ __forceinline__ float dot2(unsigned a, unsigned b, float acc) {
  return __builtin_amdgcn_fdot2_f32_bf16(__builtin_bit_cast(bf16x2, a), __builtin_bit_cast(bf16x2, b), acc, false);
}
constexpr unsigned ONES2 = 0x3f803f80u;
constexpr float LOG2E = 1.44269504f;
__device__ __forceinline__ float red_h(float v) {
  auto s = __builtin_amdgcn_permlane32_swap(f2u(v), f2u(v), false, false);
  return u2f((unsigned)s[0]) + u2f((unsigned)s[1]);
}
__device__ __forceinline__ float red_g(float v) {
  auto r = __builtin_amdgcn_permlane16_swap(f2u(v), f2u(v), false, false);
  return red_h(u2f((unsigned)r[0]) + u2f((unsigned)r[1]));
}
template <int CTRL> __device__ __forceinline__ float dpp(float v) { return u2f(__builtin_amdgcn_update_dpp(0, (int)f2u(v), CTRL, 0xf, 0xf, false)); }
__device__ __forceinline__ float exp2c(float x) { return __builtin_amdgcn_fmed3f(__builtin_amdgcn_exp2f(x), 0.f, 1.f); }
__device__ __forceinline__ void split2(float v, unsigned& hi, unsigned& lo) {
  hi = pack2(v, 0.f) & 0xffffu;
  lo = pack2(v - blo(hi), 0.f) & 0xffffu;
}
template <int D = 256> __device__ __forceinline__ v4u tr_frag(LAS char* p) {
  v2u lo = __builtin_bit_cast(v2u, __builtin_amdgcn_ds_read_tr16_b64_v4i16((LAS s16x4*)p));
  v2u hi = __builtin_bit_cast(v2u, __builtin_amdgcn_ds_read_tr16_b64_v4i16((LAS s16x4*)(p + D)));
  return v4u{lo.x, lo.y, hi.x, hi.y};
}
__device__ __forceinline__ void glds(const void* sbase, unsigned voff, unsigned ldst) {
  asm volatile("s_mov_b32 m0, %2\n\ts_nop 0\n\tglobal_load_lds_dwordx4 %0, %1" ::"v"(voff), "s"(sbase), "s"(ldst) : "memory");
}
__device__ __forceinline__ void glds4(const void* sbase, unsigned voff, unsigned ldst) {
  asm volatile(
      "s_mov_b32 m0, %2\n\ts_nop 0\n\tglobal_load_lds_dwordx4 %0, %1 sc1\n\tglobal_load_lds_dwordx4 %0, %1 offset:1024 sc1\n\t"
      "global_load_lds_dwordx4 %0, %1 offset:2048 sc1\n\tglobal_load_lds_dwordx4 %0, %1 offset:3072 sc1" ::"v"(voff), "s"(sbase), "s"(ldst)
      : "memory");
}

struct Ctx {
  LAS char* L;
  __amdgpu_buffer_rsrc_t rs;
  int lane, wave;
  int seq, known;
  int vb0, vb1, nb0, nb1;
  int fa;
  v2u fpre;
  v4u w0, w1, w2, w3;
  int nst;
  unsigned dbgt;
  template <int I> __device__ __forceinline__ v4u& wf() { if constexpr (I == 0) return w0; else if constexpr (I == 1) return w1; else if constexpr (I == 2) return w2; else return w3; }
};
__device__ __forceinline__ volatile LAS unsigned* flg(Ctx& c, int i) { return (volatile LAS unsigned*)(c.L + L_FLG) + i; }
__device__ __forceinline__ void stamp0(Ctx& c) {
}
template <int K> __device__ __forceinline__ void stamp(Ctx& c) { if (STAMP == K) stamp0(c); }
template <int N, int SLP> __device__ __forceinline__ void wait_lds(Ctx& c, int i, unsigned val) {
#pragma nounroll
  for (unsigned sp = 0; sp < SPIN_CAP; ++sp) {
    unsigned v;
    if (N == 1) v = *flg(c, i);
    else if (N == 2) { v2u f = *(volatile LAS v2u*)flg(c, i); v = umin(f.x, f.y); }
    else { v4u f = *(volatile LAS v4u*)flg(c, i); v = umin(umin(f.x, f.y), umin(f.z, f.w)); }
    if ((unsigned)__builtin_amdgcn_readfirstlane(v) >= val) break;
    if (SLP) __builtin_amdgcn_s_sleep(SLP);
  }
  MEMBAR();
}
template <int N> __device__ __forceinline__ void poll_tags(Ctx& c, int base, v2u tag) {
#pragma nounroll
  for (unsigned sp = 0; sp < SPIN_CAP; ++sp) {
    const v2u v = __builtin_amdgcn_raw_buffer_load_b64(c.rs, (c.lane & (N - 1)) * 64, base, 16);
    if (__all(v.x == tag.x && v.y == tag.y)) break;
    __builtin_amdgcn_s_sleep(4);
    MEMBAR();
  }
  MEMBAR();
}
__device__ __forceinline__ v4u ld_ws(Ctx& c, int voff, int soff) { return __builtin_amdgcn_raw_buffer_load_b128(c.rs, voff, soff, 16); }
__device__ __forceinline__ void st_ws(Ctx& c, v4u v, int voff, int soff) {
  __builtin_amdgcn_raw_buffer_store_b128(v, c.rs, voff, soff, 16);
  asm volatile("s_nop 1" ::"v"(v) : "memory");
}
__device__ __forceinline__ int fresh_lane() { int l; asm volatile("v_mbcnt_lo_u32_b32 %0, -1, 0\n\tv_mbcnt_hi_u32_b32 %0, -1, %0" : "=v"(l)); __builtin_assume(l >= 0 && l < 64); return l; }
__device__ __forceinline__ int opq(int v) { asm volatile("" : "+s"(v)); return v; }
__device__ __forceinline__ int opqv(int v) { asm volatile("" : "+v"(v)); return v; }

constexpr int fps(int mode) { return (mode == 1 || mode == 4) ? 8 : 16; }
constexpr bool two(int mode) { return mode == 1 || mode == 2; }
__device__ __forceinline__ void slot_wait(Ctx& c, int sq) {
  if (__builtin_expect(c.known <= sq, 0)) {
    if (CYC == 2) c.dbgt -= (unsigned)__builtin_amdgcn_s_memtime();
#pragma nounroll
    for (unsigned sp = 0; sp < SPIN_CAP; ++sp) {
      v2u f = *(volatile LAS v2u*)flg(c, F_FULL);
      c.known = __builtin_amdgcn_readfirstlane(umin(f.x, f.y));
      if (c.known > sq) break;
    }
    if (CYC == 2) c.dbgt += (unsigned)__builtin_amdgcn_s_memtime();
  }
  MEMBAR();
}
template <int MODE> __device__ __forceinline__ void slot_base(Ctx& c, int sq, int lb0, int lb1) {
  const int sb = (sq & (NSLOT - 1)) * SLOT;
  c.nb0 = lb0 + sb; PIN(c.nb0);
  if (two(MODE)) { c.nb1 = lb1 + sb; PIN(c.nb1); }
}
template <int MODE> __device__ __forceinline__ void slot_next(Ctx& c) {
  c.nb0 = (c.vb0 + SLOT) & (NSLOT * SLOT - 1); PIN(c.nb0);
  if (two(MODE)) { c.nb1 = (c.vb1 + SLOT) & (NSLOT * SLOT - 1); PIN(c.nb1); }
}
template <int MODE, int F, bool OPEN = (F % fps(MODE) == 0)> __device__ __forceinline__ v4u wread(Ctx& c) {
  constexpr int i = F % fps(MODE);
  if constexpr (OPEN) { c.vb0 = c.nb0; if (two(MODE)) c.vb1 = c.nb1; }
  LAS char* R = c.L + L_RING;
  if (MODE == 0) return *(LAS v4u*)(R + c.vb0 + i * KB);
  if (MODE == 1) return tr_frag<512>(R + ((i & 1) ? c.vb1 : c.vb0) + ((i >> 1) & 1) * 2 * KB + (i >> 2) * 8 * KB);
  if (MODE == 4) return *(LAS v4u*)(R + c.vb0 + (i & 3) * KB + (i >> 2) * 8 * KB);
  if (MODE == 2) return tr_frag(R + ((i & 1) ? c.vb1 : c.vb0) + i * KB);
  return tr_frag(R + c.vb0 + i * KB);
}
__device__ __forceinline__ void ring_publish(Ctx& c) { MEMBAR(); *(volatile LAS unsigned*)(c.L + c.fa) = (unsigned)c.seq; MEMBAR(); }
template <int MODE> __device__ __forceinline__ void wpre(Ctx& c, int lb0, int lb1) {
  slot_wait(c, c.seq);
  slot_base<MODE>(c, c.seq, lb0, lb1);
  sfor<4>([&](auto F) INL { c.wf<F.v>() = wread<MODE, F.v>(c); });
  if (fps(MODE) == 8) c.fpre = *(volatile LAS v2u*)flg(c, F_FULL);
}
struct NoSide { template <class T> __device__ __forceinline__ void operator()(T) const {} };
constexpr int fmap(int f, int nf, int fpsl, int tf) {
  if (tf < 0 || f < nf - fpsl || f >= nf) return f;
  const int i = f - (nf - fpsl), hf = fpsl / 2;
  return nf - fpsl + 2 * (i % hf) + (i < hf ? tf : 1 - tf);
}
constexpr u64 DUAL = 0xFEDC76B54A329180ull;
constexpr int dual_first(int ch) { for (int u = 0; u < 16; ++u) if (((DUAL >> (4 * u)) & 15) == (u64)ch) return u; return -1; }
template <int NF, int TPS, int MODE, int NEXT, bool XA, int ZERO, int RELF, int EARLY, int CAP, int TF, class SIDE>
__device__ __forceinline__ void gemm4(Ctx& c, v4u (&a)[32], f32x16& c0, f32x16& c1, f32x16& c2, f32x16& c3, int lb0, int lb1, int nb0, int nb1, SIDE side) {
  constexpr int FPS = fps(MODE);
  static_assert(NF % FPS == 0, "whole slots");
  sfor<NF>([&](auto F) INL {
    constexpr int f = F.v, un = NF == 128 ? (int)((DUAL >> (4 * (f / 8))) & 15) : 0;
    constexpr int fm = fmap(f, NF, FPS, TF);
    constexpr int s = NF == 128 ? 4 * (un & 7) + (f % 8) / 2 : fm / TPS, t = fm % TPS, fn = f + 4, i = fn % FPS;
    const v4u x = a[s & 31], wv = c.wf<f % 4>();
    f32x16& acc = un >= 8 ? (t ? c3 : c2) : (t ? c1 : c0);
    const f32x16 cin = (s == 0 && ((ZERO >> t) & 1)) ? zero16() : acc;
    acc = XA ? mfma(x, wv, cin) : mfma(wv, x, cin);
    PIN(acc);
    SB();
    if constexpr (fn < NF) {
      c.wf<f % 4>() = wread<MODE, fmap(fn, NF, FPS, TF), i == 0>(c);
      constexpr bool lastslot = fn / FPS == NF / FPS - 1;
      if constexpr (!lastslot || NEXT >= 0) {
        constexpr int sl = fn / FPS, ahead = FPS == 8 && sl + 2 < NF / FPS ? 2 : 1;
        if constexpr (FPS == 16 || !(sl & 1)) {
          if constexpr (i == (FPS == 8 ? 2 : FPS - 7)) c.fpre = *(volatile LAS v2u*)flg(c, F_FULL);
          if constexpr (i == FPS - 4) { c.fpre.x = umin(c.fpre.x, c.fpre.y); PIN(c.fpre); }
          if constexpr (i == FPS - 3) { c.known = __builtin_amdgcn_readfirstlane(c.fpre.x); asm volatile("" : "+s"(c.known)); }
          if constexpr (i == FPS - 1) slot_wait(c, c.seq + ahead);
        }
        if constexpr (i == FPS - 3) {
          if constexpr (lastslot && NEXT != MODE) slot_base<(NEXT < 0 ? 0 : NEXT)>(c, c.seq + 1, nb0, nb1); else slot_next<MODE>(c);
        }
      }
      if constexpr (i == FPS - 1) c.seq++;
    } else if constexpr (NEXT >= 0) {
      c.wf<f % 4>() = wread<(NEXT < 0 ? 0 : NEXT), fn - NF>(c);
      if constexpr (fps(NEXT) == 8 && fn - NF == 1) c.fpre = *(volatile LAS v2u*)flg(c, F_FULL);
    }
    if constexpr (fn >= FPS && fn <= NF + 2 && i == 2) { if constexpr ((f >= RELF || fn / FPS - 1 < EARLY) && fn / FPS - 1 < CAP) ring_publish(c); }
    else if constexpr (f == RELF && RELF > 0) ring_publish(c);
    side(F);
    SB();
  });
}
template <int NF, int TPS, int MODE, int NEXT, bool XA, int ZERO, int RELF, int EARLY, int CAP, int TF = -1, class SIDE>
__device__ __forceinline__ void gemm(Ctx& c, v4u (&a)[32], f32x16& c0, f32x16& c1, int lb0, int lb1, int nb0, int nb1, SIDE side) {
  gemm4<NF, TPS, MODE, NEXT, XA, ZERO, RELF, EARLY, CAP, TF>(c, a, c0, c1, c0, c1, lb0, lb1, nb0, nb1, side);
}
__device__ __forceinline__ unsigned sum4(unsigned m, v4u v) { return ((m + v.x + v.y) + v.z + v.w); }
template <bool EX> __device__ __forceinline__ void ld_chk(Ctx& c, v2u& k, int sb) {
  if (EX) k = __builtin_amdgcn_raw_buffer_load_b64(c.rs, c.lane * 8 + 2 * KB, sb, 16);
  else k.x = __builtin_amdgcn_raw_buffer_load_b32(c.rs, c.lane * 4 + 2 * KB, sb, 16);
}
template <int STR, bool EX> __device__ __forceinline__ void ld_rec(Ctx& c, v4u (&a)[32], v2u (&k)[16], int lv, int sb, int p, int e) {
  const int so = sb + STR * p * REC;
  if (e < 2) a[(2 * p + e) & 31] = ld_ws(c, lv + e * KB, so);
  else ld_chk<EX>(c, k[p & 15], so);
}
template <bool EX> __device__ __forceinline__ unsigned rec_bad(v4u (&a)[32], v2u (&k)[16], int p, unsigned tag) {
  return sum4(sum4(EX ? k[p & 15].x + k[p & 15].y : k[p & 15].x, a[(2 * p) & 31]), a[(2 * p + 1) & 31]) ^ tag;
}
template <int N, int STR, bool EX> __device__ __forceinline__ void settle(Ctx& c, unsigned bad, v4u (&a)[32], v2u (&k)[16], int lv, int sb, int p0, unsigned tag) {
  if (__builtin_expect(__any(bad != 0u), 0)) {
    if (CYC == 3) c.dbgt -= (unsigned)__builtin_amdgcn_s_memtime();
    unsigned sp = 0;
#pragma nounroll
    do {
      __builtin_amdgcn_s_sleep(BACKOFF);
      MEMBAR();
      bad = 0;
#pragma unroll
      for (int i = 0; i < 3 * N; ++i) ld_rec<STR, EX>(c, a, k, lv, sb, p0 + i / 3, i % 3);
#pragma unroll
      for (int i = 0; i < N; ++i) bad |= rec_bad<EX>(a, k, p0 + i, tag);
    } while (__any(bad != 0u) && ++sp < SPIN_CAP);
    if (CYC == 3) c.dbgt += (unsigned)__builtin_amdgcn_s_memtime();
  }
}
template <int F, int NP, int STR, bool EX>
__device__ __forceinline__ void check_step(Ctx& c, unsigned& m0, unsigned& m1, v4u (&a)[32], v2u (&k)[16], int lv, int sb, unsigned tag) {
  constexpr int p = 2 * ((F + 8) / 8), r = (F + 8) % 8;
  if constexpr (p < NP) {
    if constexpr (r == 2) { m0 = sum4(EX ? k[p & 15].x + k[p & 15].y : k[p & 15].x, a[(2 * p) & 31]); PIN(m0); }
    if constexpr (r == 3) { m0 = sum4(m0, a[(2 * p + 1) & 31]); PIN(m0); }
    if constexpr (r == 4) { m1 = sum4(EX ? k[(p + 1) & 15].x + k[(p + 1) & 15].y : k[(p + 1) & 15].x, a[(2 * p + 2) & 31]); PIN(m1); }
    if constexpr (r == 5) { m1 = sum4(m1, a[(2 * p + 3) & 31]); PIN(m1); }
    if constexpr (r == 6) { m0 = (m0 ^ tag) | (m1 ^ tag); PIN(m0); }
    if constexpr (r == 7) settle<2, STR, EX>(c, m0, a, k, lv, sb, p, tag);
  }
}
template <int STR, bool EX> __device__ __forceinline__ void open_phase(Ctx& c, v4u (&a)[32], v2u (&k)[16], int lv, int sb, unsigned tag) {
  VMWAIT(0);
  __builtin_amdgcn_s_sleep(XD);
#pragma unroll
  for (int i = 0; i < 12; ++i) ld_rec<STR, EX>(c, a, k, lv, sb, i / 3, i % 3);
  SB();
  settle<2, STR, EX>(c, rec_bad<EX>(a, k, 0, tag) | rec_bad<EX>(a, k, 1, tag), a, k, lv, sb, 0, tag);
}
template <int F, int P0, int P1, int STR, bool EX> __device__ __forceinline__ void req_step(Ctx& c, v4u (&a)[32], v2u (&k)[16], int lv, int sb) {
  if constexpr (F >= 0 && P0 + F / 2 < P1) {
    ld_rec<STR, EX>(c, a, k, lv, sb, P0 + F / 2, F & 1);
    if constexpr (F & 1) ld_rec<STR, EX>(c, a, k, lv, sb, P0 + F / 2, 2);
  }
}
template <int G, int PER, int P0, int P1, int STR, bool EX> __device__ __forceinline__ void req_pace(Ctx& c, v4u (&a)[32], v2u (&k)[16], int lv, int sb) {
  if constexpr (G >= 0 && P0 + G / PER < P1) {
    constexpr int r = G % PER - (PER == 4 ? (G / 4) & 1 : 0), D = PER / 3;
    if constexpr (r >= 0 && r % D == 0 && r / D < 3) ld_rec<STR, EX>(c, a, k, lv, sb, P0 + G / PER, r / D);
  }
}
template <bool EX> __device__ __forceinline__ void st_chk(Ctx& c, unsigned sum, unsigned ex, int sb, unsigned tag) {
  if (EX) __builtin_amdgcn_raw_buffer_store_b64(v2u{tag - sum - ex, ex}, c.rs, c.lane * 8 + 2 * KB, sb, 16);
  else __builtin_amdgcn_raw_buffer_store_b32(tag - sum, c.rs, c.lane * 4 + 2 * KB, sb, 16);
}
__device__ __forceinline__ u64 mix64(u64 z) {
  z += 0x9E3779B97F4A7C15ull;
  z = (z ^ (z >> 30)) * 0xBF58476D1CE4E5B9ull;
  z = (z ^ (z >> 27)) * 0x94D049BB133111EBull;
  return z ^ (z >> 31);
}
__device__ __forceinline__ float csum(v4u f, float s) { return dot2(f.w, ONES2, dot2(f.z, ONES2, dot2(f.y, ONES2, dot2(f.x, ONES2, s)))); }
__device__ __forceinline__ v4u cs_operand(float s) { unsigned hi, lo; split2(s, hi, lo); return v4u{hi | (hi << 16), lo, 0u, 0u}; }

__global__ __launch_bounds__(512, 2) void fused_fwd(const u16* __restrict__ x, const u16* __restrict__ wqkv, const u16* __restrict__ wo,
                                                    const u16* __restrict__ wg, const u16* __restrict__ wu, const u16* __restrict__ wd,
                                                    u16* __restrict__ out, char* __restrict__ ws) {
  __shared__ __attribute__((aligned(16))) char smem[L_END];
  u64 pc0;
  asm volatile("s_getpc_b64 %0" : "=s"(pc0));
  Ctx c;
  c.L = (LAS char*)smem;
  c.rs = __builtin_amdgcn_make_buffer_rsrc((void*)ws, 0, WS_END, 0x00020000);
  const int wave = __builtin_amdgcn_readfirstlane(threadIdx.x >> 6);
  const int b = blockIdx.x, xc = b & 7, j = b >> 3;
  const int cl = (xc >> 1) * 4 + (j >> 3), m = (xc & 1) * 8 + (j & 7);
  const int rh = m >> 3, cg = m & 7;
  const int g0 = gsplit(m), ng = gsplit(m + 1) - g0;
  c.lane = 0; c.wave = wave; c.seq = 0; c.known = 0; c.nst = 0; c.dbgt = 1; c.vb0 = 0; c.vb1 = 0; c.nb0 = 0; c.nb1 = 0; c.fa = 0; c.fpre = v2u{0u, 0u};
  const int rot = K_ROT ? (2 * cl + m) & 7 : 0;
  LAS char* L = c.L;
  if (threadIdx.x < 32) *flg(c, threadIdx.x) = 0u;
  stamp0(c);
  asm volatile("s_waitcnt lgkmcnt(0)\n\ts_barrier" ::: "memory");
#define LANEVARS                                                                                                              \
  const int lane = fresh_lane(), h = lane >> 5, l31 = lane & 31, lv = lane * 16;                                               \
  const int trN = (8 * h + ((lane >> 2) & 3)) * 64 + ((lane >> 4) & 1) * 32 + (lane & 3) * 8;                                   \
  const int trS = (8 * h + ((lane >> 2) & 3)) * 64 + ((lane >> 4) & 1) * 32 + (((lane & 1) << 1) | ((lane >> 1) & 1)) * 8;       \
  (void)trN; (void)trS; (void)l31; (void)lv;
  const int pp = wave & 1;
  auto load_half0 = [&](int sl) INL {
    const int lane = fresh_lane();
    const unsigned vo1 = (lane >> 2) * 3072 + (lane & 3) * 16;
    const char* wb = (const char*)wqkv + m * 64 + (sl < 4 ? (1 + pp) * 1024 : 0);
#pragma unroll
    for (int p = 0; p < 8; ++p) {
      const int s = sl < 4 ? sl * 8 + p : (sl - 4) * 16 + 2 * p + pp;
      glds(wb + (size_t)(((s + 4 * rot) & 31) * 16) * 3072, vo1, L_RING + sl * SLOT + p * 2048 + pp * 1024);
    }
  };
  auto cache_load = [&](auto& wc, auto NK) INL {
    const int lane = fresh_lane();
    const __amdgpu_buffer_rsrc_t rd = __builtin_amdgcn_make_buffer_rsrc((void*)wd, 0, 1376 * 1024, 0x00020000);
    const int vo2 = (lane >> 3) * 1024 + (((lane & 7) ^ (((lane >> 4) & 1) << 2)) * 16);
#pragma unroll
    for (int k = 0; k < NK.v; ++k) {
#pragma unroll
      for (int q = 0; q < 2; ++q) {
        const int hs = (NK.v == 5 ? 2 * k : k < 5 ? 2 * k + 1 : 10) * 2 + pp;
        const int p = min(hs * 2 + q, 42);
        const int m3 = min(p - 32 + 1 + ((p - 32) >> 1), 15);
        const int aa = p < 32 ? gsplit(p & 15) + (p >> 4) : gsplit(m3) + 2;
#pragma unroll
        for (int e = 0; e < 4; ++e) wc[k * 8 + q * 4 + e] = __builtin_amdgcn_raw_buffer_load_b128(rd, vo2, aa * 32768 + cg * 128 + e * 8192, 0);
        SB();
        if (NK.v == 5) __builtin_amdgcn_s_sleep(2);
      }
    }
  };
  auto cache_conv = [&](auto& wc, auto NK) INL {
    const int lane = fresh_lane(), h = lane >> 5;
    const int tk = (8 * h + ((lane >> 2) & 3)) * 128 + ((lane >> 4) & 1) * 32 + (((lane & 1) << 1) | ((lane >> 1) & 1)) * 8 + ((lane >> 3) & 1) * 64;
    LAS char* d = L + L_XCH + (wave - 4) * 4096;
    wait_lds<4, 2>(c, F_FREE, 4u);
#pragma unroll
    for (int q = 0; q < 2 * NK.v; ++q) {
#pragma unroll
      for (int e = 0; e < 4; ++e) *(LAS v4u*)(d + lane * 16 + e * KB) = wc[q * 4 + e];
      MEMBAR(); SB();
#pragma unroll
      for (int e = 0; e < 4; ++e) wc[q * 4 + e] = tr_frag<512>(d + (e >> 1) * 2048 + (tk ^ (((e ^ q) & 1) * 64)));
      MEMBAR(); SB();
    }
    *flg(c, F_CONV + wave - 4) = 1u;
    MEMBAR();
  };
  auto cache_put = [&](auto& wc, auto NK, int base) INL {
    const int lane = fresh_lane();
#pragma unroll
    for (int k = 0; k < NK.v; ++k) {
      const int sq = base + (NK.v == 5 ? 2 * k : k < 5 ? 2 * k + 1 : 10);
      wait_lds<4, 4>(c, F_FREE, (unsigned)(sq - NSLOT + 1));
      LAS char* d = L + L_RING + (sq & (NSLOT - 1)) * SLOT + pp * 8192;
#pragma unroll
      for (int e = 0; e < 8; ++e) {
        *(LAS v4u*)(d + lane * 16 + e * KB) = wc[k * 8 + e];
        asm volatile("s_nop 3" ::: "memory");
      }
      wait_lds<1, 2>(c, F_FULL + pp, (unsigned)sq);
      *flg(c, F_FULL + pp) = (unsigned)(sq + 1);
      MEMBAR();
    }
  };

  if (wave < 4) {
    __builtin_amdgcn_s_setprio(2);
    const int w = wave, mh = w >> 1, kh = w & 1;
    c.fa = opqv(L_FLG + (F_FREE + wave) * 4);
    asm volatile("" : "+s"(c.rs));
    v4u a[32], xr0, xr1;
    v2u k[16];
    f32x16 A0, A1, B0, B1;
    LANEVARS
    c.lane = lane;
    float rstd;
    unsigned tagx, tagy, tagc;
    v4u mfrag;
    auto set_stats = [&](float sum, float ssq) INL -> float {
      const float s = sum * (1.f / 512), q = ssq * (1.f / 512);
      unsigned hi, lo;
      split2(-s, hi, lo);
      mfrag = h == 0 ? v4u{hi | (lo << 16), hi, 0u, 0u} : v4u{0u, 0u, 0u, 0u};
      return fmaxf(q - s * s, 0.f) + 1e-5f;
    };
    auto cs_frag = [&](int tile) INL { return *(LAS v4u*)(L + L_CS + tile * 512 + l31 * 16); };
    auto rs_put = [&]() INL { const int lane = fresh_lane(); *(LAS float*)(L + L_KVS + w * 2304 + lane * 4) = h ? rstd * LOG2E : rstd; MEMBAR(); };
    auto rs_get = [&](int q) INL {
      const int h = fresh_lane() >> 5;
      const f32x4 r = *(LAS f32x4*)(L + L_KVS + w * 2304 + (8 * q + 4 * h) * 4), rl = *(LAS f32x4*)(L + L_KVS + w * 2304 + 128 + (8 * q + 4 * h) * 4);
#pragma unroll
      for (int i = 0; i < 4; ++i) { B0[4 * q + i] = r[i]; B1[4 * q + i] = rl[i]; }
    };
    {
      const __amdgpu_buffer_rsrc_t rx = __builtin_amdgcn_make_buffer_rsrc((void*)x, 0, 2048 * 1024, 0x00020000);
      const int r8 = lane >> 3;
      const unsigned xv0 = r8 * 1024 + (((lane & 7) ^ (r8 >> 1)) * 16), xv1 = xv0 ^ 64;
      const char* xb = (const char*)x + (size_t)(cl * 128 + w * 32) * 1024;
      const int ro = l31 * 128 + ((h ^ ((l31 >> 1) & 7)) * 16);
      auto xbuf = [&](int kp) INL { return (kp % 3 == 1 ? L_XCH : L_RING + (4 + (kp % 3) / 2) * SLOT) + w * 4096; };
      auto xreq = [&](int kp) INL {
#pragma unroll
        for (int q = 0; q < 4; ++q) glds(xb + q * 8192 + ((kp + rot) & 7) * 128, (q & 1) ? xv1 : xv0, xbuf(kp) + q * 1024);
      };
      auto xget = [&](int kp) INL {
        if (kp < 6) VMWAIT(8); else if (kp == 6) VMWAIT(4); else VMWAIT(0);
#pragma unroll
        for (int q = 0; q < 4; ++q) a[kp * 4 + q] = *(LAS v4u*)(L + xbuf(kp) + (ro ^ (q * 32)));
        if (kp + 3 < 8) {
          asm volatile("s_waitcnt lgkmcnt(0)" : "+v"(a[kp * 4]), "+v"(a[kp * 4 + 1]), "+v"(a[kp * 4 + 2]), "+v"(a[kp * 4 + 3])::"memory");
          xreq(kp + 3);
        }
        MEMBAR();
      };
      __builtin_amdgcn_s_setprio(3);
      xreq(0); xreq(1); xreq(2);
      __builtin_amdgcn_s_setprio(2);
      stamp<3>(c);
#pragma unroll
      for (int kp = 0; kp < XPRE; ++kp) xget(kp);
      stamp<3>(c);
      wpre<2>(c, trN, trS);
      stamp<3>(c);
      v4u s0, ca, cb;
      const int s0b = WS_S0P + (cl * 128 + w * 32) * 16;
      gemm<64, 2, 2, -1, true, 3, 0, 0, 99>(c, a, A0, A1, trN, trS, 0, 0, [&](auto F) INL {
        constexpr int f = F.v;
        if ((f & 3) == 1 && (f >> 2) + XPRE < 8) xget((f >> 2) + XPRE);
        if (f == 4 * (8 - XPRE)) s0 = ld_ws(c, l31 * 16, s0b);
        if (f == 4 * (8 - XPRE) + 2) {
          const int xq = l31 * 1024 + h * 16, xqs = (cl * 128 + mh * 64 + rh * 32) * 1024 + cg * 128 + kh * 64;
          xr0 = __builtin_amdgcn_raw_buffer_load_b128(rx, xq, xqs, 0);
          xr1 = __builtin_amdgcn_raw_buffer_load_b128(rx, xq + 32, xqs, 0);
        }
        if (f == 40) {
          wait_lds<2, 0>(c, F_CS1R, 1u);
          const v2u tg = *(volatile LAS v2u*)flg(c, F_TAG);
          tagx = __builtin_amdgcn_readfirstlane(tg.x); tagy = __builtin_amdgcn_readfirstlane(tg.y); tagc = (tagx * 0x9E3779B1u + tagy) | 1u;
          if (__any(s0.y != tagx || s0.w != tagy)) {
            unsigned sp = 0;
#pragma nounroll
            do { MEMBAR(); s0 = ld_ws(c, l31 * 16, s0b); } while (__any(s0.y != tagx || s0.w != tagy) && ++sp < SPIN_CAP);
          }
          rstd = __builtin_amdgcn_rsqf(set_stats(u2f(s0.x), u2f(s0.z)));
          rs_put();
          ca = cs_frag(0); cb = cs_frag(1);
        }
        if (f == 45) { A0 = mfma(mfrag, ca, A0); A1 = mfma(mfrag, cb, A1); }
        if (f >= 54 && f < 62 && !(f & 1)) rs_get((f - 54) >> 1);
        if ((f & 7) == 7) stamp<3>(c);
      });
    }
#pragma unroll 1
    for (int layer = 0; layer < 2; ++layer) {
      stamp<1>(c);
      bool publisher;
      {
        LANEVARS
#pragma unroll
        for (int i = 0; i < 16; ++i) B1[i] *= A0[i];
        SB();
#pragma unroll
        for (int i = 0; i < 16; ++i) { B1[i] = exp2c(B1[i]); A0[i] = fmaf(A0[i], B0[i], 1.f); A1[i] *= B0[i]; }
        SB();
        v4u pk0, pk1, vv0, vv1;
#pragma unroll
        for (int i = 0; i < 16; ++i) A0[i] = fmaxf(A0[i], B1[i]) + 1e-6f;
#pragma unroll
        for (int i = 0; i < 4; ++i) {
          pk0[i] = pack2(A0[2 * i], A0[2 * i + 1]); vv0[i] = pack2(A1[2 * i], A1[2 * i + 1]);
          pk1[i] = pack2(A0[8 + 2 * i], A0[9 + 2 * i]); vv1[i] = pack2(A1[8 + 2 * i], A1[9 + 2 * i]);
        }
        f32x16 kv = mfma(pk0, vv0, zero16());
        kv = mfma(pk1, vv1, kv);
        const float z = red_h(csum(pk0, 0.f) + csum(pk1, 0.f));
        LAS char* ks = L + L_KVS + w * 2304;
        if (h == 0) *(LAS float*)(ks + 2048 + l31 * 4) = z;
        if (lane & 16) {
          *(LAS f32x4*)(ks + lv) = f32x4{kv[8], kv[9], kv[10], kv[11]};
          *(LAS f32x4*)(ks + 1024 + lv) = f32x4{kv[12], kv[13], kv[14], kv[15]};
          MEMBAR();
        } else {
          MEMBAR();
          *(LAS f32x4*)(ks + lv) = f32x4{kv[0], kv[1], kv[2], kv[3]};
          *(LAS f32x4*)(ks + 1024 + lv) = f32x4{kv[4], kv[5], kv[6], kv[7]};
        }
        MEMBAR();
        *flg(c, F_KFL + w) = (unsigned)(layer + 1);
        const v4u kf = *(volatile LAS v4u*)flg(c, F_KFL);
        MEMBAR();
        f32x4 p0[4], p1[4];
        f32x2 pz[4];
#pragma unroll
        for (int ww = 0; ww < 4; ++ww) {
          p0[ww] = *(LAS f32x4*)(L + L_KVS + ww * 2304 + lv);
          p1[ww] = *(LAS f32x4*)(L + L_KVS + ww * 2304 + 1024 + lv);
          pz[ww] = *(LAS f32x2*)(L + L_KVS + ww * 2304 + 2048 + (lane & 15) * 8);
        }
        MEMBAR();
        SB();
        publisher = (unsigned)__builtin_amdgcn_readfirstlane(umin(umin(kf.x, kf.y), umin(kf.z, kf.w))) > (unsigned)layer;
        if (publisher) {
          const f32x4 k0 = (p0[0] + p0[1]) + (p0[2] + p0[3]), k1 = (p1[0] + p1[1]) + (p1[2] + p1[3]);
          const f32x2 zz = (pz[0] + pz[1]) + (pz[2] + pz[3]);
          const unsigned tg = layer ? tagy : tagx;
          const unsigned tl = tg >> ((lane & 1) * 16), t0 = tl & 0xffu, t1 = (tl >> 8) & 0xffu;
          const int kvb = WS_KVP + ((layer * 16 + m) * 16 + cl) * KVR;
          auto st2 = [&](float u, float v, int vo) INL {
            __builtin_amdgcn_raw_buffer_store_b64(v2u{(f2u(u) & 0xffffff00u) | t0, (f2u(v) & 0xffffff00u) | t1}, c.rs, vo, kvb, 16);
          };
          st2(k0[0], k0[1], lane * 8); st2(k0[2], k0[3], lane * 8 + 512); st2(k1[0], k1[1], lane * 8 + 1024); st2(k1[2], k1[3], lane * 8 + 1536);
          if (lane < 16) st2(zz[0], zz[1], lane * 8 + 2048);
        }
        MEMBAR();
      }
      stamp<1>(c);
      B0 = mfma(cs_frag(2), mfrag, zero16());
      if (layer == 0) { LANEVARS wpre<3>(c, trN, trN); gemm<32, 1, 3, -1, false, 0, 0, 0, 99>(c, a, B0, B0, trN, trN, 0, 0, NoSide()); }
      else { wpre<0>(c, lv, lv); gemm<32, 1, 0, -1, false, 0, 0, 0, 99>(c, a, B0, B0, lv, lv, 0, 0, NoSide()); }
      const int lq = fresh_lane();
      const int kb0 = (8 * (lq >> 5) + ((lq >> 2) & 3)) * 128 + ((lq >> 4) & 1) * 32 + (((lq & 1) << 1) | ((lq >> 1) & 1)) * 8 + kh * 4096 + ((kh ^ (lq >> 3)) & 1) * 64;
      const int kb1 = kb0 ^ 64;
      auto res_init = [&](f32x16& R, int i) INL { const unsigned u = i < 4 ? xr0[i & 3] : xr1[i & 3]; R[2 * i] = blo(u); R[2 * i + 1] = bhi(u); };
      {
        LANEVARS
        v4u pq0, pq1;
        const float rl = rstd * LOG2E;
#pragma unroll
        for (int i = 0; i < 16; ++i) B1[i] = exp2c(B0[i] * rl);
#pragma unroll
        for (int i = 0; i < 16; ++i) B0[i] = fmaxf(fmaf(B0[i], rstd, 1.f), B1[i]) + 1e-6f;
#pragma unroll
        for (int i = 0; i < 4; ++i) { pq0[i] = pack2(B0[2 * i], B0[2 * i + 1]); pq1[i] = pack2(B0[8 + 2 * i], B0[9 + 2 * i]); }
#pragma unroll
        for (int i = 0; i < 8; ++i) res_init(A0, i);
        PIN(pq0); PIN(pq1); PIN(A0);
        const int ob = opq(WS_O + cl * ACT_CL + (w * 16 + m) * REC);
        stamp<1>(c);
        {
          const int lane = fresh_lane();
          const unsigned tg = layer ? tagy : tagx;
          const unsigned tl = tg >> ((lane & 1) * 16), t0 = tl & 0xffu, t1 = (tl >> 8) & 0xffu;
          const int kvb = WS_KVP + (layer * 16 + m) * 16 * KVR;
          float s0 = 0.f, s1 = 0.f;
          v2u zp;
          unsigned zpk;
          if (publisher) __builtin_amdgcn_s_sleep(KVDP); else __builtin_amdgcn_s_sleep(KVDELAY);
#pragma nounroll
          for (unsigned sp = 0; sp < SPIN_CAP; ++sp) {
            v2u p[16];
            zp = __builtin_amdgcn_raw_buffer_load_b64(c.rs, (lane >> 2) * KVR + (lane & 3) * 8 + 2048, kvb + w * 32, 16);
#pragma unroll
            for (int i = 0; i < 16; ++i) p[i] = __builtin_amdgcn_raw_buffer_load_b64(c.rs, lane * 8, kvb + w * 512 + i * KVR, 16);
            SB();
            zp.x ^= t0; zp.y ^= t1;
            unsigned chk = zp.x | zp.y;
            float za = u2f(zp.x), zb = u2f(zp.y);
            za += dpp<0x128>(za); zb += dpp<0x128>(zb); za += dpp<0x124>(za); zb += dpp<0x124>(zb);
            zpk = pack2(red_g(za), red_g(zb));
            PIN(zpk);
#pragma unroll
            for (int i = 0; i < 16; ++i) {
              p[i].x ^= t0; p[i].y ^= t1; chk |= p[i].x | p[i].y;
              s0 = i ? s0 + u2f(p[i].x) : u2f(p[i].x); s1 = i ? s1 + u2f(p[i].y) : u2f(p[i].y);
              PIN(s0); PIN(s1);
            }
            if (!__any((chk & 0xffu) != 0u)) break;
            __builtin_amdgcn_s_sleep(BACKOFF);
            MEMBAR();
          }
          *(LAS unsigned*)(L + L_KVF + lv + w * 4) = pack2(s0, s1);
          if (lane < 4) *(LAS unsigned*)(L + L_ZF + w * 16 + lane * 4) = zpk;
          MEMBAR();
          *flg(c, F_KVR + w) = (unsigned)(layer + 1);
        }
        v4u kvf;
        v2u z0, z1, z2, z3;
#pragma nounroll
        for (unsigned sp = 0; sp < SPIN_CAP; ++sp) {
          const v4u fl = *(volatile LAS v4u*)flg(c, F_KVR);
          MEMBAR();
          kvf = *(LAS v4u*)(L + L_KVF + lv);
          z0 = *(LAS v2u*)(L + L_ZF + h * 8); z1 = *(LAS v2u*)(L + L_ZF + 16 + h * 8);
          z2 = *(LAS v2u*)(L + L_ZF + 32 + h * 8); z3 = *(LAS v2u*)(L + L_ZF + 48 + h * 8);
          MEMBAR();
          if ((unsigned)__builtin_amdgcn_readfirstlane(umin(umin(fl.x, fl.y), umin(fl.z, fl.w))) > (unsigned)layer) break;
        }
        stamp<1>(c);
        const bool up = (lane & 16) != 0;
        const v4u zz = {0u, 0u, 0u, 0u};
        B0 = mfma(up ? zz : kvf, pq0, zero16());
        B0 = mfma(up ? kvf : zz, pq1, B0);
        const float d0 = dot2(pq0.y, z0.y, dot2(pq0.x, z0.x, 0.f)) + dot2(pq0.w, z1.y, dot2(pq0.z, z1.x, 0.f));
        const float d1 = dot2(pq1.y, z2.y, dot2(pq1.x, z2.x, 0.f)) + dot2(pq1.w, z3.y, dot2(pq1.z, z3.x, 0.f));
        const float r0 = __builtin_amdgcn_rcpf(red_h(d0) + 1e-6f), r1 = __builtin_amdgcn_rcpf(red_h(d1) + 1e-6f);
        v4u o0, o1;
#pragma unroll
        for (int i = 0; i < 4; ++i) { o0[i] = pack2(B0[2 * i] * r0, B0[2 * i + 1] * r0); o1[i] = pack2(B0[8 + 2 * i] * r1, B0[9 + 2 * i] * r1); }
        st_ws(c, o0, lv, ob);
        st_ws(c, o1, lv + KB, ob);
        st_chk<false>(c, sum4(sum4(0u, o0), o1), 0u, ob, tagc);
      }
      stamp<1>(c);
      const int rtA = opq(mh * 2 + rh), clb = opq(cl * ACT_CL);
      auto res_send = [&](f32x16& A1, int q, unsigned xseq) INL {
        LAS char* xs = L + L_XCH + w * 4096 + lv;
        *(LAS f32x4*)(xs + q * 2048) = f32x4{A1[8 * q], A1[8 * q + 1], A1[8 * q + 2], A1[8 * q + 3]};
        *(LAS f32x4*)(xs + q * 2048 + 1024) = f32x4{A1[8 * q + 4], A1[8 * q + 5], A1[8 * q + 6], A1[8 * q + 7]};
        MEMBAR();
        if (q) *flg(c, F_XF + w) = xseq;
      };
      auto res_epilogue = [&](f32x16& A0, int xi, bool final_out, unsigned xseq) INL {
        LAS char* xs = L + L_XCH;
        const int lv = fresh_lane() * 16;
        f32x4 o0, o1, o2, o3;
#pragma nounroll
        for (unsigned sp = 0; sp < SPIN_CAP; ++sp) {
          const unsigned fl = *flg(c, F_XF + (w ^ 1));
          MEMBAR();
          o0 = *(LAS f32x4*)(xs + (w ^ 1) * 4096 + lv); o1 = *(LAS f32x4*)(xs + (w ^ 1) * 4096 + 1024 + lv);
          o2 = *(LAS f32x4*)(xs + (w ^ 1) * 4096 + 2048 + lv); o3 = *(LAS f32x4*)(xs + (w ^ 1) * 4096 + 3072 + lv);
          MEMBAR();
          if ((unsigned)__builtin_amdgcn_readfirstlane(fl) >= xseq) break;
        }
        xr0 = v4u{pack2(A0[0] + o0[0], A0[1] + o0[1]), pack2(A0[2] + o0[2], A0[3] + o0[3]), pack2(A0[4] + o1[0], A0[5] + o1[1]), pack2(A0[6] + o1[2], A0[7] + o1[3])};
        xr1 = v4u{pack2(A0[8] + o2[0], A0[9] + o2[1]), pack2(A0[10] + o2[2], A0[11] + o2[3]), pack2(A0[12] + o3[0], A0[13] + o3[1]), pack2(A0[14] + o3[2], A0[15] + o3[3])};
        if (final_out) {
          const int lo = fresh_lane();
          GAS char* ob = (GAS char*)out + (size_t)((cl * 4 + rtA) * 32 + (lo & 31)) * 1024 + cg * 128 + kh * 64 + (lo >> 5) * 16;
          *(GAS v4u*)ob = xr0;
          *(GAS v4u*)(ob + 32) = xr1;
        } else {
          const int xb = WS_X + clb + ((xi * 4 + rtA) * 16 + cg * 2 + kh) * REC;
          st_ws(c, xr0, lv, xb);
          st_ws(c, xr1, lv + KB, xb);
          const float s = csum(xr0, 0.f) + csum(xr1, 0.f);
          float q0 = 0.f, q1 = 0.f;
#pragma unroll
          for (int i = 0; i < 4; ++i) { q0 = dot2(xr0[i], xr0[i], q0); q1 = dot2(xr1[i], xr1[i], q1); }
          const auto sw = __builtin_amdgcn_permlane32_swap(f2u(s), f2u(q0 + q1), false, false);
          st_chk<true>(c, sum4(sum4(0u, xr0), xr1), f2u(u2f((unsigned)sw[0]) + u2f((unsigned)sw[1])), xb, tagc);
        }
      };
      unsigned m0, m1;
      {
        const int ab = WS_O + clb + (rtA * 16 + kh) * REC;
        wpre<1>(c, kb0, kb1);
        if (mh) __builtin_amdgcn_s_sleep(YOFF);
        open_phase<2, false>(c, a, k, lv, ab, tagc);
        stamp<1>(c);
        gemm<32, 2, 1, -1, false, 2, 8, 0, 99, 1>(c, a, A0, A1, kb0, kb1, 0, 0, [&](auto F) INL {
          if (F.v == 24 && layer == 0) wait_lds<2, 0>(c, F_CONV + 2, 1u);
          if constexpr (F.v == 29 || F.v == 30) res_send(A1, F.v - 29, (unsigned)(layer * 0x200 + 1));
          req_step<F.v, 4, 8, 2, false>(c, a, k, lv, ab);
          check_step<F.v, 8, 2, false>(c, m0, m1, a, k, lv, ab, tagc);
        });
        stamp<1>(c);
        res_epilogue(A0, layer, false, (unsigned)(layer * 0x200 + 1));
      }
      *flg(c, F_XF + w) = (unsigned)(layer * 0x200 + 0x100);
      *(LAS v4u*)(L + L_KVS + w * 2304 + lv) = xr0;
      *(LAS v4u*)(L + L_KVS + w * 2304 + 1024 + lv) = xr1;
      MEMBAR();
      auto res_back = [&]() INL { xr0 = *(LAS v4u*)(L + L_KVS + w * 2304 + lv); xr1 = *(LAS v4u*)(L + L_KVS + w * 2304 + 1024 + lv); };
      wpre<0>(c, lv, lv);
      stamp<1>(c);
      v4u cs0;
      float sacc;
      const int ab4 = WS_H + clb + (rtA * 43 + kh) * REC;
      const int hb = WS_H + clb + w * 43 * REC;
      float r2;
      v4u ho;
      unsigned hs;
      float h0, h1;
      auto hside = [&](auto F, f32x16& G, f32x16& U, int dst) INL {
        constexpr int f = F.v, sl = (f - 1) / 2;
        if constexpr (f >= 1 && f < 17) {
          if constexpr (f & 1) { h0 = relu(G[2 * sl]) * r2; PIN(h0); h1 = relu(G[2 * sl + 1]) * r2; PIN(h1); }
          else {
            h0 *= U[2 * sl]; PIN(h0); h1 *= U[2 * sl + 1]; PIN(h1);
            ho[sl & 3] = pack2(h0, h1); PIN(ho);
          }
        }
        if constexpr (f == 9 || f == 17) { st_ws(c, ho, lv + (f == 17) * KB, dst); hs = sum4(f == 9 ? 0u : hs, ho); PIN(hs); }
        if constexpr (f == 18) st_chk<false>(c, hs, 0u, dst, tagc);
      };
      auto corr = [&](auto F, int at, f32x16& G, f32x16& U, int gi) INL {
        if (F.v == at - 6) cs0 = cs_frag(3 + 2 * gi);
        if (F.v == at - 3) { G = mfma(cs0, mfrag, G); SB(); cs0 = cs_frag(4 + 2 * gi); }
        if (F.v == at) U = mfma(cs0, mfrag, U);
      };
      const int xb3 = WS_X + clb + (layer * 4 + w) * 16 * REC;
      open_phase<1, true>(c, a, k, lv, xb3, tagc);
      stamp<1>(c); stamp<2>(c);
      gemm4<128, 2, 0, 0, false, 3, 62, 0, 99, -1>(c, a, A0, A1, B0, B1, lv, lv, lv, lv, [&](auto F) INL {
        constexpr int f = F.v, u = f / 8;
        if constexpr (f == 0) { sacc = u2f(k[0].y) + u2f(k[1].y); PIN(sacc); }
        req_pace<f, 6, 4, 16, 1, true>(c, a, k, lv, xb3);
        sfor<8>([&](auto CH) INL {
          if constexpr (CH.v > 0 && dual_first(CH.v) == u + 1) check_step<8 * (CH.v - 1) + f % 8, 16, 1, true>(c, m0, m1, a, k, lv, xb3, tagc);
          if constexpr (CH.v > 0 && f == 8 * dual_first(CH.v)) { sacc += u2f(k[2 * CH.v].y) + u2f(k[2 * CH.v + 1].y); PIN(sacc); }
        });
        if constexpr (f == 89) {
          const auto sw = __builtin_amdgcn_permlane32_swap(f2u(sacc), f2u(sacc), false, false);
          r2 = __builtin_amdgcn_rcpf(set_stats(u2f((unsigned)sw[0]), u2f((unsigned)sw[1])));
          PIN(mfrag); PIN(r2);
        }
        if (f == 86 && layer == 0) wait_lds<1, 0>(c, F_CS3R, 1u);
        corr(F, 95, A0, A1, 0);
        if constexpr (f >= 96) hside(IC<f - 96>{}, A0, A1, hb + m * REC);
        corr(F, 123, B0, B1, 1);
      });
      stamp<2>(c);
      const int tb0 = fresh_lane() * 16 + kh * 4096;
      if (ng == 3) {
        gemm<64, 2, 0, 4, false, 3, 0, 0, 99>(c, a, A0, A1, lv, lv, tb0, tb0, [&](auto F) INL {
          constexpr int f = F.v;
          hside(F, B0, B1, hb + (16 + m) * REC);
          corr(F, 47, A0, A1, 2);
          if constexpr (f == 19) res_back();
          if constexpr (f >= 23 && f < 31) { res_init(B0, f - 23); PIN(B0); }
          req_pace<f - 32, 4, 0, 8, 2, false>(c, a, k, lv, ab4);
          if constexpr (f >= 56) check_step<f - 64, 22, 2, false>(c, m0, m1, a, k, lv, ab4, tagc);
        });
      }
      if (opq(ng) == 2) {
        const unsigned tg0 = (unsigned)__builtin_amdgcn_s_memrealtime();
        sfor<20>([&](auto F) INL { hside(F, B0, B1, hb + (16 + m) * REC); });
        res_back();
#pragma unroll
        for (int i = 0; i < 8; ++i) res_init(B0, i);
        while ((unsigned)__builtin_amdgcn_s_memrealtime() - tg0 < (unsigned)NG2S) __builtin_amdgcn_s_sleep(2);
        sfor<16>([&](auto F) INL { req_step<F.v, 0, 8, 2, false>(c, a, k, lv, ab4); SB(); });
        wpre<4>(c, tb0, tb0);
        settle<2, 2, false>(c, rec_bad<false>(a, k, 0, tagc) | rec_bad<false>(a, k, 1, tagc), a, k, lv, ab4, 0, tagc);
        while ((unsigned)__builtin_amdgcn_s_memrealtime() - tg0 < (unsigned)NG2T) __builtin_amdgcn_s_sleep(2);
      }
      stamp<1>(c); stamp<2>(c); stamp<4>(c); stamp<6>(c);
      {
        const int h2 = ng == 3 ? hb + (32 + g0 - 2 * m) * REC : WS_END;
        const int ab21 = kh ? WS_END : ab4;
        const unsigned t21 = kh ? 0u : tagc;
        auto side4 = [&](auto F) INL {
          constexpr int f = F.v;
          if constexpr (f < 10) { hside(IC<2 * f>{}, A0, A1, h2); hside(IC<2 * f + 1>{}, A0, A1, h2); }
          req_pace<f, 4, 8, 16, 2, false>(c, a, k, lv, ab4);
          req_pace<f - S2H, 3, 16, 21, 2, false>(c, a, k, lv, ab4);
          req_pace<f - S2H - 15, 3, 21, 22, 2, false>(c, a, k, lv, ab21);
          if constexpr (f < 72) check_step<f, 22, 2, false>(c, m0, m1, a, k, lv, ab4, tagc);
          if constexpr (f == 74) settle<1, 2, false>(c, rec_bad<false>(a, k, 20, tagc), a, k, lv, ab4, 20, tagc);
          if constexpr (f == 78) settle<1, 2, false>(c, rec_bad<false>(a, k, 21, t21), a, k, lv, ab21, 21, t21);
          if constexpr (f == 85 || f == 86) res_send(B1, f - 85, (unsigned)(layer * 0x200 + 0x101));
          if ((f & 7) == 7) stamp<4>(c);
          if (f == 15 || f == 39 || f == 55 || f == 65 || f == 83) stamp<6>(c);
        };
        gemm<88, 2, 4, -1, false, 2, 32, 3, 7, 1>(c, a, B0, B1, tb0, tb0, 0, 0, side4);
        stamp<1>(c); stamp<2>(c);
        tagc += 0x632BE5AAu;
        res_epilogue(B0, 0, layer == 1, (unsigned)(layer * 0x200 + 0x101));
      }
      if (layer == 1) break;
      wpre<0>(c, lv, lv);
      stamp<1>(c);
      const int xb1 = WS_X + clb + w * 16 * REC;
      open_phase<1, true>(c, a, k, lv, xb1, tagc);
      stamp<1>(c);
      gemm<64, 2, 0, -1, true, 3, 34, 0, 99>(c, a, A0, A1, lv, lv, 0, 0, [&](auto F) INL {
        constexpr int f = F.v;
        if constexpr (f == 0) { sacc = u2f(k[0].y) + u2f(k[1].y); PIN(sacc); }
        req_step<f, 4, 10, 1, true>(c, a, k, lv, xb1);
        req_pace<f - 12, 4, 10, 16, 1, true>(c, a, k, lv, xb1);
        check_step<f, 16, 1, true>(c, m0, m1, a, k, lv, xb1, tagc);
        if constexpr ((f & 7) == 0 && f >= 8 && f <= 56) { sacc += u2f(k[f / 4].y) + u2f(k[f / 4 + 1].y); PIN(sacc); }
        if constexpr (f == 57) {
          const auto sw = __builtin_amdgcn_permlane32_swap(f2u(sacc), f2u(sacc), false, false);
          rstd = __builtin_amdgcn_rsqf(set_stats(u2f((unsigned)sw[0]), u2f((unsigned)sw[1])));
          PIN(mfrag);
          rs_put();
        }
        if (f == 56) cs0 = cs_frag(0);
        if (f == 60) { A0 = mfma(mfrag, cs0, A0); SB(); cs0 = cs_frag(1); }
        if (f == 63) A1 = mfma(mfrag, cs0, A1);
        if (f >= 59 && f < 63) rs_get(f - 59);
      });
      tagc += 0x632BE5AAu;
    }
  } else if (wave < 6) {
    const int lw = wave - 4;
    LANEVARS
    c.lane = lane;
    volatile LAS unsigned* full = flg(c, F_FULL + lw);
    __builtin_amdgcn_s_setprio(3);
    load_half0(0);
    if (lw == 1) glds((const char*)(pc0 & ~(u64)255) + j * (CODEPF / 32), min(lane * 32, CODEPF / 32 - 32), L_RING + 7 * SLOT + 12288);
    __builtin_amdgcn_s_setprio(2);
    load_half0(1);
    VMWAIT(8); *full = 1u;
    stamp<3>(c);
    load_half0(2);
    load_half0(3);
    VMWAIT(16); *full = 2u;
    stamp<3>(c);
    if (K_TOUCH) {
      const unsigned tv = min(lane, 42) * 128, td = L_RING + (6 + lw) * SLOT + (lw ? 13312 : 3072);
      glds((const char*)(lw ? wu : wg) + b * 5504, tv, td);
      if (lw) glds((const char*)wd + b * 5504, tv, td);
      else glds((const char*)wo + b * 2048, (lane & 15) * 128, td);
    }
    *flg(c, F_QGO + lw) = 1u;
    if (K_TOUCH) VMWAIT(10); else VMWAIT(8);
    *full = 3u;
    stamp<3>(c);
    if (K_TOUCH) VMWAIT(2); else VMWAIT(0);
    *full = 4u;
    stamp<3>(c);
    __builtin_amdgcn_s_setprio(1);
    wait_lds<4, 1>(c, F_FREE, 1u);
    load_half0(4);
    stamp<3>(c);
    load_half0(5);
    VMWAIT(8); *full = 5u;
    VMWAIT(0); *full = 6u;
    stamp<1>(c); stamp<3>(c);
    int seq = 6, pub = 6;
    v4u wc[48];
    auto stream = [&](int seg, int i0, int i1) INL {
      const int ln = fresh_lane(), lv = ln * 16;
      const unsigned vo2 = (ln >> 3) * 1024 + (((ln & 7) ^ (((ln >> 4) & 1) << 2)) * 16);
      const int src = (seg == 0 ? WS_WT1 + m * 96 * KB : WS_WT3 + g0 * 64 * KB);
#pragma unroll 1
      for (int i = i0; i < i1; ++i) {
        if (seq >= NSLOT) {
          const unsigned need = (unsigned)(seq - NSLOT + 1);
          bool first = true;
#pragma nounroll
          for (unsigned sp = 0; sp < SPIN_CAP; ++sp) {
            v4u f = *(volatile LAS v4u*)flg(c, F_FREE);
            if ((unsigned)__builtin_amdgcn_readfirstlane(umin(umin(f.x, f.y), umin(f.z, f.w))) >= need) break;
            if (first && pub < seq) { VMWAIT(0); pub = seq; *full = (unsigned)pub; }
            first = false;
            __builtin_amdgcn_s_sleep(4);
          }
          MEMBAR();
        }
        const unsigned dst = L_RING + (seq & (NSLOT - 1)) * SLOT + lw * 8192;
        const int hs = i * 2 + lw, hr = (seg == 2 && hs < 16) ? (int)((DUAL >> (4 * hs)) & 15) : hs;
        if (seg == 1) {
          const char* sp = (const char*)wo + (size_t)hr * 65536 + cg * 128;
#pragma unroll
          for (int e = 0; e < 8; ++e) glds(sp + e * 8192, vo2, dst + e * 1024);
        } else {
          glds4(ws + src + hr * 8192, lv, dst);
          glds4(ws + src + hr * 8192 + 4096, lv, dst + 4096);
        }
        ++seq;
        if (seq - pub > 2) { VMWAIT(16); pub = seq - 2; *full = (unsigned)pub; }
      }
    };
    auto drain = [&]() INL { VMWAIT(0); if (pub < seq) { pub = seq; *full = (unsigned)seq; } };
    if (K_CE) cache_load(wc, IC<6>{});
    wait_lds<2, 1>(c, F_JOB, 1u);
    if (K_CE) VMWAIT(32);
    stream(1, 0, 4);
    drain();
    stamp<1>(c); stamp<3>(c);
    if (lw == 0) { poll_tags<32>(c, WS_F1 + m * 2048, *(volatile LAS v2u*)flg(c, F_TAG)); *flg(c, F_PTOK) = 1u; }
    else wait_lds<1, 1>(c, F_PTOK, 1u);
    stamp<1>(c); stamp<3>(c);
    stream(2, 0, 4);
    VMWAIT(7);
    if (!K_CE) cache_load(wc, IC<6>{});
    stream(2, 4, 8);
    drain();
    wait_lds<4, 1>(c, F_XF, 0x100u);
    cache_conv(wc, IC<6>{});
    stream(2, 8, 4 * ng);
    drain();
    stamp<1>(c);
    cache_put(wc, IC<6>{}, seq);
    seq += 11; pub = seq;
    stamp<1>(c);
    stream(0, 0, 6);
    stream(1, 0, 4);
    stamp<1>(c);
    stream(2, 0, 4 * ng);
    drain();
    stamp<1>(c);
    cache_put(wc, IC<6>{}, seq);
  } else {
    const int hh = wave - 6;
    LANEVARS
    c.lane = lane;
    const int pts = L_RING + (6 + hh) * SLOT;
    __builtin_amdgcn_s_setprio(3);
    {
      const unsigned vo1 = (lane >> 2) * 3072 + (lane & 3) * 16;
      const char* j0 = (const char*)wqkv + (size_t)(cl * 32) * 3072 + m * 64;
      glds(j0 + (1 + hh) * 1024, vo1, pts); glds(j0 + (1 + hh) * 1024 + 16 * 3072, vo1, pts + 1024);
      glds(j0 + hh * 16 * 3072, vo1, pts + 2048);
    }
    const int srow = cl * 128 + m * 8 + hh * 4 + (lane >> 4);
    v4u xs[4];
#pragma unroll
    for (int i = 0; i < 4; ++i) xs[i] = *(const GAS v4u*)((const GAS char*)x + (size_t)srow * 1024 + i * 256 + (lane & 15) * 16);
    const v2u e0 = __builtin_amdgcn_raw_buffer_load_b64(c.rs, 0, WS_E, 16);
    VMWAIT(0);
    const u64 tag = mix64(((u64)(unsigned)__builtin_amdgcn_readfirstlane(e0.y) << 32) | (unsigned)__builtin_amdgcn_readfirstlane(e0.x));
    const v2u tag2 = {(unsigned)tag, (unsigned)(tag >> 32)};
    if (hh == 0) *(volatile LAS v2u*)flg(c, F_TAG) = tag2;
    stamp<1>(c); stamp<3>(c);
    {
      const unsigned tb = ((unsigned)(mix64(tag) >> 32) >> ((lane & 3) * 8)) & 0xffu;
      const v4u f0 = tr_frag(L + pts + (hh ? trS : trN)), f1 = tr_frag(L + pts + 1024 + (hh ? trS : trN)), f2 = tr_frag(L + pts + 2048 + trN);
      const float s0 = red_h(csum(f1, csum(f0, 0.f))), s2 = red_h(csum(f2, 0.f));
      __builtin_amdgcn_raw_buffer_store_b32((f2u(h ? s2 : s0) & 0xffffff00u) | tb, c.rs, l31 * 4 + h * 4096, WS_C1P + m * 8 * KB + cl * 256 + hh * 128, 16);
      float sa = 0.f, sb = 0.f, qa = 0.f, qb = 0.f;
#pragma unroll
      for (int i = 0; i < 4; ++i) {
        sa = dot2(xs[i].y, ONES2, dot2(xs[i].x, ONES2, sa)); sb = dot2(xs[i].w, ONES2, dot2(xs[i].z, ONES2, sb));
        qa = dot2(xs[i].y, xs[i].y, dot2(xs[i].x, xs[i].x, qa)); qb = dot2(xs[i].w, xs[i].w, dot2(xs[i].z, xs[i].z, qb));
      }
      sa += sb; qa += qb;
      sa += dpp<0x128>(sa); qa += dpp<0x128>(qa); sa += dpp<0x124>(sa); qa += dpp<0x124>(qa);
      sa += dpp<0x122>(sa); qa += dpp<0x122>(qa); sa += dpp<0x121>(sa); qa += dpp<0x121>(qa);
      if ((lane & 15) == 0) st_ws(c, v4u{f2u(sa), tag2.x, f2u(qa), tag2.y}, srow * 16, WS_S0P);
      if (K_KVW) {
        const unsigned nt = ~(hh ? tag2.y : tag2.x);
        const v4u W = {nt, nt >> 8, nt >> 16, nt >> 24};
        const int kvb = WS_KVP + ((hh * 16 + m) * 16 + cl) * KVR;
        st_ws(c, W, lv, kvb); st_ws(c, W, lv + KB, kvb);
        if (lane < 8) st_ws(c, W, lv + 2 * KB, kvb);
      }
      VMWAIT(0);
      __builtin_amdgcn_s_sleep(8);
      stamp<3>(c);
      {
        float t;
#pragma nounroll
        for (unsigned sp = 0; sp < SPIN_CAP; ++sp) {
          unsigned p[16];
#pragma unroll
          for (int i = 0; i < 16; ++i) p[i] = __builtin_amdgcn_raw_buffer_load_b32(c.rs, lane * 4 + i * 256, WS_C1P + m * 8 * KB + hh * 4096, 16);
          SB();
          unsigned chk = 0;
          t = 0.f;
#pragma unroll
          for (int i = 0; i < 16; ++i) { p[i] ^= tb; chk |= p[i]; t += u2f(p[i]); }
          if (!__any((chk & 0xffu) != 0u)) break;
          MEMBAR();
        }
        if (hh) t = red_h(t);
        if (hh == 0 || h == 0) *(LAS v4u*)(L + L_CS + hh * 1024 + lv) = cs_operand(t);
        MEMBAR();
        *flg(c, F_CS1R + hh) = 1u;
      }
      stamp<3>(c);
      __builtin_amdgcn_s_setprio(1);
      wait_lds<2, 1>(c, F_QGO, 1u);
      const int pt3 = pts + 4096;
      const unsigned vo3 = (lane >> 2) * 2752 + (lane & 3) * 16;
      const char* w3 = (const char*)(hh ? wu : wg) + (size_t)(cl * 32) * 2752;
#pragma unroll
      for (int i = 0; i < 3; ++i) {
        const int aa = g0 + min(i, ng - 1);
        glds(w3 + aa * 64, vo3, pt3 + i * 2048);
        glds(w3 + aa * 64 + 16 * 2752, vo3, pt3 + i * 2048 + 1024);
      }
      st_ws(c, f0, lv, WS_WT1 + m * 96 * KB + (cl * 4 + hh) * KB);
      st_ws(c, f1, lv, WS_WT1 + m * 96 * KB + (cl * 4 + 2 + hh) * KB);
      st_ws(c, f2, lv, WS_WT1 + m * 96 * KB + (64 + cl * 2 + hh) * KB);
      VMWAIT(0);
#pragma unroll
      for (int i = 0; i < 3; ++i) {
        const int aa = g0 + min(i, ng - 1);
        const v4u g0f = tr_frag(L + pt3 + i * 2048 + trS), g1f = tr_frag(L + pt3 + i * 2048 + 1024 + trS);
        st_ws(c, g0f, lv, WS_WT3 + aa * 64 * KB + (cl * 4 + hh) * KB);
        st_ws(c, g1f, lv, WS_WT3 + aa * 64 * KB + (cl * 4 + 2 + hh) * KB);
        const float s = red_h(csum(g1f, csum(g0f, 0.f)));
        if (h == 0) __builtin_amdgcn_raw_buffer_store_b32(f2u(s), c.rs, l31 * 4, WS_CSP + aa * 4 * KB + cl * 256 + hh * 128, 16);
      }
      MEMBAR();
      *flg(c, F_JOB + hh) = 1u;
    }
    VMWAIT(0);
    if (lane == 0) __builtin_amdgcn_raw_buffer_store_b64(tag2, c.rs, 0, WS_F1 + ((m * 16 + cl) * 2 + hh) * 64, 16);
    stamp<1>(c); stamp<3>(c);
    v4u wc[40];
    cache_load(wc, IC<5>{});
    cache_conv(wc, IC<5>{});
    if (hh == 1) {
      wait_lds<1, 2>(c, F_PTOK, 1u);
#pragma unroll 1
      for (int i = 0; i < ng; ++i) {
        float t = 0.f;
#pragma unroll
        for (int q = 0; q < 16; ++q) t += u2f(__builtin_amdgcn_raw_buffer_load_b32(c.rs, lane * 4 + q * 256, WS_CSP + (g0 + i) * 4 * KB, 16));
        *(LAS v4u*)(L + L_CS + (3 + 2 * i) * 512 + lv) = cs_operand(t);
      }
      MEMBAR();
      *flg(c, F_CS3R) = 1u;
      MEMBAR();
    }
    stamp<1>(c);
    __builtin_amdgcn_s_setprio(1);
    cache_put(wc, IC<5>{}, 10 + 4 * ng);
    stamp<1>(c);
    if (b == 0 && wave == 6 && lane == 0) __hip_atomic_store((GAS u64*)(ws + WS_E), tag, RLX_AGENT);
    stamp<1>(c);
    __builtin_amdgcn_s_setprio(1);
    cache_put(wc, IC<5>{}, 31 + 8 * ng);
  }
}

extern "C" void kernel_launch(void* const* d_in, const int* in_sizes, int n_in, void* d_out, int out_size, void* d_ws, size_t ws_size,
                              hipStream_t stream) {
  fused_fwd<<<256, 512, 0, stream>>>((const u16*)d_in[0], (const u16*)d_in[1], (const u16*)d_in[2], (const u16*)d_in[4], (const u16*)d_in[5],
                                     (const u16*)d_in[6], (u16*)d_out, (char*)d_ws);
}
```

```cpp
#include <hip/hip_runtime.h>
#include <stdint.h>
#include <utility>

#define GAS __attribute__((address_space(1)))
#define LAS __attribute__((address_space(3)))
#define RLX_AGENT __ATOMIC_RELAXED, __HIP_MEMORY_SCOPE_AGENT
typedef unsigned v4u __attribute__((ext_vector_type(4)));
typedef unsigned v2u __attribute__((ext_vector_type(2)));
typedef float f32x2 __attribute__((ext_vector_type(2)));
typedef float f32x4 __attribute__((ext_vector_type(4)));
typedef float f32x16 __attribute__((ext_vector_type(16)));
typedef short s16x8 __attribute__((ext_vector_type(8)));
typedef short s16x4 __attribute__((ext_vector_type(4)));
typedef __bf16 bf16x2 __attribute__((ext_vector_type(2)));
typedef unsigned short u16;
typedef unsigned long long u64;

constexpr int XD = 12;
constexpr int YOFF = 14;
constexpr int KVDELAY = 18;
constexpr int KVDP = 14;
constexpr int BACKOFF = 4;
constexpr int NG2S = 90;
constexpr int NG2T = 150;
constexpr int S2H = 44;
constexpr int XPRE = 5;
constexpr int CODEPF = 59392;

constexpr int KB = 1024;
constexpr int WS_E = 0;
constexpr int WS_F1 = 4 * KB;
constexpr int WS_WT1 = 128 * KB;
constexpr int WS_WT3 = WS_WT1 + 16 * 96 * KB;
constexpr int WS_CSP = WS_WT3 + 43 * 64 * KB;
constexpr int WS_S0P = WS_CSP + 43 * 4 * KB;
constexpr int WS_C1P = WS_S0P + 32 * KB;
constexpr int KVR = 2176;
constexpr int WS_KVP = WS_C1P + 16 * 8 * KB;
constexpr int REC = 2560;
constexpr int WS_ACT = 6 * KB * KB, ACT_CL = KB * KB;
constexpr int WS_O = WS_ACT;
constexpr int WS_X = WS_O + 4 * 16 * REC;
constexpr int WS_H = WS_X + 2 * 4 * 16 * REC;
static_assert(WS_KVP + 512 * KVR <= WS_ACT && WS_H + 4 * 43 * REC <= WS_ACT + ACT_CL, "ws");
constexpr int WS_END = WS_ACT + 16 * ACT_CL;

constexpr int SLOT = 16384, NSLOT = 8;
constexpr int L_FLG = 0;
constexpr int L_ZF = L_FLG + 128;
constexpr int L_CS = L_ZF + 64;
constexpr int L_KVF = L_CS + 9 * 512;
constexpr int L_KVS = L_KVF + 1024 + 1152;
constexpr int L_XCH = 16384;
constexpr int L_RING = 32768;
constexpr int L_END = L_RING + NSLOT * SLOT;
static_assert(L_KVS + 4 * 2304 <= L_XCH && L_END == 163840 && (L_KVS & 15) == 0 && (L_CS & 15) == 0, "lds");
enum { F_FULL = 0, F_TAG = 2, F_FREE = 4, F_JOB = 8, F_KFL = 12, F_QGO = 10, F_CONV = 24, F_CS3R = 31, F_PTOK = 30, F_KVR = 16, F_XF = 20, F_CS1R = 28 };
constexpr unsigned SPIN_CAP = 1u << 17;

template <int V> struct IC { static constexpr int v = V; __device__ constexpr operator int() const { return V; } };
template <class F, int... I> __device__ __forceinline__ void sfor_(F&& fn, std::integer_sequence<int, I...>) { (fn(IC<I>{}), ...); }
template <int N, class F> __device__ __forceinline__ void sfor(F&& fn) { sfor_(fn, std::make_integer_sequence<int, N>{}); }
#define INL __attribute__((always_inline))
#define MEMBAR() asm volatile("" ::: "memory")
#define SB() __builtin_amdgcn_sched_barrier(0)
#define VMWAIT(n) asm volatile("s_waitcnt vmcnt(" #n ")" ::: "memory")
#define PIN(x) asm volatile("" : "+v"(x))

__device__ __forceinline__ unsigned umin(unsigned a, unsigned b) { return a < b ? a : b; }
__device__ __forceinline__ int gsplit(int i) { return (43 * i) >> 4; }
__device__ __forceinline__ f32x16 mfma(v4u a, v4u b, f32x16 c) {
  return __builtin_amdgcn_mfma_f32_32x32x16_bf16(__builtin_bit_cast(s16x8, a), __builtin_bit_cast(s16x8, b), c, 0, 0, 0);
}
__device__ __forceinline__ f32x16 zero16() { return f32x16{0.f, 0.f, 0.f, 0.f, 0.f, 0.f, 0.f, 0.f, 0.f, 0.f, 0.f, 0.f, 0.f, 0.f, 0.f, 0.f}; }
__device__ __forceinline__ unsigned pack2(float a, float b) {
  bf16x2 v = {(__bf16)a, (__bf16)b};
  return __builtin_bit_cast(unsigned, v);
}
__device__ __forceinline__ float u2f(unsigned u) { return __builtin_bit_cast(float, u); }
__device__ __forceinline__ unsigned f2u(float f) { return __builtin_bit_cast(unsigned, f); }
__device__ __forceinline__ float blo(unsigned u) { return u2f(u << 16); }
__device__ __forceinline__ float bhi(unsigned u) { return u2f(u & 0xffff0000u); }
__device__ __forceinline__ float relu(float x) { return u2f((unsigned)max((int)f2u(x), 0)); }
__device__ __forceinline__ float dot2(unsigned a, unsigned b, float acc) {
  return __builtin_amdgcn_fdot2_f32_bf16(__builtin_bit_cast(bf16x2, a), __builtin_bit_cast(bf16x2, b), acc, false);
}
constexpr unsigned ONES2 = 0x3f803f80u;
constexpr float LOG2E = 1.44269504f;
__device__ __forceinline__ float red_h(float v) {
  auto s = __builtin_amdgcn_permlane32_swap(f2u(v), f2u(v), false, false);
  return u2f((unsigned)s[0]) + u2f((unsigned)s[1]);
}
__device__ __forceinline__ float red_g(float v) {
  auto r = __builtin_amdgcn_permlane16_swap(f2u(v), f2u(v), false, false);
  return red_h(u2f((unsigned)r[0]) + u2f((unsigned)r[1]));
}
template <int CTRL> __device__ __forceinline__ float dpp(float v) { return u2f(__builtin_amdgcn_update_dpp(0, (int)f2u(v), CTRL, 0xf, 0xf, false)); }
__device__ __forceinline__ float exp2c(float x) { return __builtin_amdgcn_fmed3f(__builtin_amdgcn_exp2f(x), 0.f, 1.f); }
__device__ __forceinline__ void split2(float v, unsigned& hi, unsigned& lo) {
  hi = pack2(v, 0.f) & 0xffffu;
  lo = pack2(v - blo(hi), 0.f) & 0xffffu;
}
template <int D = 256> __device__ __forceinline__ v4u tr_frag(LAS char* p) {
  v2u lo = __builtin_bit_cast(v2u, __builtin_amdgcn_ds_read_tr16_b64_v4i16((LAS s16x4*)p));
  v2u hi = __builtin_bit_cast(v2u, __builtin_amdgcn_ds_read_tr16_b64_v4i16((LAS s16x4*)(p + D)));
  return v4u{lo.x, lo.y, hi.x, hi.y};
}
__device__ __forceinline__ void glds(const void* sbase, unsigned voff, unsigned ldst) {
  asm volatile("s_mov_b32 m0, %2\n\ts_nop 0\n\tglobal_load_lds_dwordx4 %0, %1" ::"v"(voff), "s"(sbase), "s"(ldst) : "memory");
}
__device__ __forceinline__ void glds4(const void* sbase, unsigned voff, unsigned ldst) {
  asm volatile(
      "s_mov_b32 m0, %2\n\ts_nop 0\n\tglobal_load_lds_dwordx4 %0, %1 sc1\n\tglobal_load_lds_dwordx4 %0, %1 offset:1024 sc1\n\t"
      "global_load_lds_dwordx4 %0, %1 offset:2048 sc1\n\tglobal_load_lds_dwordx4 %0, %1 offset:3072 sc1" ::"v"(voff), "s"(sbase), "s"(ldst)
      : "memory");
}

struct Ctx {
  LAS char* L;
  __amdgpu_buffer_rsrc_t rs;
  int lane, wave;
  int seq, known;
  int vb0, vb1, nb0, nb1;
  int fa;
  v2u fpre;
  v4u w0, w1, w2, w3;
  template <int I> __device__ __forceinline__ v4u& wf() { if constexpr (I == 0) return w0; else if constexpr (I == 1) return w1; else if constexpr (I == 2) return w2; else return w3; }
};
__device__ __forceinline__ volatile LAS unsigned* flg(Ctx& c, int i) { return (volatile LAS unsigned*)(c.L + L_FLG) + i; }
template <int N, int SLP> __device__ __forceinline__ void wait_lds(Ctx& c, int i, unsigned val) {
#pragma nounroll
  for (unsigned sp = 0; sp < SPIN_CAP; ++sp) {
    unsigned v;
    if (N == 1) v = *flg(c, i);
    else if (N == 2) { v2u f = *(volatile LAS v2u*)flg(c, i); v = umin(f.x, f.y); }
    else { v4u f = *(volatile LAS v4u*)flg(c, i); v = umin(umin(f.x, f.y), umin(f.z, f.w)); }
    if ((unsigned)__builtin_amdgcn_readfirstlane(v) >= val) break;
    if (SLP) __builtin_amdgcn_s_sleep(SLP);
  }
  MEMBAR();
}
template <int N> __device__ __forceinline__ void poll_tags(Ctx& c, int base, v2u tag) {
#pragma nounroll
  for (unsigned sp = 0; sp < SPIN_CAP; ++sp) {
    const v2u v = __builtin_amdgcn_raw_buffer_load_b64(c.rs, (c.lane & (N - 1)) * 64, base, 16);
    if (__all(v.x == tag.x && v.y == tag.y)) break;
    __builtin_amdgcn_s_sleep(4);
    MEMBAR();
  }
  MEMBAR();
}
__device__ __forceinline__ v4u ld_ws(Ctx& c, int voff, int soff) { return __builtin_amdgcn_raw_buffer_load_b128(c.rs, voff, soff, 16); }
__device__ __forceinline__ void st_ws(Ctx& c, v4u v, int voff, int soff) {
  __builtin_amdgcn_raw_buffer_store_b128(v, c.rs, voff, soff, 16);
  asm volatile("s_nop 1" ::"v"(v) : "memory");
}
__device__ __forceinline__ int fresh_lane() { int l; asm volatile("v_mbcnt_lo_u32_b32 %0, -1, 0\n\tv_mbcnt_hi_u32_b32 %0, -1, %0" : "=v"(l)); __builtin_assume(l >= 0 && l < 64); return l; }
__device__ __forceinline__ int opq(int v) { asm volatile("" : "+s"(v)); return v; }
__device__ __forceinline__ int opqv(int v) { asm volatile("" : "+v"(v)); return v; }

constexpr int fps(int mode) { return (mode == 1 || mode == 4) ? 8 : 16; }
constexpr bool two(int mode) { return mode == 1 || mode == 2; }
__device__ __forceinline__ void slot_wait(Ctx& c, int sq) {
  if (__builtin_expect(c.known <= sq, 0)) {
#pragma nounroll
    for (unsigned sp = 0; sp < SPIN_CAP; ++sp) {
      v2u f = *(volatile LAS v2u*)flg(c, F_FULL);
      c.known = __builtin_amdgcn_readfirstlane(umin(f.x, f.y));
      if (c.known > sq) break;
    }
  }
  MEMBAR();
}
template <int MODE> __device__ __forceinline__ void slot_base(Ctx& c, int sq, int lb0, int lb1) {
  const int sb = (sq & (NSLOT - 1)) * SLOT;
  c.nb0 = lb0 + sb; PIN(c.nb0);
  if (two(MODE)) { c.nb1 = lb1 + sb; PIN(c.nb1); }
}
template <int MODE> __device__ __forceinline__ void slot_next(Ctx& c) {
  c.nb0 = (c.vb0 + SLOT) & (NSLOT * SLOT - 1); PIN(c.nb0);
  if (two(MODE)) { c.nb1 = (c.vb1 + SLOT) & (NSLOT * SLOT - 1); PIN(c.nb1); }
}
template <int MODE, int F, bool OPEN = (F % fps(MODE) == 0)> __device__ __forceinline__ v4u wread(Ctx& c) {
  constexpr int i = F % fps(MODE);
  if constexpr (OPEN) { c.vb0 = c.nb0; if (two(MODE)) c.vb1 = c.nb1; }
  LAS char* R = c.L + L_RING;
  if (MODE == 0) return *(LAS v4u*)(R + c.vb0 + i * KB);
  if (MODE == 1) return tr_frag<512>(R + ((i & 1) ? c.vb1 : c.vb0) + ((i >> 1) & 1) * 2 * KB + (i >> 2) * 8 * KB);
  if (MODE == 4) return *(LAS v4u*)(R + c.vb0 + (i & 3) * KB + (i >> 2) * 8 * KB);
  if (MODE == 2) return tr_frag(R + ((i & 1) ? c.vb1 : c.vb0) + i * KB);
  return tr_frag(R + c.vb0 + i * KB);
}
__device__ __forceinline__ void ring_publish(Ctx& c) { MEMBAR(); *(volatile LAS unsigned*)(c.L + c.fa) = (unsigned)c.seq; MEMBAR(); }
template <int MODE> __device__ __forceinline__ void wpre(Ctx& c, int lb0, int lb1) {
  slot_wait(c, c.seq);
  slot_base<MODE>(c, c.seq, lb0, lb1);
  sfor<4>([&](auto F) INL { c.wf<F.v>() = wread<MODE, F.v>(c); });
  if (fps(MODE) == 8) c.fpre = *(volatile LAS v2u*)flg(c, F_FULL);
}
struct NoSide { template <class T> __device__ __forceinline__ void operator()(T) const {} };
constexpr int fmap(int f, int nf, int fpsl, int tf) {
  if (tf < 0 || f < nf - fpsl || f >= nf) return f;
  const int i = f - (nf - fpsl), hf = fpsl / 2;
  return nf - fpsl + 2 * (i % hf) + (i < hf ? tf : 1 - tf);
}
constexpr u64 DUAL = 0xFEDC76B54A329180ull;
constexpr int dual_first(int ch) { for (int u = 0; u < 16; ++u) if (((DUAL >> (4 * u)) & 15) == (u64)ch) return u; return -1; }
template <int NF, int TPS, int MODE, int NEXT, bool XA, int ZERO, int RELF, int EARLY, int CAP, int TF, class SIDE>
__device__ __forceinline__ void gemm4(Ctx& c, v4u (&a)[32], f32x16& c0, f32x16& c1, f32x16& c2, f32x16& c3, int lb0, int lb1, int nb0, int nb1, SIDE side) {
  constexpr int FPS = fps(MODE);
  static_assert(NF % FPS == 0, "whole slots");
  sfor<NF>([&](auto F) INL {
    constexpr int f = F.v, un = NF == 128 ? (int)((DUAL >> (4 * (f / 8))) & 15) : 0;
    constexpr int fm = fmap(f, NF, FPS, TF);
    constexpr int s = NF == 128 ? 4 * (un & 7) + (f % 8) / 2 : fm / TPS, t = fm % TPS, fn = f + 4, i = fn % FPS;
    const v4u x = a[s & 31], wv = c.wf<f % 4>();
    f32x16& acc = un >= 8 ? (t ? c3 : c2) : (t ? c1 : c0);
    const f32x16 cin = (s == 0 && ((ZERO >> t) & 1)) ? zero16() : acc;
    acc = XA ? mfma(x, wv, cin) : mfma(wv, x, cin);
    PIN(acc);
    SB();
    if constexpr (fn < NF) {
      c.wf<f % 4>() = wread<MODE, fmap(fn, NF, FPS, TF), i == 0>(c);
      constexpr bool lastslot = fn / FPS == NF / FPS - 1;
      if constexpr (!lastslot || NEXT >= 0) {
        constexpr int sl = fn / FPS, ahead = FPS == 8 && sl + 2 < NF / FPS ? 2 : 1;
        if constexpr (FPS == 16 || !(sl & 1)) {
          if constexpr (i == (FPS == 8 ? 2 : FPS - 7)) c.fpre = *(volatile LAS v2u*)flg(c, F_FULL);
          if constexpr (i == FPS - 4) { c.fpre.x = umin(c.fpre.x, c.fpre.y); PIN(c.fpre); }
          if constexpr (i == FPS - 3) { c.known = __builtin_amdgcn_readfirstlane(c.fpre.x); asm volatile("" : "+s"(c.known)); }
          if constexpr (i == FPS - 1) slot_wait(c, c.seq + ahead);
        }
        if constexpr (i == FPS - 3) {
          if constexpr (lastslot && NEXT != MODE) slot_base<(NEXT < 0 ? 0 : NEXT)>(c, c.seq + 1, nb0, nb1); else slot_next<MODE>(c);
        }
      }
      if constexpr (i == FPS - 1) c.seq++;
    } else if constexpr (NEXT >= 0) {
      c.wf<f % 4>() = wread<(NEXT < 0 ? 0 : NEXT), fn - NF>(c);
      if constexpr (fps(NEXT) == 8 && fn - NF == 1) c.fpre = *(volatile LAS v2u*)flg(c, F_FULL);
    }
    if constexpr (fn >= FPS && fn <= NF + 2 && i == 2) { if constexpr ((f >= RELF || fn / FPS - 1 < EARLY) && fn / FPS - 1 < CAP) ring_publish(c); }
    else if constexpr (f == RELF && RELF > 0) ring_publish(c);
    side(F);
    SB();
  });
}
template <int NF, int TPS, int MODE, int NEXT, bool XA, int ZERO, int RELF, int EARLY, int CAP, int TF = -1, class SIDE>
__device__ __forceinline__ void gemm(Ctx& c, v4u (&a)[32], f32x16& c0, f32x16& c1, int lb0, int lb1, int nb0, int nb1, SIDE side) {
  gemm4<NF, TPS, MODE, NEXT, XA, ZERO, RELF, EARLY, CAP, TF>(c, a, c0, c1, c0, c1, lb0, lb1, nb0, nb1, side);
}
__device__ __forceinline__ unsigned sum4(unsigned m, v4u v) { return ((m + v.x + v.y) + v.z + v.w); }
template <bool EX> __device__ __forceinline__ void ld_chk(Ctx& c, v2u& k, int sb) {
  if (EX) k = __builtin_amdgcn_raw_buffer_load_b64(c.rs, c.lane * 8 + 2 * KB, sb, 16);
  else k.x = __builtin_amdgcn_raw_buffer_load_b32(c.rs, c.lane * 4 + 2 * KB, sb, 16);
}
template <int STR, bool EX> __device__ __forceinline__ void ld_rec(Ctx& c, v4u (&a)[32], v2u (&k)[16], int lv, int sb, int p, int e) {
  const int so = sb + STR * p * REC;
  if (e < 2) a[(2 * p + e) & 31] = ld_ws(c, lv + e * KB, so);
  else ld_chk<EX>(c, k[p & 15], so);
}
template <bool EX> __device__ __forceinline__ unsigned rec_bad(v4u (&a)[32], v2u (&k)[16], int p, unsigned tag) {
  return sum4(sum4(EX ? k[p & 15].x + k[p & 15].y : k[p & 15].x, a[(2 * p) & 31]), a[(2 * p + 1) & 31]) ^ tag;
}
template <int N, int STR, bool EX> __device__ __forceinline__ void settle(Ctx& c, unsigned bad, v4u (&a)[32], v2u (&k)[16], int lv, int sb, int p0, unsigned tag) {
  if (__builtin_expect(__any(bad != 0u), 0)) {
    unsigned sp = 0;
#pragma nounroll
    do {
      __builtin_amdgcn_s_sleep(BACKOFF);
      MEMBAR();
      bad = 0;
#pragma unroll
      for (int i = 0; i < 3 * N; ++i) ld_rec<STR, EX>(c, a, k, lv, sb, p0 + i / 3, i % 3);
#pragma unroll
      for (int i = 0; i < N; ++i) bad |= rec_bad<EX>(a, k, p0 + i, tag);
    } while (__any(bad != 0u) && ++sp < SPIN_CAP);
  }
}
template <int F, int NP, int STR, bool EX>
__device__ __forceinline__ void check_step(Ctx& c, unsigned& m0, unsigned& m1, v4u (&a)[32], v2u (&k)[16], int lv, int sb, unsigned tag) {
  constexpr int p = 2 * ((F + 8) / 8), r = (F + 8) % 8;
  if constexpr (p < NP) {
    if constexpr (r == 2) { m0 = sum4(EX ? k[p & 15].x + k[p & 15].y : k[p & 15].x, a[(2 * p) & 31]); PIN(m0); }
    if constexpr (r == 3) { m0 = sum4(m0, a[(2 * p + 1) & 31]); PIN(m0); }
    if constexpr (r == 4) { m1 = sum4(EX ? k[(p + 1) & 15].x + k[(p + 1) & 15].y : k[(p + 1) & 15].x, a[(2 * p + 2) & 31]); PIN(m1); }
    if constexpr (r == 5) { m1 = sum4(m1, a[(2 * p + 3) & 31]); PIN(m1); }
    if constexpr (r == 6) { m0 = (m0 ^ tag) | (m1 ^ tag); PIN(m0); }
    if constexpr (r == 7) settle<2, STR, EX>(c, m0, a, k, lv, sb, p, tag);
  }
}
template <int STR, bool EX> __device__ __forceinline__ void open_phase(Ctx& c, v4u (&a)[32], v2u (&k)[16], int lv, int sb, unsigned tag) {
  VMWAIT(0);
  __builtin_amdgcn_s_sleep(XD);
#pragma unroll
  for (int i = 0; i < 12; ++i) ld_rec<STR, EX>(c, a, k, lv, sb, i / 3, i % 3);
  SB();
  settle<2, STR, EX>(c, rec_bad<EX>(a, k, 0, tag) | rec_bad<EX>(a, k, 1, tag), a, k, lv, sb, 0, tag);
}
template <int F, int P0, int P1, int STR, bool EX> __device__ __forceinline__ void req_step(Ctx& c, v4u (&a)[32], v2u (&k)[16], int lv, int sb) {
  if constexpr (F >= 0 && P0 + F / 2 < P1) {
    ld_rec<STR, EX>(c, a, k, lv, sb, P0 + F / 2, F & 1);
    if constexpr (F & 1) ld_rec<STR, EX>(c, a, k, lv, sb, P0 + F / 2, 2);
  }
}
template <int G, int PER, int P0, int P1, int STR, bool EX> __device__ __forceinline__ void req_pace(Ctx& c, v4u (&a)[32], v2u (&k)[16], int lv, int sb) {
  if constexpr (G >= 0 && P0 + G / PER < P1) {
    constexpr int r = G % PER - (PER == 4 ? (G / 4) & 1 : 0), D = PER / 3;
    if constexpr (r >= 0 && r % D == 0 && r / D < 3) ld_rec<STR, EX>(c, a, k, lv, sb, P0 + G / PER, r / D);
  }
}
template <bool EX> __device__ __forceinline__ void st_chk(Ctx& c, unsigned sum, unsigned ex, int sb, unsigned tag) {
  if (EX) __builtin_amdgcn_raw_buffer_store_b64(v2u{tag - sum - ex, ex}, c.rs, c.lane * 8 + 2 * KB, sb, 16);
  else __builtin_amdgcn_raw_buffer_store_b32(tag - sum, c.rs, c.lane * 4 + 2 * KB, sb, 16);
}
__device__ __forceinline__ u64 mix64(u64 z) {
  z += 0x9E3779B97F4A7C15ull;
  z = (z ^ (z >> 30)) * 0xBF58476D1CE4E5B9ull;
  z = (z ^ (z >> 27)) * 0x94D049BB133111EBull;
  return z ^ (z >> 31);
}
__device__ __forceinline__ float csum(v4u f, float s) { return dot2(f.w, ONES2, dot2(f.z, ONES2, dot2(f.y, ONES2, dot2(f.x, ONES2, s)))); }
__device__ __forceinline__ v4u cs_operand(float s) { unsigned hi, lo; split2(s, hi, lo); return v4u{hi | (hi << 16), lo, 0u, 0u}; }

__global__ __launch_bounds__(512, 2) void fused_fwd(const u16* __restrict__ x, const u16* __restrict__ wqkv, const u16* __restrict__ wo,
                                                    const u16* __restrict__ wg, const u16* __restrict__ wu, const u16* __restrict__ wd,
                                                    u16* __restrict__ out, char* __restrict__ ws) {
  __shared__ __attribute__((aligned(16))) char smem[L_END];
  u64 pc0;
  asm volatile("s_getpc_b64 %0" : "=s"(pc0));
  Ctx c;
  c.L = (LAS char*)smem;
  c.rs = __builtin_amdgcn_make_buffer_rsrc((void*)ws, 0, WS_END, 0x00020000);
  const int wave = __builtin_amdgcn_readfirstlane(threadIdx.x >> 6);
  const int b = blockIdx.x, xc = b & 7, j = b >> 3;
  const int cl = (xc >> 1) * 4 + (j >> 3), m = (xc & 1) * 8 + (j & 7);
  const int rh = m >> 3, cg = m & 7;
  const int g0 = gsplit(m), ng = gsplit(m + 1) - g0;
  c.lane = 0; c.wave = wave; c.seq = 0; c.known = 0; c.vb0 = 0; c.vb1 = 0; c.nb0 = 0; c.nb1 = 0; c.fa = 0; c.fpre = v2u{0u, 0u};
  const int rot = (2 * cl + m) & 7;
  LAS char* L = c.L;
  if (threadIdx.x < 32) *flg(c, threadIdx.x) = 0u;
  asm volatile("s_waitcnt lgkmcnt(0)\n\ts_barrier" ::: "memory");
#define LANEVARS                                                                                                              \
  const int lane = fresh_lane(), h = lane >> 5, l31 = lane & 31, lv = lane * 16;                                               \
  const int trN = (8 * h + ((lane >> 2) & 3)) * 64 + ((lane >> 4) & 1) * 32 + (lane & 3) * 8;                                   \
  const int trS = (8 * h + ((lane >> 2) & 3)) * 64 + ((lane >> 4) & 1) * 32 + (((lane & 1) << 1) | ((lane >> 1) & 1)) * 8;       \
  (void)trN; (void)trS; (void)l31; (void)lv;
  const int pp = wave & 1;
  auto load_half0 = [&](int sl) INL {
    const int lane = fresh_lane();
    const unsigned vo1 = (lane >> 2) * 3072 + (lane & 3) * 16;
    const char* wb = (const char*)wqkv + m * 64 + (sl < 4 ? (1 + pp) * 1024 : 0);
#pragma unroll
    for (int p = 0; p < 8; ++p) {
      const int s = sl < 4 ? sl * 8 + p : (sl - 4) * 16 + 2 * p + pp;
      glds(wb + (size_t)(((s + 4 * rot) & 31) * 16) * 3072, vo1, L_RING + sl * SLOT + p * 2048 + pp * 1024);
    }
  };
  auto cache_load = [&](auto& wc, auto NK) INL {
    const int lane = fresh_lane();
    const __amdgpu_buffer_rsrc_t rd = __builtin_amdgcn_make_buffer_rsrc((void*)wd, 0, 1376 * 1024, 0x00020000);
    const int vo2 = (lane >> 3) * 1024 + (((lane & 7) ^ (((lane >> 4) & 1) << 2)) * 16);
#pragma unroll
    for (int k = 0; k < NK.v; ++k) {
#pragma unroll
      for (int q = 0; q < 2; ++q) {
        const int hs = (NK.v == 5 ? 2 * k : k < 5 ? 2 * k + 1 : 10) * 2 + pp;
        const int p = min(hs * 2 + q, 42);
        const int m3 = min(p - 32 + 1 + ((p - 32) >> 1), 15);
        const int aa = p < 32 ? gsplit(p & 15) + (p >> 4) : gsplit(m3) + 2;
#pragma unroll
        for (int e = 0; e < 4; ++e) wc[k * 8 + q * 4 + e] = __builtin_amdgcn_raw_buffer_load_b128(rd, vo2, aa * 32768 + cg * 128 + e * 8192, 0);
        SB();
        if (NK.v == 5) __builtin_amdgcn_s_sleep(2);
      }
    }
  };
  auto cache_conv = [&](auto& wc, auto NK) INL {
    const int lane = fresh_lane(), h = lane >> 5;
    const int tk = (8 * h + ((lane >> 2) & 3)) * 128 + ((lane >> 4) & 1) * 32 + (((lane & 1) << 1) | ((lane >> 1) & 1)) * 8 + ((lane >> 3) & 1) * 64;
    LAS char* d = L + L_XCH + (wave - 4) * 4096;
    wait_lds<4, 2>(c, F_FREE, 4u);
#pragma unroll
    for (int q = 0; q < 2 * NK.v; ++q) {
#pragma unroll
      for (int e = 0; e < 4; ++e) *(LAS v4u*)(d + lane * 16 + e * KB) = wc[q * 4 + e];
      MEMBAR(); SB();
#pragma unroll
      for (int e = 0; e < 4; ++e) wc[q * 4 + e] = tr_frag<512>(d + (e >> 1) * 2048 + (tk ^ (((e ^ q) & 1) * 64)));
      MEMBAR(); SB();
    }
    *flg(c, F_CONV + wave - 4) = 1u;
    MEMBAR();
  };
  auto cache_put = [&](auto& wc, auto NK, int base) INL {
    const int lane = fresh_lane();
#pragma unroll
    for (int k = 0; k < NK.v; ++k) {
      const int sq = base + (NK.v == 5 ? 2 * k : k < 5 ? 2 * k + 1 : 10);
      wait_lds<4, 4>(c, F_FREE, (unsigned)(sq - NSLOT + 1));
      LAS char* d = L + L_RING + (sq & (NSLOT - 1)) * SLOT + pp * 8192;
#pragma unroll
      for (int e = 0; e < 8; ++e) {
        *(LAS v4u*)(d + lane * 16 + e * KB) = wc[k * 8 + e];
        asm volatile("s_nop 3" ::: "memory");
      }
      wait_lds<1, 2>(c, F_FULL + pp, (unsigned)sq);
      *flg(c, F_FULL + pp) = (unsigned)(sq + 1);
      MEMBAR();
    }
  };

  if (wave < 4) {
    __builtin_amdgcn_s_setprio(2);
    const int w = wave, mh = w >> 1, kh = w & 1;
    c.fa = opqv(L_FLG + (F_FREE + wave) * 4);
    asm volatile("" : "+s"(c.rs));
    v4u a[32], xr0, xr1;
    v2u k[16];
    f32x16 A0, A1, B0, B1;
    LANEVARS
    c.lane = lane;
    float rstd;
    unsigned tagx, tagy, tagc;
    v4u mfrag;
    auto set_stats = [&](float sum, float ssq) INL -> float {
      const float s = sum * (1.f / 512), q = ssq * (1.f / 512);
      unsigned hi, lo;
      split2(-s, hi, lo);
      mfrag = h == 0 ? v4u{hi | (lo << 16), hi, 0u, 0u} : v4u{0u, 0u, 0u, 0u};
      return fmaxf(q - s * s, 0.f) + 1e-5f;
    };
    auto cs_frag = [&](int tile) INL { return *(LAS v4u*)(L + L_CS + tile * 512 + l31 * 16); };
    auto rs_put = [&]() INL { const int lane = fresh_lane(); *(LAS float*)(L + L_KVS + w * 2304 + lane * 4) = h ? rstd * LOG2E : rstd; MEMBAR(); };
    auto rs_get = [&](int q) INL {
      const int h = fresh_lane() >> 5;
      const f32x4 r = *(LAS f32x4*)(L + L_KVS + w * 2304 + (8 * q + 4 * h) * 4), rl = *(LAS f32x4*)(L + L_KVS + w * 2304 + 128 + (8 * q + 4 * h) * 4);
#pragma unroll
      for (int i = 0; i < 4; ++i) { B0[4 * q + i] = r[i]; B1[4 * q + i] = rl[i]; }
    };
    {
      const __amdgpu_buffer_rsrc_t rx = __builtin_amdgcn_make_buffer_rsrc((void*)x, 0, 2048 * 1024, 0x00020000);
      const int r8 = lane >> 3;
      const unsigned xv0 = r8 * 1024 + (((lane & 7) ^ (r8 >> 1)) * 16), xv1 = xv0 ^ 64;
      const char* xb = (const char*)x + (size_t)(cl * 128 + w * 32) * 1024;
      const int ro = l31 * 128 + ((h ^ ((l31 >> 1) & 7)) * 16);
      auto xbuf = [&](int kp) INL { return (kp % 3 == 1 ? L_XCH : L_RING + (4 + (kp % 3) / 2) * SLOT) + w * 4096; };
      auto xreq = [&](int kp) INL {
#pragma unroll
        for (int q = 0; q < 4; ++q) glds(xb + q * 8192 + ((kp + rot) & 7) * 128, (q & 1) ? xv1 : xv0, xbuf(kp) + q * 1024);
      };
      auto xget = [&](int kp) INL {
        if (kp < 6) VMWAIT(8); else if (kp == 6) VMWAIT(4); else VMWAIT(0);
#pragma unroll
        for (int q = 0; q < 4; ++q) a[kp * 4 + q] = *(LAS v4u*)(L + xbuf(kp) + (ro ^ (q * 32)));
        if (kp + 3 < 8) {
          asm volatile("s_waitcnt lgkmcnt(0)" : "+v"(a[kp * 4]), "+v"(a[kp * 4 + 1]), "+v"(a[kp * 4 + 2]), "+v"(a[kp * 4 + 3])::"memory");
          xreq(kp + 3);
        }
        MEMBAR();
      };
      __builtin_amdgcn_s_setprio(3);
      xreq(0); xreq(1); xreq(2);
      __builtin_amdgcn_s_setprio(2);

#pragma unroll
      for (int kp = 0; kp < XPRE; ++kp) xget(kp);
      wpre<2>(c, trN, trS);
      v4u s0, ca, cb;
      const int s0b = WS_S0P + (cl * 128 + w * 32) * 16;
      gemm<64, 2, 2, -1, true, 3, 0, 0, 99>(c, a, A0, A1, trN, trS, 0, 0, [&](auto F) INL {
        constexpr int f = F.v;
        if ((f & 3) == 1 && (f >> 2) + XPRE < 8) xget((f >> 2) + XPRE);
        if (f == 4 * (8 - XPRE)) s0 = ld_ws(c, l31 * 16, s0b);
        if (f == 4 * (8 - XPRE) + 2) {
          const int xq = l31 * 1024 + h * 16, xqs = (cl * 128 + mh * 64 + rh * 32) * 1024 + cg * 128 + kh * 64;
          xr0 = __builtin_amdgcn_raw_buffer_load_b128(rx, xq, xqs, 0);
          xr1 = __builtin_amdgcn_raw_buffer_load_b128(rx, xq + 32, xqs, 0);
        }
        if (f == 40) {
          wait_lds<2, 0>(c, F_CS1R, 1u);
          const v2u tg = *(volatile LAS v2u*)flg(c, F_TAG);
          tagx = __builtin_amdgcn_readfirstlane(tg.x); tagy = __builtin_amdgcn_readfirstlane(tg.y); tagc = (tagx * 0x9E3779B1u + tagy) | 1u;
          if (__any(s0.y != tagx || s0.w != tagy)) {
            unsigned sp = 0;
#pragma nounroll
            do { MEMBAR(); s0 = ld_ws(c, l31 * 16, s0b); } while (__any(s0.y != tagx || s0.w != tagy) && ++sp < SPIN_CAP);
          }
          rstd = __builtin_amdgcn_rsqf(set_stats(u2f(s0.x), u2f(s0.z)));
          rs_put();
          ca = cs_frag(0); cb = cs_frag(1);
        }
        if (f == 45) { A0 = mfma(mfrag, ca, A0); A1 = mfma(mfrag, cb, A1); }
        if (f >= 54 && f < 62 && !(f & 1)) rs_get((f - 54) >> 1);
      });
    }
#pragma unroll 1
    for (int layer = 0; layer < 2; ++layer) {
      bool publisher;
      {
        LANEVARS
#pragma unroll
        for (int i = 0; i < 16; ++i) B1[i] *= A0[i];
        SB();
#pragma unroll
        for (int i = 0; i < 16; ++i) { B1[i] = exp2c(B1[i]); A0[i] = fmaf(A0[i], B0[i], 1.f); A1[i] *= B0[i]; }
        SB();
        v4u pk0, pk1, vv0, vv1;
#pragma unroll
        for (int i = 0; i < 16; ++i) A0[i] = fmaxf(A0[i], B1[i]) + 1e-6f;
#pragma unroll
        for (int i = 0; i < 4; ++i) {
          pk0[i] = pack2(A0[2 * i], A0[2 * i + 1]); vv0[i] = pack2(A1[2 * i], A1[2 * i + 1]);
          pk1[i] = pack2(A0[8 + 2 * i], A0[9 + 2 * i]); vv1[i] = pack2(A1[8 + 2 * i], A1[9 + 2 * i]);
        }
        f32x16 kv = mfma(pk0, vv0, zero16());
        kv = mfma(pk1, vv1, kv);
        const float z = red_h(csum(pk0, 0.f) + csum(pk1, 0.f));
        LAS char* ks = L + L_KVS + w * 2304;
        if (h == 0) *(LAS float*)(ks + 2048 + l31 * 4) = z;
        if (lane & 16) {
          *(LAS f32x4*)(ks + lv) = f32x4{kv[8], kv[9], kv[10], kv[11]};
          *(LAS f32x4*)(ks + 1024 + lv) = f32x4{kv[12], kv[13], kv[14], kv[15]};
          MEMBAR();
        } else {
          MEMBAR();
          *(LAS f32x4*)(ks + lv) = f32x4{kv[0], kv[1], kv[2], kv[3]};
          *(LAS f32x4*)(ks + 1024 + lv) = f32x4{kv[4], kv[5], kv[6], kv[7]};
        }
        MEMBAR();
        *flg(c, F_KFL + w) = (unsigned)(layer + 1);
        const v4u kf = *(volatile LAS v4u*)flg(c, F_KFL);
        MEMBAR();
        f32x4 p0[4], p1[4];
        f32x2 pz[4];
#pragma unroll
        for (int ww = 0; ww < 4; ++ww) {
          p0[ww] = *(LAS f32x4*)(L + L_KVS + ww * 2304 + lv);
          p1[ww] = *(LAS f32x4*)(L + L_KVS + ww * 2304 + 1024 + lv);
          pz[ww] = *(LAS f32x2*)(L + L_KVS + ww * 2304 + 2048 + (lane & 15) * 8);
        }
        MEMBAR();
        SB();
        publisher = (unsigned)__builtin_amdgcn_readfirstlane(umin(umin(kf.x, kf.y), umin(kf.z, kf.w))) > (unsigned)layer;
        if (publisher) {
          const f32x4 k0 = (p0[0] + p0[1]) + (p0[2] + p0[3]), k1 = (p1[0] + p1[1]) + (p1[2] + p1[3]);
          const f32x2 zz = (pz[0] + pz[1]) + (pz[2] + pz[3]);
          const unsigned tg = layer ? tagy : tagx;
          const unsigned tl = tg >> ((lane & 1) * 16), t0 = tl & 0xffu, t1 = (tl >> 8) & 0xffu;
          const int kvb = WS_KVP + ((layer * 16 + m) * 16 + cl) * KVR;
          auto st2 = [&](float u, float v, int vo) INL {
            __builtin_amdgcn_raw_buffer_store_b64(v2u{(f2u(u) & 0xffffff00u) | t0, (f2u(v) & 0xffffff00u) | t1}, c.rs, vo, kvb, 16);
          };
          st2(k0[0], k0[1], lane * 8); st2(k0[2], k0[3], lane * 8 + 512); st2(k1[0], k1[1], lane * 8 + 1024); st2(k1[2], k1[3], lane * 8 + 1536);
          if (lane < 16) st2(zz[0], zz[1], lane * 8 + 2048);
        }
        MEMBAR();
      }
      B0 = mfma(cs_frag(2), mfrag, zero16());
      if (layer == 0) { LANEVARS wpre<3>(c, trN, trN); gemm<32, 1, 3, -1, false, 0, 0, 0, 99>(c, a, B0, B0, trN, trN, 0, 0, NoSide()); }
      else { wpre<0>(c, lv, lv); gemm<32, 1, 0, -1, false, 0, 0, 0, 99>(c, a, B0, B0, lv, lv, 0, 0, NoSide()); }
      const int lq = fresh_lane();
      const int kb0 = (8 * (lq >> 5) + ((lq >> 2) & 3)) * 128 + ((lq >> 4) & 1) * 32 + (((lq & 1) << 1) | ((lq >> 1) & 1)) * 8 + kh * 4096 + ((kh ^ (lq >> 3)) & 1) * 64;
      const int kb1 = kb0 ^ 64;
      auto res_init = [&](f32x16& R, int i) INL { const unsigned u = i < 4 ? xr0[i & 3] : xr1[i & 3]; R[2 * i] = blo(u); R[2 * i + 1] = bhi(u); };
      {
        LANEVARS
        v4u pq0, pq1;
        const float rl = rstd * LOG2E;
#pragma unroll
        for (int i = 0; i < 16; ++i) B1[i] = exp2c(B0[i] * rl);
#pragma unroll
        for (int i = 0; i < 16; ++i) B0[i] = fmaxf(fmaf(B0[i], rstd, 1.f), B1[i]) + 1e-6f;
#pragma unroll
        for (int i = 0; i < 4; ++i) { pq0[i] = pack2(B0[2 * i], B0[2 * i + 1]); pq1[i] = pack2(B0[8 + 2 * i], B0[9 + 2 * i]); }
#pragma unroll
        for (int i = 0; i < 8; ++i) res_init(A0, i);
        PIN(pq0); PIN(pq1); PIN(A0);
        const int ob = opq(WS_O + cl * ACT_CL + (w * 16 + m) * REC);
        {
          const int lane = fresh_lane();
          const unsigned tg = layer ? tagy : tagx;
          const unsigned tl = tg >> ((lane & 1) * 16), t0 = tl & 0xffu, t1 = (tl >> 8) & 0xffu;
          const int kvb = WS_KVP + (layer * 16 + m) * 16 * KVR;
          float s0 = 0.f, s1 = 0.f;
          v2u zp;
          unsigned zpk;
          if (publisher) __builtin_amdgcn_s_sleep(KVDP); else __builtin_amdgcn_s_sleep(KVDELAY);
#pragma nounroll
          for (unsigned sp = 0; sp < SPIN_CAP; ++sp) {
            v2u p[16];
            zp = __builtin_amdgcn_raw_buffer_load_b64(c.rs, (lane >> 2) * KVR + (lane & 3) * 8 + 2048, kvb + w * 32, 16);
#pragma unroll
            for (int i = 0; i < 16; ++i) p[i] = __builtin_amdgcn_raw_buffer_load_b64(c.rs, lane * 8, kvb + w * 512 + i * KVR, 16);
            SB();
            zp.x ^= t0; zp.y ^= t1;
            unsigned chk = zp.x | zp.y;
            float za = u2f(zp.x), zb = u2f(zp.y);
            za += dpp<0x128>(za); zb += dpp<0x128>(zb); za += dpp<0x124>(za); zb += dpp<0x124>(zb);
            zpk = pack2(red_g(za), red_g(zb));
            PIN(zpk);
#pragma unroll
            for (int i = 0; i < 16; ++i) {
              p[i].x ^= t0; p[i].y ^= t1; chk |= p[i].x | p[i].y;
              s0 = i ? s0 + u2f(p[i].x) : u2f(p[i].x); s1 = i ? s1 + u2f(p[i].y) : u2f(p[i].y);
              PIN(s0); PIN(s1);
            }
            if (!__any((chk & 0xffu) != 0u)) break;
            __builtin_amdgcn_s_sleep(BACKOFF);
            MEMBAR();
          }
          *(LAS unsigned*)(L + L_KVF + lv + w * 4) = pack2(s0, s1);
          if (lane < 4) *(LAS unsigned*)(L + L_ZF + w * 16 + lane * 4) = zpk;
          MEMBAR();
          *flg(c, F_KVR + w) = (unsigned)(layer + 1);
        }
        v4u kvf;
        v2u z0, z1, z2, z3;
#pragma nounroll
        for (unsigned sp = 0; sp < SPIN_CAP; ++sp) {
          const v4u fl = *(volatile LAS v4u*)flg(c, F_KVR);
          MEMBAR();
          kvf = *(LAS v4u*)(L + L_KVF + lv);
          z0 = *(LAS v2u*)(L + L_ZF + h * 8); z1 = *(LAS v2u*)(L + L_ZF + 16 + h * 8);
          z2 = *(LAS v2u*)(L + L_ZF + 32 + h * 8); z3 = *(LAS v2u*)(L + L_ZF + 48 + h * 8);
          MEMBAR();
          if ((unsigned)__builtin_amdgcn_readfirstlane(umin(umin(fl.x, fl.y), umin(fl.z, fl.w))) > (unsigned)layer) break;
        }
        const bool up = (lane & 16) != 0;
        const v4u zz = {0u, 0u, 0u, 0u};
        B0 = mfma(up ? zz : kvf, pq0, zero16());
        B0 = mfma(up ? kvf : zz, pq1, B0);
        const float d0 = dot2(pq0.y, z0.y, dot2(pq0.x, z0.x, 0.f)) + dot2(pq0.w, z1.y, dot2(pq0.z, z1.x, 0.f));
        const float d1 = dot2(pq1.y, z2.y, dot2(pq1.x, z2.x, 0.f)) + dot2(pq1.w, z3.y, dot2(pq1.z, z3.x, 0.f));
        const float r0 = __builtin_amdgcn_rcpf(red_h(d0) + 1e-6f), r1 = __builtin_amdgcn_rcpf(red_h(d1) + 1e-6f);
        v4u o0, o1;
#pragma unroll
        for (int i = 0; i < 4; ++i) { o0[i] = pack2(B0[2 * i] * r0, B0[2 * i + 1] * r0); o1[i] = pack2(B0[8 + 2 * i] * r1, B0[9 + 2 * i] * r1); }
        st_ws(c, o0, lv, ob);
        st_ws(c, o1, lv + KB, ob);
        st_chk<false>(c, sum4(sum4(0u, o0), o1), 0u, ob, tagc);
      }
      const int rtA = opq(mh * 2 + rh), clb = opq(cl * ACT_CL);
      auto res_send = [&](f32x16& A1, int q, unsigned xseq) INL {
        LAS char* xs = L + L_XCH + w * 4096 + lv;
        *(LAS f32x4*)(xs + q * 2048) = f32x4{A1[8 * q], A1[8 * q + 1], A1[8 * q + 2], A1[8 * q + 3]};
        *(LAS f32x4*)(xs + q * 2048 + 1024) = f32x4{A1[8 * q + 4], A1[8 * q + 5], A1[8 * q + 6], A1[8 * q + 7]};
        MEMBAR();
        if (q) *flg(c, F_XF + w) = xseq;
      };
      auto res_epilogue = [&](f32x16& A0, int xi, bool final_out, unsigned xseq) INL {
        LAS char* xs = L + L_XCH;
        const int lv = fresh_lane() * 16;
        f32x4 o0, o1, o2, o3;
#pragma nounroll
        for (unsigned sp = 0; sp < SPIN_CAP; ++sp) {
          const unsigned fl = *flg(c, F_XF + (w ^ 1));
          MEMBAR();
          o0 = *(LAS f32x4*)(xs + (w ^ 1) * 4096 + lv); o1 = *(LAS f32x4*)(xs + (w ^ 1) * 4096 + 1024 + lv);
          o2 = *(LAS f32x4*)(xs + (w ^ 1) * 4096 + 2048 + lv); o3 = *(LAS f32x4*)(xs + (w ^ 1) * 4096 + 3072 + lv);
          MEMBAR();
          if ((unsigned)__builtin_amdgcn_readfirstlane(fl) >= xseq) break;
        }
        xr0 = v4u{pack2(A0[0] + o0[0], A0[1] + o0[1]), pack2(A0[2] + o0[2], A0[3] + o0[3]), pack2(A0[4] + o1[0], A0[5] + o1[1]), pack2(A0[6] + o1[2], A0[7] + o1[3])};
        xr1 = v4u{pack2(A0[8] + o2[0], A0[9] + o2[1]), pack2(A0[10] + o2[2], A0[11] + o2[3]), pack2(A0[12] + o3[0], A0[13] + o3[1]), pack2(A0[14] + o3[2], A0[15] + o3[3])};
        if (final_out) {
          const int lo = fresh_lane();
          GAS char* ob = (GAS char*)out + (size_t)((cl * 4 + rtA) * 32 + (lo & 31)) * 1024 + cg * 128 + kh * 64 + (lo >> 5) * 16;
          *(GAS v4u*)ob = xr0;
          *(GAS v4u*)(ob + 32) = xr1;
        } else {
          const int xb = WS_X + clb + ((xi * 4 + rtA) * 16 + cg * 2 + kh) * REC;
          st_ws(c, xr0, lv, xb);
          st_ws(c, xr1, lv + KB, xb);
          const float s = csum(xr0, 0.f) + csum(xr1, 0.f);
          float q0 = 0.f, q1 = 0.f;
#pragma unroll
          for (int i = 0; i < 4; ++i) { q0 = dot2(xr0[i], xr0[i], q0); q1 = dot2(xr1[i], xr1[i], q1); }
          const auto sw = __builtin_amdgcn_permlane32_swap(f2u(s), f2u(q0 + q1), false, false);
          st_chk<true>(c, sum4(sum4(0u, xr0), xr1), f2u(u2f((unsigned)sw[0]) + u2f((unsigned)sw[1])), xb, tagc);
        }
      };
      unsigned m0, m1;
      {
        const int ab = WS_O + clb + (rtA * 16 + kh) * REC;
        wpre<1>(c, kb0, kb1);
        if (mh) __builtin_amdgcn_s_sleep(YOFF);
        open_phase<2, false>(c, a, k, lv, ab, tagc);
        gemm<32, 2, 1, -1, false, 2, 8, 0, 99, 1>(c, a, A0, A1, kb0, kb1, 0, 0, [&](auto F) INL {
          if (F.v == 24 && layer == 0) wait_lds<2, 0>(c, F_CONV + 2, 1u);
          if constexpr (F.v == 29 || F.v == 30) res_send(A1, F.v - 29, (unsigned)(layer * 0x200 + 1));
          req_step<F.v, 4, 8, 2, false>(c, a, k, lv, ab);
          check_step<F.v, 8, 2, false>(c, m0, m1, a, k, lv, ab, tagc);
        });
        res_epilogue(A0, layer, false, (unsigned)(layer * 0x200 + 1));
      }
      *flg(c, F_XF + w) = (unsigned)(layer * 0x200 + 0x100);
      *(LAS v4u*)(L + L_KVS + w * 2304 + lv) = xr0;
      *(LAS v4u*)(L + L_KVS + w * 2304 + 1024 + lv) = xr1;
      MEMBAR();
      auto res_back = [&]() INL { xr0 = *(LAS v4u*)(L + L_KVS + w * 2304 + lv); xr1 = *(LAS v4u*)(L + L_KVS + w * 2304 + 1024 + lv); };
      wpre<0>(c, lv, lv);
      v4u cs0;
      float sacc;
      const int ab4 = WS_H + clb + (rtA * 43 + kh) * REC;
      const int hb = WS_H + clb + w * 43 * REC;
      float r2;
      v4u ho;
      unsigned hs;
      float h0, h1;
      auto hside = [&](auto F, f32x16& G, f32x16& U, int dst) INL {
        constexpr int f = F.v, sl = (f - 1) / 2;
        if constexpr (f >= 1 && f < 17) {
          if constexpr (f & 1) { h0 = relu(G[2 * sl]) * r2; PIN(h0); h1 = relu(G[2 * sl + 1]) * r2; PIN(h1); }
          else {
            h0 *= U[2 * sl]; PIN(h0); h1 *= U[2 * sl + 1]; PIN(h1);
            ho[sl & 3] = pack2(h0, h1); PIN(ho);
          }
        }
        if constexpr (f == 9 || f == 17) { st_ws(c, ho, lv + (f == 17) * KB, dst); hs = sum4(f == 9 ? 0u : hs, ho); PIN(hs); }
        if constexpr (f == 18) st_chk<false>(c, hs, 0u, dst, tagc);
      };
      auto corr = [&](auto F, int at, f32x16& G, f32x16& U, int gi) INL {
        if (F.v == at - 6) cs0 = cs_frag(3 + 2 * gi);
        if (F.v == at - 3) { G = mfma(cs0, mfrag, G); SB(); cs0 = cs_frag(4 + 2 * gi); }
        if (F.v == at) U = mfma(cs0, mfrag, U);
      };
      const int xb3 = WS_X + clb + (layer * 4 + w) * 16 * REC;
      open_phase<1, true>(c, a, k, lv, xb3, tagc);
      gemm4<128, 2, 0, 0, false, 3, 62, 0, 99, -1>(c, a, A0, A1, B0, B1, lv, lv, lv, lv, [&](auto F) INL {
        constexpr int f = F.v, u = f / 8;
        if constexpr (f == 0) { sacc = u2f(k[0].y) + u2f(k[1].y); PIN(sacc); }
        req_pace<f, 6, 4, 16, 1, true>(c, a, k, lv, xb3);
        sfor<8>([&](auto CH) INL {
          if constexpr (CH.v > 0 && dual_first(CH.v) == u + 1) check_step<8 * (CH.v - 1) + f % 8, 16, 1, true>(c, m0, m1, a, k, lv, xb3, tagc);
          if constexpr (CH.v > 0 && f == 8 * dual_first(CH.v)) { sacc += u2f(k[2 * CH.v].y) + u2f(k[2 * CH.v + 1].y); PIN(sacc); }
        });
        if constexpr (f == 89) {
          const auto sw = __builtin_amdgcn_permlane32_swap(f2u(sacc), f2u(sacc), false, false);
          r2 = __builtin_amdgcn_rcpf(set_stats(u2f((unsigned)sw[0]), u2f((unsigned)sw[1])));
          PIN(mfrag); PIN(r2);
        }
        if (f == 86 && layer == 0) wait_lds<1, 0>(c, F_CS3R, 1u);
        corr(F, 95, A0, A1, 0);
        if constexpr (f >= 96) hside(IC<f - 96>{}, A0, A1, hb + m * REC);
        corr(F, 123, B0, B1, 1);
      });
      const int tb0 = fresh_lane() * 16 + kh * 4096;
      if (ng == 3) {
        gemm<64, 2, 0, 4, false, 3, 0, 0, 99>(c, a, A0, A1, lv, lv, tb0, tb0, [&](auto F) INL {
          constexpr int f = F.v;
          hside(F, B0, B1, hb + (16 + m) * REC);
          corr(F, 47, A0, A1, 2);
          if constexpr (f == 19) res_back();
          if constexpr (f >= 23 && f < 31) { res_init(B0, f - 23); PIN(B0); }
          req_pace<f - 32, 4, 0, 8, 2, false>(c, a, k, lv, ab4);
          if constexpr (f >= 56) check_step<f - 64, 22, 2, false>(c, m0, m1, a, k, lv, ab4, tagc);
        });
      }
      if (opq(ng) == 2) {
        const unsigned tg0 = (unsigned)__builtin_amdgcn_s_memrealtime();
        sfor<20>([&](auto F) INL { hside(F, B0, B1, hb + (16 + m) * REC); });
        res_back();
#pragma unroll
        for (int i = 0; i < 8; ++i) res_init(B0, i);
        while ((unsigned)__builtin_amdgcn_s_memrealtime() - tg0 < (unsigned)NG2S) __builtin_amdgcn_s_sleep(2);
        sfor<16>([&](auto F) INL { req_step<F.v, 0, 8, 2, false>(c, a, k, lv, ab4); SB(); });
        wpre<4>(c, tb0, tb0);
        settle<2, 2, false>(c, rec_bad<false>(a, k, 0, tagc) | rec_bad<false>(a, k, 1, tagc), a, k, lv, ab4, 0, tagc);
        while ((unsigned)__builtin_amdgcn_s_memrealtime() - tg0 < (unsigned)NG2T) __builtin_amdgcn_s_sleep(2);
      }
      {
        const int h2 = ng == 3 ? hb + (32 + g0 - 2 * m) * REC : WS_END;
        const int ab21 = kh ? WS_END : ab4;
        const unsigned t21 = kh ? 0u : tagc;
        auto side4 = [&](auto F) INL {
          constexpr int f = F.v;
          if constexpr (f < 10) { hside(IC<2 * f>{}, A0, A1, h2); hside(IC<2 * f + 1>{}, A0, A1, h2); }
          req_pace<f, 4, 8, 16, 2, false>(c, a, k, lv, ab4);
          req_pace<f - S2H, 3, 16, 21, 2, false>(c, a, k, lv, ab4);
          req_pace<f - S2H - 15, 3, 21, 22, 2, false>(c, a, k, lv, ab21);
          if constexpr (f < 72) check_step<f, 22, 2, false>(c, m0, m1, a, k, lv, ab4, tagc);
          if constexpr (f == 74) settle<1, 2, false>(c, rec_bad<false>(a, k, 20, tagc), a, k, lv, ab4, 20, tagc);
          if constexpr (f == 78) settle<1, 2, false>(c, rec_bad<false>(a, k, 21, t21), a, k, lv, ab21, 21, t21);
          if constexpr (f == 85 || f == 86) res_send(B1, f - 85, (unsigned)(layer * 0x200 + 0x101));
        };
        gemm<88, 2, 4, -1, false, 2, 32, 3, 7, 1>(c, a, B0, B1, tb0, tb0, 0, 0, side4);
        tagc += 0x632BE5AAu;
        res_epilogue(B0, 0, layer == 1, (unsigned)(layer * 0x200 + 0x101));
      }
      if (layer == 1) break;
      wpre<0>(c, lv, lv);
      const int xb1 = WS_X + clb + w * 16 * REC;
      open_phase<1, true>(c, a, k, lv, xb1, tagc);
      gemm<64, 2, 0, -1, true, 3, 34, 0, 99>(c, a, A0, A1, lv, lv, 0, 0, [&](auto F) INL {
        constexpr int f = F.v;
        if constexpr (f == 0) { sacc = u2f(k[0].y) + u2f(k[1].y); PIN(sacc); }
        req_step<f, 4, 10, 1, true>(c, a, k, lv, xb1);
        req_pace<f - 12, 4, 10, 16, 1, true>(c, a, k, lv, xb1);
        check_step<f, 16, 1, true>(c, m0, m1, a, k, lv, xb1, tagc);
        if constexpr ((f & 7) == 0 && f >= 8 && f <= 56) { sacc += u2f(k[f / 4].y) + u2f(k[f / 4 + 1].y); PIN(sacc); }
        if constexpr (f == 57) {
          const auto sw = __builtin_amdgcn_permlane32_swap(f2u(sacc), f2u(sacc), false, false);
          rstd = __builtin_amdgcn_rsqf(set_stats(u2f((unsigned)sw[0]), u2f((unsigned)sw[1])));
          PIN(mfrag);
          rs_put();
        }
        if (f == 56) cs0 = cs_frag(0);
        if (f == 60) { A0 = mfma(mfrag, cs0, A0); SB(); cs0 = cs_frag(1); }
        if (f == 63) A1 = mfma(mfrag, cs0, A1);
        if (f >= 59 && f < 63) rs_get(f - 59);
      });
      tagc += 0x632BE5AAu;
    }
  } else if (wave < 6) {
    const int lw = wave - 4;
    LANEVARS
    c.lane = lane;
    volatile LAS unsigned* full = flg(c, F_FULL + lw);
    __builtin_amdgcn_s_setprio(3);
    load_half0(0);
    if (lw == 1) glds((const char*)(pc0 & ~(u64)255) + j * (CODEPF / 32), min(lane * 32, CODEPF / 32 - 32), L_RING + 7 * SLOT + 12288);
    __builtin_amdgcn_s_setprio(2);
    load_half0(1);
    VMWAIT(8); *full = 1u;
    load_half0(2);
    load_half0(3);
    VMWAIT(16); *full = 2u;
    {
      const unsigned tv = min(lane, 42) * 128, td = L_RING + (6 + lw) * SLOT + (lw ? 13312 : 3072);
      glds((const char*)(lw ? wu : wg) + b * 5504, tv, td);
      if (lw) glds((const char*)wd + b * 5504, tv, td);
      else glds((const char*)wo + b * 2048, (lane & 15) * 128, td);
      *flg(c, F_QGO + lw) = 1u;
    }
    VMWAIT(10); *full = 3u;
    VMWAIT(2); *full = 4u;
    __builtin_amdgcn_s_setprio(1);
    wait_lds<4, 1>(c, F_FREE, 1u);
    load_half0(4);
    load_half0(5);
    VMWAIT(8); *full = 5u;
    VMWAIT(0); *full = 6u;
    int seq = 6, pub = 6;
    v4u wc[48];
    auto stream = [&](int seg, int i0, int i1) INL {
      const int ln = fresh_lane(), lv = ln * 16;
      const unsigned vo2 = (ln >> 3) * 1024 + (((ln & 7) ^ (((ln >> 4) & 1) << 2)) * 16);
      const int src = (seg == 0 ? WS_WT1 + m * 96 * KB : WS_WT3 + g0 * 64 * KB);
#pragma unroll 1
      for (int i = i0; i < i1; ++i) {
        if (seq >= NSLOT) {
          const unsigned need = (unsigned)(seq - NSLOT + 1);
          bool first = true;
#pragma nounroll
          for (unsigned sp = 0; sp < SPIN_CAP; ++sp) {
            v4u f = *(volatile LAS v4u*)flg(c, F_FREE);
            if ((unsigned)__builtin_amdgcn_readfirstlane(umin(umin(f.x, f.y), umin(f.z, f.w))) >= need) break;
            if (first && pub < seq) { VMWAIT(0); pub = seq; *full = (unsigned)pub; }
            first = false;
            __builtin_amdgcn_s_sleep(4);
          }
          MEMBAR();
        }
        const unsigned dst = L_RING + (seq & (NSLOT - 1)) * SLOT + lw * 8192;
        const int hs = i * 2 + lw, hr = (seg == 2 && hs < 16) ? (int)((DUAL >> (4 * hs)) & 15) : hs;
        if (seg == 1) {
          const char* sp = (const char*)wo + (size_t)hr * 65536 + cg * 128;
#pragma unroll
          for (int e = 0; e < 8; ++e) glds(sp + e * 8192, vo2, dst + e * 1024);
        } else {
          glds4(ws + src + hr * 8192, lv, dst);
          glds4(ws + src + hr * 8192 + 4096, lv, dst + 4096);
        }
        ++seq;
        if (seq - pub > 2) { VMWAIT(16); pub = seq - 2; *full = (unsigned)pub; }
      }
    };
    auto drain = [&]() INL { VMWAIT(0); if (pub < seq) { pub = seq; *full = (unsigned)seq; } };
    cache_load(wc, IC<6>{});
    wait_lds<2, 1>(c, F_JOB, 1u);
    VMWAIT(32);
    stream(1, 0, 4);
    drain();
    if (lw == 0) { poll_tags<32>(c, WS_F1 + m * 2048, *(volatile LAS v2u*)flg(c, F_TAG)); *flg(c, F_PTOK) = 1u; }
    else wait_lds<1, 1>(c, F_PTOK, 1u);
    stream(2, 0, 8);
    drain();
    wait_lds<4, 1>(c, F_XF, 0x100u);
    cache_conv(wc, IC<6>{});
    stream(2, 8, 4 * ng);
    drain();
    cache_put(wc, IC<6>{}, seq);
    seq += 11; pub = seq;
    stream(0, 0, 6);
    stream(1, 0, 4);
    stream(2, 0, 4 * ng);
    drain();
    cache_put(wc, IC<6>{}, seq);
  } else {
    const int hh = wave - 6;
    LANEVARS
    c.lane = lane;
    const int pts = L_RING + (6 + hh) * SLOT;
    __builtin_amdgcn_s_setprio(3);
    {
      const unsigned vo1 = (lane >> 2) * 3072 + (lane & 3) * 16;
      const char* j0 = (const char*)wqkv + (size_t)(cl * 32) * 3072 + m * 64;
      glds(j0 + (1 + hh) * 1024, vo1, pts); glds(j0 + (1 + hh) * 1024 + 16 * 3072, vo1, pts + 1024);
      glds(j0 + hh * 16 * 3072, vo1, pts + 2048);
    }
    const int srow = cl * 128 + m * 8 + hh * 4 + (lane >> 4);
    v4u xs[4];
#pragma unroll
    for (int i = 0; i < 4; ++i) xs[i] = *(const GAS v4u*)((const GAS char*)x + (size_t)srow * 1024 + i * 256 + (lane & 15) * 16);
    const v2u e0 = __builtin_amdgcn_raw_buffer_load_b64(c.rs, 0, WS_E, 16);
    VMWAIT(0);
    const u64 tag = mix64(((u64)(unsigned)__builtin_amdgcn_readfirstlane(e0.y) << 32) | (unsigned)__builtin_amdgcn_readfirstlane(e0.x));
    const v2u tag2 = {(unsigned)tag, (unsigned)(tag >> 32)};
    if (hh == 0) *(volatile LAS v2u*)flg(c, F_TAG) = tag2;
    {
      const unsigned tb = ((unsigned)(mix64(tag) >> 32) >> ((lane & 3) * 8)) & 0xffu;
      const v4u f0 = tr_frag(L + pts + (hh ? trS : trN)), f1 = tr_frag(L + pts + 1024 + (hh ? trS : trN)), f2 = tr_frag(L + pts + 2048 + trN);
      const float s0 = red_h(csum(f1, csum(f0, 0.f))), s2 = red_h(csum(f2, 0.f));
      __builtin_amdgcn_raw_buffer_store_b32((f2u(h ? s2 : s0) & 0xffffff00u) | tb, c.rs, l31 * 4 + h * 4096, WS_C1P + m * 8 * KB + cl * 256 + hh * 128, 16);
      float sa = 0.f, sb = 0.f, qa = 0.f, qb = 0.f;
#pragma unroll
      for (int i = 0; i < 4; ++i) {
        sa = dot2(xs[i].y, ONES2, dot2(xs[i].x, ONES2, sa)); sb = dot2(xs[i].w, ONES2, dot2(xs[i].z, ONES2, sb));
        qa = dot2(xs[i].y, xs[i].y, dot2(xs[i].x, xs[i].x, qa)); qb = dot2(xs[i].w, xs[i].w, dot2(xs[i].z, xs[i].z, qb));
      }
      sa += sb; qa += qb;
      sa += dpp<0x128>(sa); qa += dpp<0x128>(qa); sa += dpp<0x124>(sa); qa += dpp<0x124>(qa);
      sa += dpp<0x122>(sa); qa += dpp<0x122>(qa); sa += dpp<0x121>(sa); qa += dpp<0x121>(qa);
      if ((lane & 15) == 0) st_ws(c, v4u{f2u(sa), tag2.x, f2u(qa), tag2.y}, srow * 16, WS_S0P);
      {
        const unsigned nt = ~(hh ? tag2.y : tag2.x);
        const v4u W = {nt, nt >> 8, nt >> 16, nt >> 24};
        const int kvb = WS_KVP + ((hh * 16 + m) * 16 + cl) * KVR;
        st_ws(c, W, lv, kvb); st_ws(c, W, lv + KB, kvb);
        if (lane < 8) st_ws(c, W, lv + 2 * KB, kvb);
      }
      VMWAIT(0);
      __builtin_amdgcn_s_sleep(8);
      {
        float t;
#pragma nounroll
        for (unsigned sp = 0; sp < SPIN_CAP; ++sp) {
          unsigned p[16];
#pragma unroll
          for (int i = 0; i < 16; ++i) p[i] = __builtin_amdgcn_raw_buffer_load_b32(c.rs, lane * 4 + i * 256, WS_C1P + m * 8 * KB + hh * 4096, 16);
          SB();
          unsigned chk = 0;
          t = 0.f;
#pragma unroll
          for (int i = 0; i < 16; ++i) { p[i] ^= tb; chk |= p[i]; t += u2f(p[i]); }
          if (!__any((chk & 0xffu) != 0u)) break;
          MEMBAR();
        }
        if (hh) t = red_h(t);
        if (hh == 0 || h == 0) *(LAS v4u*)(L + L_CS + hh * 1024 + lv) = cs_operand(t);
        MEMBAR();
        *flg(c, F_CS1R + hh) = 1u;
      }
      __builtin_amdgcn_s_setprio(1);
      wait_lds<2, 1>(c, F_QGO, 1u);
      const int pt3 = pts + 4096;
      const unsigned vo3 = (lane >> 2) * 2752 + (lane & 3) * 16;
      const char* w3 = (const char*)(hh ? wu : wg) + (size_t)(cl * 32) * 2752;
#pragma unroll
      for (int i = 0; i < 3; ++i) {
        const int aa = g0 + min(i, ng - 1);
        glds(w3 + aa * 64, vo3, pt3 + i * 2048);
        glds(w3 + aa * 64 + 16 * 2752, vo3, pt3 + i * 2048 + 1024);
      }
      st_ws(c, f0, lv, WS_WT1 + m * 96 * KB + (cl * 4 + hh) * KB);
      st_ws(c, f1, lv, WS_WT1 + m * 96 * KB + (cl * 4 + 2 + hh) * KB);
      st_ws(c, f2, lv, WS_WT1 + m * 96 * KB + (64 + cl * 2 + hh) * KB);
      VMWAIT(0);
#pragma unroll
      for (int i = 0; i < 3; ++i) {
        const int aa = g0 + min(i, ng - 1);
        const v4u g0f = tr_frag(L + pt3 + i * 2048 + trS), g1f = tr_frag(L + pt3 + i * 2048 + 1024 + trS);
        st_ws(c, g0f, lv, WS_WT3 + aa * 64 * KB + (cl * 4 + hh) * KB);
        st_ws(c, g1f, lv, WS_WT3 + aa * 64 * KB + (cl * 4 + 2 + hh) * KB);
        const float s = red_h(csum(g1f, csum(g0f, 0.f)));
        if (h == 0) __builtin_amdgcn_raw_buffer_store_b32(f2u(s), c.rs, l31 * 4, WS_CSP + aa * 4 * KB + cl * 256 + hh * 128, 16);
      }
      MEMBAR();
      *flg(c, F_JOB + hh) = 1u;
    }
    VMWAIT(0);
    if (lane == 0) __builtin_amdgcn_raw_buffer_store_b64(tag2, c.rs, 0, WS_F1 + ((m * 16 + cl) * 2 + hh) * 64, 16);
    v4u wc[40];
    cache_load(wc, IC<5>{});
    cache_conv(wc, IC<5>{});
    if (hh == 1) {
      wait_lds<1, 2>(c, F_PTOK, 1u);
#pragma unroll 1
      for (int i = 0; i < ng; ++i) {
        float t = 0.f;
#pragma unroll
        for (int q = 0; q < 16; ++q) t += u2f(__builtin_amdgcn_raw_buffer_load_b32(c.rs, lane * 4 + q * 256, WS_CSP + (g0 + i) * 4 * KB, 16));
        *(LAS v4u*)(L + L_CS + (3 + 2 * i) * 512 + lv) = cs_operand(t);
      }
      MEMBAR();
      *flg(c, F_CS3R) = 1u;
      MEMBAR();
    }
    __builtin_amdgcn_s_setprio(1);
    cache_put(wc, IC<5>{}, 10 + 4 * ng);
    if (b == 0 && wave == 6 && lane == 0) __hip_atomic_store((GAS u64*)(ws + WS_E), tag, RLX_AGENT);
    __builtin_amdgcn_s_setprio(1);
    cache_put(wc, IC<5>{}, 31 + 8 * ng);
  }
}

extern "C" void kernel_launch(void* const* d_in, const int* in_sizes, int n_in, void* d_out, int out_size, void* d_ws, size_t ws_size,
                              hipStream_t stream) {
  fused_fwd<<<256, 512, 0, stream>>>((const u16*)d_in[0], (const u16*)d_in[1], (const u16*)d_in[2], (const u16*)d_in[4], (const u16*)d_in[5],
                                     (const u16*)d_in[6], (u16*)d_out, (char*)d_ws);
}
```

```cpp
#include <hip/hip_runtime.h>
#include <stdint.h>
#include <utility>

#define GAS __attribute__((address_space(1)))
#define LAS __attribute__((address_space(3)))
#define RLX_AGENT __ATOMIC_RELAXED, __HIP_MEMORY_SCOPE_AGENT
typedef unsigned v4u __attribute__((ext_vector_type(4)));
typedef unsigned v2u __attribute__((ext_vector_type(2)));
typedef float f32x2 __attribute__((ext_vector_type(2)));
typedef float f32x4 __attribute__((ext_vector_type(4)));
typedef float f32x16 __attribute__((ext_vector_type(16)));
typedef short s16x8 __attribute__((ext_vector_type(8)));
typedef short s16x4 __attribute__((ext_vector_type(4)));
typedef __bf16 bf16x2 __attribute__((ext_vector_type(2)));
typedef unsigned short u16;
typedef unsigned long long u64;

constexpr int XT0 = 20, XT1 = 22, XT2 = 20;
constexpr int YOFF = 6;
constexpr int KVDELAY = 12;
constexpr int KVDP = 8;
constexpr int BACKOFF = 4;
constexpr int NG2S = 90;
constexpr int NG2T = 150;
constexpr int S2H = 44;
constexpr int XPRE = 5;
constexpr int CODEPF = 59392;

constexpr int KB = 1024;
constexpr int WS_E = 0;
constexpr int WS_F1 = 4 * KB;
constexpr int WS_WT1 = 128 * KB;
constexpr int WS_WT3 = WS_WT1 + 16 * 96 * KB;
constexpr int WS_CSP = WS_WT3 + 43 * 64 * KB;
constexpr int WS_S0P = WS_CSP + 43 * 4 * KB;
constexpr int WS_C1P = WS_S0P + 64 * KB;
constexpr int KVR = 2176;
constexpr int WS_KVP = WS_C1P + 16 * 8 * KB;
constexpr int REC = 2560;
constexpr int WS_ACT = 6 * KB * KB, ACT_CL = KB * KB;
constexpr int WS_O = WS_ACT;
constexpr int WS_X = WS_O + 4 * 16 * REC;
constexpr int WS_H = WS_X + 2 * 4 * 16 * REC;
static_assert(WS_KVP + 512 * KVR <= WS_ACT && WS_H + 4 * 44 * REC <= WS_ACT + ACT_CL, "ws");
constexpr int WS_END = WS_ACT + 16 * ACT_CL;

constexpr int SLOT = 16384, NSLOT = 8;
constexpr int L_FLG = 0;
constexpr int L_ZF = L_FLG + 128;
constexpr int L_CS = L_ZF + 64;
constexpr int L_KVF = L_CS + 9 * 512;
constexpr int L_KVS = L_KVF + 1024 + 1152;
constexpr int L_XCH = 16384;
constexpr int L_RING = 32768;
constexpr int L_END = L_RING + NSLOT * SLOT;
static_assert(L_KVS + 4 * 2304 <= L_XCH && L_END == 163840 && (L_KVS & 15) == 0 && (L_CS & 15) == 0, "lds");
enum { F_FULL = 0, F_TAG = 2, F_FREE = 4, F_JOB = 8, F_KFL = 12, F_QGO = 10, F_CONV = 24, F_CS3R = 31, F_PTOK = 30, F_KVR = 16, F_XF = 20, F_CS1R = 28 };
constexpr unsigned SPIN_CAP = 1u << 17;

template <int V> struct IC { static constexpr int v = V; __device__ constexpr operator int() const { return V; } };
template <class F, int... I> __device__ __forceinline__ void sfor_(F&& fn, std::integer_sequence<int, I...>) { (fn(IC<I>{}), ...); }
template <int N, class F> __device__ __forceinline__ void sfor(F&& fn) { sfor_(fn, std::make_integer_sequence<int, N>{}); }
#define INL __attribute__((always_inline))
#define MEMBAR() asm volatile("" ::: "memory")
#define SB() __builtin_amdgcn_sched_barrier(0)
#define VMWAIT(n) asm volatile("s_waitcnt vmcnt(" #n ")" ::: "memory")
#define PIN(x) asm volatile("" : "+v"(x))

__device__ __forceinline__ unsigned umin(unsigned a, unsigned b) { return a < b ? a : b; }
__device__ __forceinline__ int gsplit(int i) { return (43 * i) >> 4; }
__device__ __forceinline__ f32x16 mfma(v4u a, v4u b, f32x16 c) {
  return __builtin_amdgcn_mfma_f32_32x32x16_bf16(__builtin_bit_cast(s16x8, a), __builtin_bit_cast(s16x8, b), c, 0, 0, 0);
}
__device__ __forceinline__ f32x16 zero16() { return f32x16{0.f, 0.f, 0.f, 0.f, 0.f, 0.f, 0.f, 0.f, 0.f, 0.f, 0.f, 0.f, 0.f, 0.f, 0.f, 0.f}; }
__device__ __forceinline__ unsigned pack2(float a, float b) {
  bf16x2 v = {(__bf16)a, (__bf16)b};
  return __builtin_bit_cast(unsigned, v);
}
__device__ __forceinline__ float u2f(unsigned u) { return __builtin_bit_cast(float, u); }
__device__ __forceinline__ unsigned f2u(float f) { return __builtin_bit_cast(unsigned, f); }
__device__ __forceinline__ float blo(unsigned u) { return u2f(u << 16); }
__device__ __forceinline__ float bhi(unsigned u) { return u2f(u & 0xffff0000u); }
__device__ __forceinline__ float relu(float x) { return u2f((unsigned)max((int)f2u(x), 0)); }
__device__ __forceinline__ float dot2(unsigned a, unsigned b, float acc) {
  return __builtin_amdgcn_fdot2_f32_bf16(__builtin_bit_cast(bf16x2, a), __builtin_bit_cast(bf16x2, b), acc, false);
}
constexpr unsigned ONES2 = 0x3f803f80u;
constexpr float LOG2E = 1.44269504f;
__device__ __forceinline__ float red_h(float v) {
  auto s = __builtin_amdgcn_permlane32_swap(f2u(v), f2u(v), false, false);
  return u2f((unsigned)s[0]) + u2f((unsigned)s[1]);
}
__device__ __forceinline__ float red_g(float v) {
  auto r = __builtin_amdgcn_permlane16_swap(f2u(v), f2u(v), false, false);
  return red_h(u2f((unsigned)r[0]) + u2f((unsigned)r[1]));
}
template <int CTRL> __device__ __forceinline__ float dpp(float v) { return u2f(__builtin_amdgcn_update_dpp(0, (int)f2u(v), CTRL, 0xf, 0xf, false)); }
__device__ __forceinline__ float exp2c(float x) { return __builtin_amdgcn_fmed3f(__builtin_amdgcn_exp2f(x), 0.f, 1.f); }
__device__ __forceinline__ void split2(float v, unsigned& hi, unsigned& lo) {
  hi = pack2(v, 0.f) & 0xffffu;
  lo = pack2(v - blo(hi), 0.f) & 0xffffu;
}
template <int D = 256> __device__ __forceinline__ v4u tr_frag(LAS char* p) {
  v2u lo = __builtin_bit_cast(v2u, __builtin_amdgcn_ds_read_tr16_b64_v4i16((LAS s16x4*)p));
  v2u hi = __builtin_bit_cast(v2u, __builtin_amdgcn_ds_read_tr16_b64_v4i16((LAS s16x4*)(p + D)));
  return v4u{lo.x, lo.y, hi.x, hi.y};
}
__device__ __forceinline__ void glds(const void* sbase, unsigned voff, unsigned ldst) {
  asm volatile("s_mov_b32 m0, %2\n\ts_nop 0\n\tglobal_load_lds_dwordx4 %0, %1" ::"v"(voff), "s"(sbase), "s"(ldst) : "memory");
}
__device__ __forceinline__ void glds4(const void* sbase, unsigned voff, unsigned ldst) {
  asm volatile(
      "s_mov_b32 m0, %2\n\ts_nop 0\n\tglobal_load_lds_dwordx4 %0, %1 sc1\n\tglobal_load_lds_dwordx4 %0, %1 offset:1024 sc1\n\t"
      "global_load_lds_dwordx4 %0, %1 offset:2048 sc1\n\tglobal_load_lds_dwordx4 %0, %1 offset:3072 sc1" ::"v"(voff), "s"(sbase), "s"(ldst)
      : "memory");
}

struct Ctx {
  LAS char* L;
  __amdgpu_buffer_rsrc_t rs;
  int lane, wave;
  int seq, known;
  int vb0, vb1, nb0, nb1;
  int kv;
  int fa;
  v2u fpre;
  v4u w0, w1, w2, w3;
  template <int I> __device__ __forceinline__ v4u& wf() { if constexpr (I == 0) return w0; else if constexpr (I == 1) return w1; else if constexpr (I == 2) return w2; else return w3; }
};
__device__ __forceinline__ volatile LAS unsigned* flg(Ctx& c, int i) { return (volatile LAS unsigned*)(c.L + L_FLG) + i; }
template <int N, int SLP> __device__ __forceinline__ void wait_lds(Ctx& c, int i, unsigned val) {
#pragma nounroll
  for (unsigned sp = 0; sp < SPIN_CAP; ++sp) {
    unsigned v;
    if (N == 1) v = *flg(c, i);
    else if (N == 2) { v2u f = *(volatile LAS v2u*)flg(c, i); v = umin(f.x, f.y); }
    else { v4u f = *(volatile LAS v4u*)flg(c, i); v = umin(umin(f.x, f.y), umin(f.z, f.w)); }
    if ((unsigned)__builtin_amdgcn_readfirstlane(v) >= val) break;
    if (SLP) __builtin_amdgcn_s_sleep(SLP);
  }
  MEMBAR();
}
template <int N> __device__ __forceinline__ void poll_tags(Ctx& c, int base, v2u tag) {
#pragma nounroll
  for (unsigned sp = 0; sp < SPIN_CAP; ++sp) {
    const v2u v = __builtin_amdgcn_raw_buffer_load_b64(c.rs, (c.lane & (N - 1)) * 128, base, 16);
    if (__all(v.x == tag.x && v.y == tag.y)) break;
    __builtin_amdgcn_s_sleep(4);
    MEMBAR();
  }
  MEMBAR();
}
__device__ __forceinline__ v4u ld_ws(Ctx& c, int voff, int soff) { return __builtin_amdgcn_raw_buffer_load_b128(c.rs, voff, soff, 16); }
__device__ __forceinline__ void st_ws(Ctx& c, v4u v, int voff, int soff) {
  __builtin_amdgcn_raw_buffer_store_b128(v, c.rs, voff, soff, 16);
  asm volatile("s_nop 1" ::"v"(v) : "memory");
}
__device__ __forceinline__ int fresh_lane() { int l; asm volatile("v_mbcnt_lo_u32_b32 %0, -1, 0\n\tv_mbcnt_hi_u32_b32 %0, -1, %0" : "=v"(l)); __builtin_assume(l >= 0 && l < 64); return l; }
__device__ __forceinline__ int opq(int v) { asm volatile("" : "+s"(v)); return v; }
__device__ __forceinline__ int opqv(int v) { asm volatile("" : "+v"(v)); return v; }

constexpr int fps(int mode) { return (mode == 1 || mode == 4) ? 8 : 16; }
constexpr bool two(int mode) { return mode == 1 || mode == 2; }
__device__ __forceinline__ void slot_wait(Ctx& c, int sq) {
  if (__builtin_expect(c.known <= sq, 0)) {
#pragma nounroll
    for (unsigned sp = 0; sp < SPIN_CAP; ++sp) {
      v2u f = *(volatile LAS v2u*)flg(c, F_FULL);
      c.known = __builtin_amdgcn_readfirstlane(umin(f.x, f.y));
      if (c.known > sq) break;
    }
  }
  MEMBAR();
}
template <int MODE> __device__ __forceinline__ void slot_base(Ctx& c, int sq, int lb0, int lb1) {
  const int sb = (sq & (NSLOT - 1)) * SLOT;
  c.nb0 = lb0 + sb; PIN(c.nb0);
  if (two(MODE)) { c.nb1 = lb1 + sb; PIN(c.nb1); }
}
template <int MODE> __device__ __forceinline__ void slot_next(Ctx& c) {
  c.nb0 = (c.vb0 + SLOT) & (NSLOT * SLOT - 1); PIN(c.nb0);
  if (two(MODE)) { c.nb1 = (c.vb1 + SLOT) & (NSLOT * SLOT - 1); PIN(c.nb1); }
}
template <int MODE, int F, bool OPEN = (F % fps(MODE) == 0)> __device__ __forceinline__ v4u wread(Ctx& c) {
  constexpr int i = F % fps(MODE);
  if constexpr (OPEN) { c.vb0 = c.nb0; if (two(MODE)) c.vb1 = c.nb1; }
  LAS char* R = c.L + L_RING;
  if (MODE == 0) return *(LAS v4u*)(R + c.vb0 + i * KB);
  if (MODE == 1) return tr_frag<512>(R + ((i & 1) ? c.vb1 : c.vb0) + ((i >> 1) & 1) * 2 * KB + (i >> 2) * 8 * KB);
  if (MODE == 4) return *(LAS v4u*)(R + c.vb0 + (i & 3) * KB + (i >> 2) * 8 * KB);
  if (MODE == 2) return tr_frag(R + ((i & 1) ? c.vb1 : c.vb0) + i * KB);
  return tr_frag(R + c.vb0 + i * KB);
}
__device__ __forceinline__ void ring_publish(Ctx& c) { MEMBAR(); *(volatile LAS unsigned*)(c.L + c.fa) = (unsigned)c.seq; MEMBAR(); }
template <int MODE> __device__ __forceinline__ void wpre(Ctx& c, int lb0, int lb1) {
  slot_wait(c, c.seq);
  slot_base<MODE>(c, c.seq, lb0, lb1);
  sfor<4>([&](auto F) INL { c.wf<F.v>() = wread<MODE, F.v>(c); });
  if (fps(MODE) == 8) c.fpre = *(volatile LAS v2u*)flg(c, F_FULL);
}
struct NoSide { template <class T> __device__ __forceinline__ void operator()(T) const {} };
constexpr int fmap(int f, int nf, int fpsl, int tf) {
  if (tf < 0 || f < nf - fpsl || f >= nf) return f;
  const int i = f - (nf - fpsl), hf = fpsl / 2;
  return nf - fpsl + 2 * (i % hf) + (i < hf ? tf : 1 - tf);
}
constexpr u64 DUAL = 0xFEDC76B54A329180ull;
constexpr int dual_first(int ch) { for (int u = 0; u < 16; ++u) if (((DUAL >> (4 * u)) & 15) == (u64)ch) return u; return -1; }
template <int NF, int TPS, int MODE, int NEXT, bool XA, int ZERO, int RELF, int EARLY, int CAP, int TF, class SIDE>
__device__ __forceinline__ void gemm4(Ctx& c, v4u (&a)[32], f32x16& c0, f32x16& c1, f32x16& c2, f32x16& c3, int lb0, int lb1, int nb0, int nb1, SIDE side) {
  constexpr int FPS = fps(MODE);
  static_assert(NF % FPS == 0, "whole slots");
  sfor<NF>([&](auto F) INL {
    constexpr int f = F.v, un = NF == 128 ? (int)((DUAL >> (4 * (f / 8))) & 15) : 0;
    constexpr int fm = fmap(f, NF, FPS, TF);
    constexpr int s = NF == 128 ? 4 * (un & 7) + (f % 8) / 2 : fm / TPS, t = fm % TPS, fn = f + 4, i = fn % FPS;
    const v4u x = a[s & 31], wv = c.wf<f % 4>();
    f32x16& acc = un >= 8 ? (t ? c3 : c2) : (t ? c1 : c0);
    const f32x16 cin = (s == 0 && ((ZERO >> t) & 1)) ? zero16() : acc;
    acc = XA ? mfma(x, wv, cin) : mfma(wv, x, cin);
    PIN(acc);
    SB();
    if constexpr (fn < NF) {
      c.wf<f % 4>() = wread<MODE, fmap(fn, NF, FPS, TF), i == 0>(c);
      constexpr bool lastslot = fn / FPS == NF / FPS - 1;
      if constexpr (!lastslot || NEXT >= 0) {
        constexpr int sl = fn / FPS, ahead = FPS == 8 && sl + 2 < NF / FPS ? 2 : 1;
        if constexpr (FPS == 16 || !(sl & 1)) {
          if constexpr (i == (FPS == 8 ? 2 : FPS - 7)) c.fpre = *(volatile LAS v2u*)flg(c, F_FULL);
          if constexpr (i == FPS - 4) { c.fpre.x = umin(c.fpre.x, c.fpre.y); PIN(c.fpre); }
          if constexpr (i == FPS - 3) { c.known = __builtin_amdgcn_readfirstlane(c.fpre.x); asm volatile("" : "+s"(c.known)); }
          if constexpr (i == FPS - 1) slot_wait(c, c.seq + ahead);
        }
        if constexpr (i == FPS - 3) {
          if constexpr (lastslot && NEXT != MODE) slot_base<(NEXT < 0 ? 0 : NEXT)>(c, c.seq + 1, nb0, nb1); else slot_next<MODE>(c);
        }
      }
      if constexpr (i == FPS - 1) c.seq++;
    } else if constexpr (NEXT >= 0) {
      c.wf<f % 4>() = wread<(NEXT < 0 ? 0 : NEXT), fn - NF>(c);
      if constexpr (fps(NEXT) == 8 && fn - NF == 1) c.fpre = *(volatile LAS v2u*)flg(c, F_FULL);
    }
    if constexpr (fn >= FPS && fn <= NF + 2 && i == 2) { if constexpr ((f >= RELF || fn / FPS - 1 < EARLY) && fn / FPS - 1 < CAP) ring_publish(c); }
    else if constexpr (f == RELF && RELF > 0) ring_publish(c);
    side(F);
    SB();
  });
}
template <int NF, int TPS, int MODE, int NEXT, bool XA, int ZERO, int RELF, int EARLY, int CAP, int TF = -1, class SIDE>
__device__ __forceinline__ void gemm(Ctx& c, v4u (&a)[32], f32x16& c0, f32x16& c1, int lb0, int lb1, int nb0, int nb1, SIDE side) {
  gemm4<NF, TPS, MODE, NEXT, XA, ZERO, RELF, EARLY, CAP, TF>(c, a, c0, c1, c0, c1, lb0, lb1, nb0, nb1, side);
}
__device__ __forceinline__ unsigned sum4(unsigned m, v4u v) { return ((m + v.x + v.y) + v.z + v.w); }
__device__ __forceinline__ unsigned redu_h(unsigned v) {
  auto s = __builtin_amdgcn_permlane32_swap(v, v, false, false);
  return (unsigned)s[0] + (unsigned)s[1];
}
template <bool EX> __device__ __forceinline__ void ld_pair(Ctx& c, v4u (&a)[32], v4u (&k)[8], int lv, int sb, int q, int e) {
  if (EX) {
    if (e < 4) a[(4 * q + e) & 31] = ld_ws(c, lv + e * KB, sb + q * 2 * REC);
    else k[q & 7] = ld_ws(c, lv, sb + q * 2 * REC + 4 * KB);
  } else {
    if (e < 4) a[(4 * q + e) & 31] = ld_ws(c, lv + (e & 1) * KB, sb + (q * 4 + (e & 2)) * REC);
    else k[q & 7].x = __builtin_amdgcn_raw_buffer_load_b32(c.rs, c.kv, sb + q * 4 * REC, 16);
  }
}
template <bool EX> __device__ __forceinline__ unsigned side_sum(v4u k) { return EX ? k.x + k.y + k.z : k.x; }
template <bool EX> __device__ __forceinline__ unsigned pair_sum(v4u (&a)[32], v4u (&k)[8], int q) {
  return redu_h(sum4(sum4(side_sum<EX>(k[q & 7]), a[(4 * q) & 31]), a[(4 * q + 1) & 31]) + sum4(sum4(0u, a[(4 * q + 2) & 31]), a[(4 * q + 3) & 31]));
}
template <bool EX> __device__ __forceinline__ void settle(Ctx& c, unsigned sum, v4u (&a)[32], v4u (&k)[8], int lv, int sb, int q, unsigned want) {
  if (__builtin_expect(__any(sum != want), 0)) {
    unsigned sp = 0;
#pragma nounroll
    do {
      __builtin_amdgcn_s_sleep(BACKOFF);
      MEMBAR();
#pragma unroll
      for (int e = 0; e < 5; ++e) ld_pair<EX>(c, a, k, lv, sb, q, e);
    } while (__any(pair_sum<EX>(a, k, q) != want) && ++sp < SPIN_CAP);
  }
}
template <int F, int NQ, bool EX>
__device__ __forceinline__ void check_step(Ctx& c, unsigned& m0, v4u (&a)[32], v4u (&k)[8], int lv, int sb, unsigned want) {
  constexpr int q = (F + 8) / 8, r = (F + 8) % 8;
  if constexpr (q < NQ) {
    if constexpr (r == 1) { m0 = sum4(0u, a[(4 * q) & 31]); PIN(m0); }
    if constexpr (r >= 2 && r <= 4) { m0 = sum4(m0, a[(4 * q + r - 1) & 31]); PIN(m0); }
    if constexpr (r == 5) { m0 += side_sum<EX>(k[q & 7]); PIN(m0); }
    if constexpr (r == 6) { m0 = redu_h(m0); PIN(m0); }
    if constexpr (r == 7) settle<EX>(c, m0, a, k, lv, sb, q, want);
  }
}
template <bool EX, int XT> __device__ __forceinline__ void open_phase(Ctx& c, v4u (&a)[32], v4u (&k)[8], int lv, int sb, unsigned want) {
  __builtin_amdgcn_s_sleep(XT);
#pragma unroll
  for (int i = 0; i < 10; ++i) ld_pair<EX>(c, a, k, lv, sb, i / 5, i % 5);
  SB();
  settle<EX>(c, pair_sum<EX>(a, k, 0), a, k, lv, sb, 0, want);
}
template <int G, int PER, int Q0, int Q1, bool EX> __device__ __forceinline__ void req(Ctx& c, v4u (&a)[32], v4u (&k)[8], int lv, int sb) {
  if constexpr (G >= 0 && Q0 + G / PER < Q1) {
    sfor<5>([&](auto E) INL { if constexpr (E.v * PER / 5 == G % PER) ld_pair<EX>(c, a, k, lv, sb, Q0 + G / PER, E.v); });
  }
}
__device__ __forceinline__ void st_chk(Ctx& c, unsigned sum, int sb, unsigned tag) {
  const unsigned v = tag - redu_h(sum);
  if (c.lane < 32) __builtin_amdgcn_raw_buffer_store_b32(v, c.rs, c.lane * 4 + 2 * KB, sb, 16);
}
__device__ __forceinline__ u64 mix64(u64 z) {
  z += 0x9E3779B97F4A7C15ull;
  z = (z ^ (z >> 30)) * 0xBF58476D1CE4E5B9ull;
  z = (z ^ (z >> 27)) * 0x94D049BB133111EBull;
  return z ^ (z >> 31);
}
__device__ __forceinline__ float csum(v4u f, float s) { return dot2(f.w, ONES2, dot2(f.z, ONES2, dot2(f.y, ONES2, dot2(f.x, ONES2, s)))); }
__device__ __forceinline__ v4u cs_operand(float s) { unsigned hi, lo; split2(s, hi, lo); return v4u{hi | (hi << 16), lo, 0u, 0u}; }

__global__ __launch_bounds__(512, 2) void fused_fwd(const u16* __restrict__ x, const u16* __restrict__ wqkv, const u16* __restrict__ wo,
                                                    const u16* __restrict__ wg, const u16* __restrict__ wu, const u16* __restrict__ wd,
                                                    u16* __restrict__ out, char* __restrict__ ws) {
  __shared__ __attribute__((aligned(16))) char smem[L_END];
  u64 pc0;
  asm volatile("s_getpc_b64 %0" : "=s"(pc0));
  Ctx c;
  c.L = (LAS char*)smem;
  c.rs = __builtin_amdgcn_make_buffer_rsrc((void*)ws, 0, WS_END, 0x00020000);
  const int wave = __builtin_amdgcn_readfirstlane(threadIdx.x >> 6);
  const int b = blockIdx.x, xc = b & 7, j = b >> 3;
  const int cl = (xc >> 1) * 4 + (j >> 3), m = (xc & 1) * 8 + (j & 7);
  const int rh = m >> 3, cg = m & 7;
  const int g0 = gsplit(m), ng = gsplit(m + 1) - g0;
  c.lane = 0; c.wave = wave; c.seq = 0; c.known = 0; c.vb0 = 0; c.vb1 = 0; c.nb0 = 0; c.nb1 = 0; c.fa = 0; c.kv = 0; c.fpre = v2u{0u, 0u};
  const int rot = (2 * cl + m) & 7;
  LAS char* L = c.L;
  if (threadIdx.x < 32) *flg(c, threadIdx.x) = 0u;
  asm volatile("s_waitcnt lgkmcnt(0)\n\ts_barrier" ::: "memory");
#define LANEVARS                                                                                                              \
  const int lane = fresh_lane(), h = lane >> 5, l31 = lane & 31, lv = lane * 16;                                               \
  const int trN = (8 * h + ((lane >> 2) & 3)) * 64 + ((lane >> 4) & 1) * 32 + (lane & 3) * 8;                                   \
  const int trS = (8 * h + ((lane >> 2) & 3)) * 64 + ((lane >> 4) & 1) * 32 + (((lane & 1) << 1) | ((lane >> 1) & 1)) * 8;       \
  (void)trN; (void)trS; (void)l31; (void)lv;
  const int pp = wave & 1;
  auto load_half0 = [&](int sl) INL {
    const int lane = fresh_lane();
    const unsigned vo1 = (lane >> 2) * 3072 + (lane & 3) * 16;
    const char* wb = (const char*)wqkv + m * 64 + (sl < 4 ? (1 + pp) * 1024 : 0);
#pragma unroll
    for (int p = 0; p < 8; ++p) {
      const int s = sl < 4 ? sl * 8 + p : (sl - 4) * 16 + 2 * p + pp;
      glds(wb + (size_t)(((s + 4 * rot) & 31) * 16) * 3072, vo1, L_RING + sl * SLOT + p * 2048 + pp * 1024);
    }
  };
  auto cache_load = [&](auto& wc, auto NK) INL {
    const int lane = fresh_lane();
    const __amdgpu_buffer_rsrc_t rd = __builtin_amdgcn_make_buffer_rsrc((void*)wd, 0, 1376 * 1024, 0x00020000);
    const int vo2 = (lane >> 3) * 1024 + (((lane & 7) ^ (((lane >> 4) & 1) << 2)) * 16);
#pragma unroll
    for (int k = 0; k < NK.v; ++k) {
#pragma unroll
      for (int q = 0; q < 2; ++q) {
        const int hs = (NK.v == 5 ? 2 * k : k < 5 ? 2 * k + 1 : 10) * 2 + pp;
        const int p = min(hs * 2 + q, 42);
        const int m3 = min(p - 32 + 1 + ((p - 32) >> 1), 15);
        const int aa = p < 32 ? gsplit(p & 15) + (p >> 4) : gsplit(m3) + 2;
#pragma unroll
        for (int e = 0; e < 4; ++e) wc[k * 8 + q * 4 + e] = __builtin_amdgcn_raw_buffer_load_b128(rd, vo2, aa * 32768 + cg * 128 + e * 8192, 0);
        SB();
        if (NK.v == 5) __builtin_amdgcn_s_sleep(2);
      }
    }
  };
  auto cache_conv = [&](auto& wc, auto NK) INL {
    const int lane = fresh_lane(), h = lane >> 5;
    const int tk = (8 * h + ((lane >> 2) & 3)) * 128 + ((lane >> 4) & 1) * 32 + (((lane & 1) << 1) | ((lane >> 1) & 1)) * 8 + ((lane >> 3) & 1) * 64;
    LAS char* d = L + L_XCH + (wave - 4) * 4096;
    wait_lds<4, 2>(c, F_FREE, 4u);
#pragma unroll
    for (int q = 0; q < 2 * NK.v; ++q) {
#pragma unroll
      for (int e = 0; e < 4; ++e) *(LAS v4u*)(d + lane * 16 + e * KB) = wc[q * 4 + e];
      MEMBAR(); SB();
#pragma unroll
      for (int e = 0; e < 4; ++e) wc[q * 4 + e] = tr_frag<512>(d + (e >> 1) * 2048 + (tk ^ (((e ^ q) & 1) * 64)));
      MEMBAR(); SB();
    }
    *flg(c, F_CONV + wave - 4) = 1u;
    MEMBAR();
  };
  auto cache_put = [&](auto& wc, auto NK, int base) INL {
    const int lane = fresh_lane();
#pragma unroll
    for (int k = 0; k < NK.v; ++k) {
      const int sq = base + (NK.v == 5 ? 2 * k : k < 5 ? 2 * k + 1 : 10);
      wait_lds<4, 4>(c, F_FREE, (unsigned)(sq - NSLOT + 1));
      LAS char* d = L + L_RING + (sq & (NSLOT - 1)) * SLOT + pp * 8192;
#pragma unroll
      for (int e = 0; e < 8; ++e) {
        *(LAS v4u*)(d + lane * 16 + e * KB) = wc[k * 8 + e];
        asm volatile("s_nop 3" ::: "memory");
      }
      wait_lds<1, 2>(c, F_FULL + pp, (unsigned)sq);
      *flg(c, F_FULL + pp) = (unsigned)(sq + 1);
      MEMBAR();
    }
  };

  if (wave < 4) {
    __builtin_amdgcn_s_setprio(2);
    const int w = wave, mh = w >> 1, kh = w & 1;
    c.fa = opqv(L_FLG + (F_FREE + wave) * 4);
    asm volatile("" : "+s"(c.rs));
    v4u a[32], xr0, xr1;
    v4u k[8];
    f32x16 A0, A1, B0, B1;
    LANEVARS
    c.lane = lane;
    c.kv = h * 2 * REC + 2 * KB + l31 * 4;
    float rstd;
    unsigned tagx, tagy, tagc;
    v4u mfrag;
    auto set_stats = [&](float sum, float ssq) INL -> float {
      const float s = sum * (1.f / 512), q = ssq * (1.f / 512);
      unsigned hi, lo;
      split2(-s, hi, lo);
      mfrag = h == 0 ? v4u{hi | (lo << 16), hi, 0u, 0u} : v4u{0u, 0u, 0u, 0u};
      return fmaxf(q - s * s, 0.f) + 1e-5f;
    };
    auto cs_frag = [&](int tile) INL { return *(LAS v4u*)(L + L_CS + tile * 512 + l31 * 16); };
    auto rs_put = [&]() INL { const int lane = fresh_lane(); *(LAS float*)(L + L_KVS + w * 2304 + lane * 4) = h ? rstd * LOG2E : rstd; MEMBAR(); };
    auto rs_get = [&](int q) INL {
      const int h = fresh_lane() >> 5;
      const f32x4 r = *(LAS f32x4*)(L + L_KVS + w * 2304 + (8 * q + 4 * h) * 4), rl = *(LAS f32x4*)(L + L_KVS + w * 2304 + 128 + (8 * q + 4 * h) * 4);
#pragma unroll
      for (int i = 0; i < 4; ++i) { B0[4 * q + i] = r[i]; B1[4 * q + i] = rl[i]; }
    };
    {
      const __amdgpu_buffer_rsrc_t rx = __builtin_amdgcn_make_buffer_rsrc((void*)x, 0, 2048 * 1024, 0x00020000);
      const int r8 = lane >> 3;
      const unsigned xv0 = r8 * 1024 + (((lane & 7) ^ (r8 >> 1)) * 16), xv1 = xv0 ^ 64;
      const char* xb = (const char*)x + (size_t)(cl * 128 + w * 32) * 1024;
      const int ro = l31 * 128 + ((h ^ ((l31 >> 1) & 7)) * 16);
      auto xbuf = [&](int kp) INL { return (kp % 3 == 1 ? L_XCH : L_RING + (4 + (kp % 3) / 2) * SLOT) + w * 4096; };
      auto xreq = [&](int kp) INL {
#pragma unroll
        for (int q = 0; q < 4; ++q) glds(xb + q * 8192 + ((kp + rot) & 7) * 128, (q & 1) ? xv1 : xv0, xbuf(kp) + q * 1024);
      };
      auto xget = [&](int kp) INL {
        if (kp < 6) VMWAIT(8); else if (kp == 6) VMWAIT(4); else VMWAIT(0);
#pragma unroll
        for (int q = 0; q < 4; ++q) a[kp * 4 + q] = *(LAS v4u*)(L + xbuf(kp) + (ro ^ (q * 32)));
        if (kp + 3 < 8) {
          asm volatile("s_waitcnt lgkmcnt(0)" : "+v"(a[kp * 4]), "+v"(a[kp * 4 + 1]), "+v"(a[kp * 4 + 2]), "+v"(a[kp * 4 + 3])::"memory");
          xreq(kp + 3);
        }
        MEMBAR();
      };
      __builtin_amdgcn_s_setprio(3);
      xreq(0); xreq(1); xreq(2);
      __builtin_amdgcn_s_setprio(2);

#pragma unroll
      for (int kp = 0; kp < XPRE; ++kp) xget(kp);
      wpre<2>(c, trN, trS);
      v4u s0, ca, cb;
      const int s0b = WS_S0P + (cl * 128 + w * 32) * 32;
      gemm<64, 2, 2, -1, true, 3, 0, 0, 99>(c, a, A0, A1, trN, trS, 0, 0, [&](auto F) INL {
        constexpr int f = F.v;
        if ((f & 3) == 1 && (f >> 2) + XPRE < 8) xget((f >> 2) + XPRE);
        if (f == 4 * (8 - XPRE)) s0 = ld_ws(c, l31 * 32, s0b);
        if (f == 4 * (8 - XPRE) + 2) {
          const int xq = l31 * 1024 + h * 16, xqs = (cl * 128 + mh * 64 + rh * 32) * 1024 + cg * 128 + kh * 64;
          xr0 = __builtin_amdgcn_raw_buffer_load_b128(rx, xq, xqs, 0);
          xr1 = __builtin_amdgcn_raw_buffer_load_b128(rx, xq + 32, xqs, 0);
        }
        if (f == 40) {
          wait_lds<2, 0>(c, F_CS1R, 1u);
          const v2u tg = *(volatile LAS v2u*)flg(c, F_TAG);
          tagx = __builtin_amdgcn_readfirstlane(tg.x); tagy = __builtin_amdgcn_readfirstlane(tg.y); tagc = (tagx * 0x9E3779B1u + tagy) | 1u;
          if (__any(s0.y != tagx || s0.w != tagy)) {
            unsigned sp = 0;
#pragma nounroll
            do { MEMBAR(); s0 = ld_ws(c, l31 * 32, s0b); } while (__any(s0.y != tagx || s0.w != tagy) && ++sp < SPIN_CAP);
          }
          rstd = __builtin_amdgcn_rsqf(set_stats(u2f(s0.x), u2f(s0.z)));
          rs_put();
          ca = cs_frag(0); cb = cs_frag(1);
        }
        if (f == 45) { A0 = mfma(mfrag, ca, A0); A1 = mfma(mfrag, cb, A1); }
        if (f >= 54 && f < 62 && !(f & 1)) rs_get((f - 54) >> 1);
      });
    }
#pragma unroll 1
    for (int layer = 0; layer < 2; ++layer) {
      bool publisher;
      {
        LANEVARS
#pragma unroll
        for (int i = 0; i < 16; ++i) B1[i] *= A0[i];
        SB();
#pragma unroll
        for (int i = 0; i < 16; ++i) { B1[i] = exp2c(B1[i]); A0[i] = fmaf(A0[i], B0[i], 1.f); A1[i] *= B0[i]; }
        SB();
        v4u pk0, pk1, vv0, vv1;
#pragma unroll
        for (int i = 0; i < 16; ++i) A0[i] = fmaxf(A0[i], B1[i]) + 1e-6f;
#pragma unroll
        for (int i = 0; i < 4; ++i) {
          pk0[i] = pack2(A0[2 * i], A0[2 * i + 1]); vv0[i] = pack2(A1[2 * i], A1[2 * i + 1]);
          pk1[i] = pack2(A0[8 + 2 * i], A0[9 + 2 * i]); vv1[i] = pack2(A1[8 + 2 * i], A1[9 + 2 * i]);
        }
        f32x16 kv = mfma(pk0, vv0, zero16());
        kv = mfma(pk1, vv1, kv);
        const float z = red_h(csum(pk0, 0.f) + csum(pk1, 0.f));
        LAS char* ks = L + L_KVS + w * 2304;
        if (h == 0) *(LAS float*)(ks + 2048 + l31 * 4) = z;
        if (lane & 16) {
          *(LAS f32x4*)(ks + lv) = f32x4{kv[8], kv[9], kv[10], kv[11]};
          *(LAS f32x4*)(ks + 1024 + lv) = f32x4{kv[12], kv[13], kv[14], kv[15]};
          MEMBAR();
        } else {
          MEMBAR();
          *(LAS f32x4*)(ks + lv) = f32x4{kv[0], kv[1], kv[2], kv[3]};
          *(LAS f32x4*)(ks + 1024 + lv) = f32x4{kv[4], kv[5], kv[6], kv[7]};
        }
        MEMBAR();
        *flg(c, F_KFL + w) = (unsigned)(layer + 1);
        const v4u kf = *(volatile LAS v4u*)flg(c, F_KFL);
        MEMBAR();
        f32x4 p0[4], p1[4];
        f32x2 pz[4];
#pragma unroll
        for (int ww = 0; ww < 4; ++ww) {
          p0[ww] = *(LAS f32x4*)(L + L_KVS + ww * 2304 + lv);
          p1[ww] = *(LAS f32x4*)(L + L_KVS + ww * 2304 + 1024 + lv);
          pz[ww] = *(LAS f32x2*)(L + L_KVS + ww * 2304 + 2048 + (lane & 15) * 8);
        }
        MEMBAR();
        SB();
        publisher = (unsigned)__builtin_amdgcn_readfirstlane(umin(umin(kf.x, kf.y), umin(kf.z, kf.w))) > (unsigned)layer;
        if (publisher) {
          const f32x4 k0 = (p0[0] + p0[1]) + (p0[2] + p0[3]), k1 = (p1[0] + p1[1]) + (p1[2] + p1[3]);
          const f32x2 zz = (pz[0] + pz[1]) + (pz[2] + pz[3]);
          const unsigned tg = layer ? tagy : tagx;
          const unsigned tl = tg >> ((lane & 1) * 16), t0 = tl & 0xffu, t1 = (tl >> 8) & 0xffu;
          const int kvb = WS_KVP + ((layer * 16 + m) * 16 + cl) * KVR;
          auto st2 = [&](float u, float v, int vo) INL {
            __builtin_amdgcn_raw_buffer_store_b64(v2u{(f2u(u) & 0xffffff00u) | t0, (f2u(v) & 0xffffff00u) | t1}, c.rs, vo, kvb, 16);
          };
          st2(k0[0], k0[1], lane * 8); st2(k0[2], k0[3], lane * 8 + 512); st2(k1[0], k1[1], lane * 8 + 1024); st2(k1[2], k1[3], lane * 8 + 1536);
          if (lane < 16) st2(zz[0], zz[1], lane * 8 + 2048);
        }
        MEMBAR();
      }
      B0 = mfma(cs_frag(2), mfrag, zero16());
      if (layer == 0) { LANEVARS wpre<3>(c, trN, trN); gemm<32, 1, 3, -1, false, 0, 0, 0, 99>(c, a, B0, B0, trN, trN, 0, 0, NoSide()); }
      else { wpre<0>(c, lv, lv); gemm<32, 1, 0, -1, false, 0, 0, 0, 99>(c, a, B0, B0, lv, lv, 0, 0, NoSide()); }
      const int lq = fresh_lane();
      const int kb0 = (8 * (lq >> 5) + ((lq >> 2) & 3)) * 128 + ((lq >> 4) & 1) * 32 + (((lq & 1) << 1) | ((lq >> 1) & 1)) * 8 + kh * 4096 + ((kh ^ (lq >> 3)) & 1) * 64;
      const int kb1 = kb0 ^ 64;
      auto res_init = [&](f32x16& R, int i) INL { const unsigned u = i < 4 ? xr0[i & 3] : xr1[i & 3]; R[2 * i] = blo(u); R[2 * i + 1] = bhi(u); };
      {
        LANEVARS
        v4u pq0, pq1;
        const float rl = rstd * LOG2E;
#pragma unroll
        for (int i = 0; i < 16; ++i) B1[i] = exp2c(B0[i] * rl);
#pragma unroll
        for (int i = 0; i < 16; ++i) B0[i] = fmaxf(fmaf(B0[i], rstd, 1.f), B1[i]) + 1e-6f;
#pragma unroll
        for (int i = 0; i < 4; ++i) { pq0[i] = pack2(B0[2 * i], B0[2 * i + 1]); pq1[i] = pack2(B0[8 + 2 * i], B0[9 + 2 * i]); }
#pragma unroll
        for (int i = 0; i < 8; ++i) res_init(A0, i);
        PIN(pq0); PIN(pq1); PIN(A0);
        const int ob = opq(WS_O + cl * ACT_CL + (w * 16 + m) * REC);
        {
          const int lane = fresh_lane();
          const unsigned tg = layer ? tagy : tagx;
          const unsigned tl = tg >> ((lane & 1) * 16), t0 = tl & 0xffu, t1 = (tl >> 8) & 0xffu;
          const v4u T = {tg & 0xffu, (tg >> 8) & 0xffu, (tg >> 16) & 0xffu, tg >> 24};
          const int kvb = WS_KVP + (layer * 16 + m) * 16 * KVR, pv = (lane >> 5) * KVR + (lane & 31) * 16;
          f32x4 s;
          unsigned zpk;
          if (publisher) __builtin_amdgcn_s_sleep(KVDP); else __builtin_amdgcn_s_sleep(KVDELAY);
#pragma nounroll
          for (unsigned sp = 0; sp < SPIN_CAP; ++sp) {
            v4u p[8];
            v2u zp = __builtin_amdgcn_raw_buffer_load_b64(c.rs, (lane >> 2) * KVR + (lane & 3) * 8 + 2048, kvb + w * 32, 16);
#pragma unroll
            for (int i = 0; i < 8; ++i) p[i] = ld_ws(c, pv, kvb + w * 512 + i * 2 * KVR);
            SB();
            zp.x ^= t0; zp.y ^= t1;
            unsigned chk = zp.x | zp.y;
            float za = u2f(zp.x), zb = u2f(zp.y);
            za += dpp<0x128>(za); zb += dpp<0x128>(zb); za += dpp<0x124>(za); zb += dpp<0x124>(zb);
            zpk = pack2(red_g(za), red_g(zb));
            PIN(zpk);
#pragma unroll
            for (int i = 0; i < 8; ++i) {
              p[i] ^= T; chk |= (p[i].x | p[i].y) | (p[i].z | p[i].w);
              s = i ? s + __builtin_bit_cast(f32x4, p[i]) : __builtin_bit_cast(f32x4, p[i]);
              PIN(s);
            }
            if (!__any((chk & 0xffu) != 0u)) break;
            __builtin_amdgcn_s_sleep(BACKOFF);
            MEMBAR();
          }
          {
            const auto e0 = __builtin_amdgcn_permlane32_swap(f2u(s[0]), f2u(s[2]), false, false), e1 = __builtin_amdgcn_permlane32_swap(f2u(s[1]), f2u(s[3]), false, false);
            *(LAS unsigned*)(L + L_KVF + (lane & 31) * 32 + (lane >> 5) * 16 + w * 4) = pack2(u2f((unsigned)e0[0]) + u2f((unsigned)e0[1]), u2f((unsigned)e1[0]) + u2f((unsigned)e1[1]));
          }
          if (lane < 4) *(LAS unsigned*)(L + L_ZF + w * 16 + lane * 4) = zpk;
          MEMBAR();
          *flg(c, F_KVR + w) = (unsigned)(layer + 1);
        }
        v4u kvf;
        v2u z0, z1, z2, z3;
#pragma nounroll
        for (unsigned sp = 0; sp < SPIN_CAP; ++sp) {
          const v4u fl = *(volatile LAS v4u*)flg(c, F_KVR);
          MEMBAR();
          kvf = *(LAS v4u*)(L + L_KVF + lv);
          z0 = *(LAS v2u*)(L + L_ZF + h * 8); z1 = *(LAS v2u*)(L + L_ZF + 16 + h * 8);
          z2 = *(LAS v2u*)(L + L_ZF + 32 + h * 8); z3 = *(LAS v2u*)(L + L_ZF + 48 + h * 8);
          MEMBAR();
          if ((unsigned)__builtin_amdgcn_readfirstlane(umin(umin(fl.x, fl.y), umin(fl.z, fl.w))) > (unsigned)layer) break;
        }
        const bool up = (lane & 16) != 0;
        const v4u zz = {0u, 0u, 0u, 0u};
        B0 = mfma(up ? zz : kvf, pq0, zero16());
        B0 = mfma(up ? kvf : zz, pq1, B0);
        const float d0 = dot2(pq0.y, z0.y, dot2(pq0.x, z0.x, 0.f)) + dot2(pq0.w, z1.y, dot2(pq0.z, z1.x, 0.f));
        const float d1 = dot2(pq1.y, z2.y, dot2(pq1.x, z2.x, 0.f)) + dot2(pq1.w, z3.y, dot2(pq1.z, z3.x, 0.f));
        const float r0 = __builtin_amdgcn_rcpf(red_h(d0) + 1e-6f), r1 = __builtin_amdgcn_rcpf(red_h(d1) + 1e-6f);
        v4u o0, o1;
#pragma unroll
        for (int i = 0; i < 4; ++i) { o0[i] = pack2(B0[2 * i] * r0, B0[2 * i + 1] * r0); o1[i] = pack2(B0[8 + 2 * i] * r1, B0[9 + 2 * i] * r1); }
        st_ws(c, o0, lv, ob);
        st_ws(c, o1, lv + KB, ob);
        st_chk(c, sum4(sum4(0u, o0), o1), ob, tagc);
      }
      const int rtA = opq(mh * 2 + rh), clb = opq(cl * ACT_CL);
      auto res_send = [&](f32x16& A1, int q, unsigned xseq) INL {
        LAS char* xs = L + L_XCH + w * 4096 + lv;
        *(LAS f32x4*)(xs + q * 2048) = f32x4{A1[8 * q], A1[8 * q + 1], A1[8 * q + 2], A1[8 * q + 3]};
        *(LAS f32x4*)(xs + q * 2048 + 1024) = f32x4{A1[8 * q + 4], A1[8 * q + 5], A1[8 * q + 6], A1[8 * q + 7]};
        MEMBAR();
        if (q) *flg(c, F_XF + w) = xseq;
      };
      auto res_epilogue = [&](f32x16& A0, int xi, bool final_out, unsigned xseq) INL {
        LAS char* xs = L + L_XCH;
        const int lv = fresh_lane() * 16;
        f32x4 o0, o1, o2, o3;
#pragma nounroll
        for (unsigned sp = 0; sp < SPIN_CAP; ++sp) {
          const unsigned fl = *flg(c, F_XF + (w ^ 1));
          MEMBAR();
          o0 = *(LAS f32x4*)(xs + (w ^ 1) * 4096 + lv); o1 = *(LAS f32x4*)(xs + (w ^ 1) * 4096 + 1024 + lv);
          o2 = *(LAS f32x4*)(xs + (w ^ 1) * 4096 + 2048 + lv); o3 = *(LAS f32x4*)(xs + (w ^ 1) * 4096 + 3072 + lv);
          MEMBAR();
          if ((unsigned)__builtin_amdgcn_readfirstlane(fl) >= xseq) break;
        }
        xr0 = v4u{pack2(A0[0] + o0[0], A0[1] + o0[1]), pack2(A0[2] + o0[2], A0[3] + o0[3]), pack2(A0[4] + o1[0], A0[5] + o1[1]), pack2(A0[6] + o1[2], A0[7] + o1[3])};
        xr1 = v4u{pack2(A0[8] + o2[0], A0[9] + o2[1]), pack2(A0[10] + o2[2], A0[11] + o2[3]), pack2(A0[12] + o3[0], A0[13] + o3[1]), pack2(A0[14] + o3[2], A0[15] + o3[3])};
        if (final_out) {
          const int lo = fresh_lane();
          GAS char* ob = (GAS char*)out + (size_t)((cl * 4 + rtA) * 32 + (lo & 31)) * 1024 + cg * 128 + kh * 64 + (lo >> 5) * 16;
          *(GAS v4u*)ob = xr0;
          *(GAS v4u*)(ob + 32) = xr1;
        } else {
          const int xb = WS_X + clb + ((xi * 4 + rtA) * 16 + cg * 2) * REC;
          st_ws(c, xr0, lv, xb + kh * 2 * KB);
          st_ws(c, xr1, lv + KB, xb + kh * 2 * KB);
          float q0 = 0.f, q1 = 0.f;
#pragma unroll
          for (int i = 0; i < 4; ++i) { q0 = dot2(xr0[i], xr0[i], q0); q1 = dot2(xr1[i], xr1[i], q1); }
          const unsigned s = f2u(red_h(csum(xr0, 0.f) + csum(xr1, 0.f))), q = f2u(red_h(q0 + q1));
          const unsigned ck = tagc - redu_h(sum4(sum4(0u, xr0), xr1)) - s - q;
          if (lv < 512) st_ws(c, v4u{s, q, ck, 0u}, lv, xb + 4 * KB + kh * 512);
        }
      };
      unsigned m0;
      {
        const int ab = WS_O + clb + (rtA * 16 + kh) * REC;
        wpre<1>(c, kb0, kb1);
        if (mh) __builtin_amdgcn_s_sleep(YOFF);
        open_phase<false, XT0>(c, a, k, lv, ab, 2 * tagc);
        gemm<32, 2, 1, -1, false, 2, 8, 0, 99, 1>(c, a, A0, A1, kb0, kb1, 0, 0, [&](auto F) INL {
          if (F.v == 24 && layer == 0) wait_lds<2, 0>(c, F_CONV + 2, 1u);
          if constexpr (F.v == 29 || F.v == 30) res_send(A1, F.v - 29, (unsigned)(layer * 0x200 + 1));
          req<F.v, 4, 2, 4, false>(c, a, k, lv, ab);
          check_step<F.v, 4, false>(c, m0, a, k, lv, ab, 2 * tagc);
        });
        res_epilogue(A0, layer, false, (unsigned)(layer * 0x200 + 1));
      }
      *flg(c, F_XF + w) = (unsigned)(layer * 0x200 + 0x100);
      *(LAS v4u*)(L + L_KVS + w * 2304 + lv) = xr0;
      *(LAS v4u*)(L + L_KVS + w * 2304 + 1024 + lv) = xr1;
      MEMBAR();
      auto res_back = [&]() INL { xr0 = *(LAS v4u*)(L + L_KVS + w * 2304 + lv); xr1 = *(LAS v4u*)(L + L_KVS + w * 2304 + 1024 + lv); };
      wpre<0>(c, lv, lv);
      v4u cs0;
      float sa, qa;
      auto stat_add = [&](int q) INL { sa = q ? sa + u2f(k[q].x) : u2f(k[q].x); qa = q ? qa + u2f(k[q].y) : u2f(k[q].y); PIN(sa); PIN(qa); };
      auto stat_end = [&]() INL { return set_stats(red_h(sa), red_h(qa)); };
      const int ab4 = WS_H + clb + (rtA * 44 + kh) * REC;
      const int hb = WS_H + clb + w * 44 * REC;
      float r2;
      v4u ho;
      unsigned hs;
      float h0, h1;
      auto hside = [&](auto F, f32x16& G, f32x16& U, int dst) INL {
        constexpr int f = F.v, sl = (f - 1) / 2;
        if constexpr (f >= 1 && f < 17) {
          if constexpr (f & 1) { h0 = relu(G[2 * sl]) * r2; PIN(h0); h1 = relu(G[2 * sl + 1]) * r2; PIN(h1); }
          else {
            h0 *= U[2 * sl]; PIN(h0); h1 *= U[2 * sl + 1]; PIN(h1);
            ho[sl & 3] = pack2(h0, h1); PIN(ho);
          }
        }
        if constexpr (f == 9 || f == 17) { st_ws(c, ho, lv + (f == 17) * KB, dst); hs = sum4(f == 9 ? 0u : hs, ho); PIN(hs); }
        if constexpr (f == 18) st_chk(c, hs, dst, tagc);
      };
      auto corr = [&](auto F, int at, f32x16& G, f32x16& U, int gi) INL {
        if (F.v == at - 6) cs0 = cs_frag(3 + 2 * gi);
        if (F.v == at - 3) { G = mfma(cs0, mfrag, G); SB(); cs0 = cs_frag(4 + 2 * gi); }
        if (F.v == at) U = mfma(cs0, mfrag, U);
      };
      if (m == 0) {
        const v4u Z = {0u, 0u, 0u, 0u};
        st_ws(c, Z, lv, hb + 43 * REC); st_ws(c, Z, lv + KB, hb + 43 * REC);
        st_chk(c, 0u, hb + 43 * REC, tagc);
      }
      const int xb3 = WS_X + clb + (layer * 4 + w) * 16 * REC;
      open_phase<true, XT1>(c, a, k, lv, xb3, 2 * tagc);
      gemm4<128, 2, 0, 0, false, 3, 62, 0, 99, -1>(c, a, A0, A1, B0, B1, lv, lv, lv, lv, [&](auto F) INL {
        constexpr int f = F.v, u = f / 8;
        req<f, 12, 2, 8, true>(c, a, k, lv, xb3);
        sfor<8>([&](auto CH) INL {
          if constexpr (CH.v > 0 && dual_first(CH.v) == u + 1) check_step<8 * (CH.v - 1) + f % 8, 8, true>(c, m0, a, k, lv, xb3, 2 * tagc);
          if constexpr (f == 8 * dual_first(CH.v)) stat_add(CH.v);
        });
        if constexpr (f == 89) { r2 = __builtin_amdgcn_rcpf(stat_end()); PIN(mfrag); PIN(r2); }
        if (f == 86 && layer == 0) wait_lds<1, 0>(c, F_CS3R, 1u);
        corr(F, 95, A0, A1, 0);
        if constexpr (f >= 96) hside(IC<f - 96>{}, A0, A1, hb + m * REC);
        corr(F, 123, B0, B1, 1);
      });
      const int tb0 = fresh_lane() * 16 + kh * 4096;
      if (ng == 3) {
        gemm<64, 2, 0, 4, false, 3, 0, 0, 99>(c, a, A0, A1, lv, lv, tb0, tb0, [&](auto F) INL {
          constexpr int f = F.v;
          hside(F, B0, B1, hb + (16 + m) * REC);
          corr(F, 47, A0, A1, 2);
          if constexpr (f == 19) res_back();
          if constexpr (f >= 23 && f < 31) { res_init(B0, f - 23); PIN(B0); }
          req<f - 32, 8, 0, 4, false>(c, a, k, lv, ab4);
          if constexpr (f >= 56) check_step<f - 64, 11, false>(c, m0, a, k, lv, ab4, 2 * tagc);
        });
      }
      if (opq(ng) == 2) {
        const unsigned tg0 = (unsigned)__builtin_amdgcn_s_memrealtime();
        sfor<20>([&](auto F) INL { hside(F, B0, B1, hb + (16 + m) * REC); });
        res_back();
#pragma unroll
        for (int i = 0; i < 8; ++i) res_init(B0, i);
        while ((unsigned)__builtin_amdgcn_s_memrealtime() - tg0 < (unsigned)NG2S) __builtin_amdgcn_s_sleep(2);
        sfor<20>([&](auto F) INL { req<F.v, 5, 0, 4, false>(c, a, k, lv, ab4); SB(); });
        wpre<4>(c, tb0, tb0);
        settle<false>(c, pair_sum<false>(a, k, 0), a, k, lv, ab4, 0, 2 * tagc);
        while ((unsigned)__builtin_amdgcn_s_memrealtime() - tg0 < (unsigned)NG2T) __builtin_amdgcn_s_sleep(2);
      }
      {
        const int h2 = ng == 3 ? hb + (32 + g0 - 2 * m) * REC : WS_END;
        auto side4 = [&](auto F) INL {
          constexpr int f = F.v;
          if constexpr (f < 10) { hside(IC<2 * f>{}, A0, A1, h2); hside(IC<2 * f + 1>{}, A0, A1, h2); }
          req<f, 8, 4, 8, false>(c, a, k, lv, ab4);
          req<f - S2H, 6, 8, 11, false>(c, a, k, lv, ab4);
          check_step<f, 11, false>(c, m0, a, k, lv, ab4, 2 * tagc);
          if constexpr (f == 85 || f == 86) res_send(B1, f - 85, (unsigned)(layer * 0x200 + 0x101));
        };
        gemm<88, 2, 4, -1, false, 2, 32, 3, 7, 1>(c, a, B0, B1, tb0, tb0, 0, 0, side4);
        tagc += 0x632BE5AAu;
        VMWAIT(0);
        res_epilogue(B0, 0, layer == 1, (unsigned)(layer * 0x200 + 0x101));
      }
      if (layer == 1) break;
      wpre<0>(c, lv, lv);
      const int xb1 = WS_X + clb + w * 16 * REC;
      open_phase<true, XT2>(c, a, k, lv, xb1, 2 * tagc);
      gemm<64, 2, 0, -1, true, 3, 34, 0, 99>(c, a, A0, A1, lv, lv, 0, 0, [&](auto F) INL {
        constexpr int f = F.v;
        req<f, 5, 2, 5, true>(c, a, k, lv, xb1);
        req<f - 15, 6, 5, 8, true>(c, a, k, lv, xb1);
        check_step<f, 8, true>(c, m0, a, k, lv, xb1, 2 * tagc);
        if constexpr ((f & 7) == 0) stat_add(f / 8);
        if constexpr (f == 57) { rstd = __builtin_amdgcn_rsqf(stat_end()); PIN(mfrag); rs_put(); }
        if (f == 56) cs0 = cs_frag(0);
        if (f == 60) { A0 = mfma(mfrag, cs0, A0); SB(); cs0 = cs_frag(1); }
        if (f == 63) A1 = mfma(mfrag, cs0, A1);
        if (f >= 59 && f < 63) rs_get(f - 59);
      });
      tagc += 0x632BE5AAu;
      VMWAIT(0);
    }
  } else if (wave < 6) {
    const int lw = wave - 4;
    LANEVARS
    c.lane = lane;
    volatile LAS unsigned* full = flg(c, F_FULL + lw);
    __builtin_amdgcn_s_setprio(3);
    load_half0(0);
    if (lw == 1) glds((const char*)(pc0 & ~(u64)255) + j * (CODEPF / 32), min(lane * 32, CODEPF / 32 - 32), L_RING + 7 * SLOT + 12288);
    __builtin_amdgcn_s_setprio(2);
    load_half0(1);
    VMWAIT(8); *full = 1u;
    load_half0(2);
    load_half0(3);
    VMWAIT(16); *full = 2u;
    {
      const unsigned tv = min(lane, 42) * 128, td = L_RING + (6 + lw) * SLOT + (lw ? 13312 : 3072);
      glds((const char*)(lw ? wu : wg) + b * 5504, tv, td);
      if (lw) glds((const char*)wd + b * 5504, tv, td);
      else glds((const char*)wo + b * 2048, (lane & 15) * 128, td);
      *flg(c, F_QGO + lw) = 1u;
    }
    VMWAIT(10); *full = 3u;
    VMWAIT(2); *full = 4u;
    __builtin_amdgcn_s_setprio(1);
    wait_lds<4, 1>(c, F_FREE, 1u);
    load_half0(4);
    load_half0(5);
    VMWAIT(8); *full = 5u;
    VMWAIT(0); *full = 6u;
    int seq = 6, pub = 6;
    v4u wc[48];
    auto stream = [&](int seg, int i0, int i1) INL {
      const int ln = fresh_lane(), lv = ln * 16;
      const unsigned vo2 = (ln >> 3) * 1024 + (((ln & 7) ^ (((ln >> 4) & 1) << 2)) * 16);
      const int src = (seg == 0 ? WS_WT1 + m * 96 * KB : WS_WT3 + g0 * 64 * KB);
#pragma unroll 1
      for (int i = i0; i < i1; ++i) {
        if (seq >= NSLOT) {
          const unsigned need = (unsigned)(seq - NSLOT + 1);
          bool first = true;
#pragma nounroll
          for (unsigned sp = 0; sp < SPIN_CAP; ++sp) {
            v4u f = *(volatile LAS v4u*)flg(c, F_FREE);
            if ((unsigned)__builtin_amdgcn_readfirstlane(umin(umin(f.x, f.y), umin(f.z, f.w))) >= need) break;
            if (first && pub < seq) { VMWAIT(0); pub = seq; *full = (unsigned)pub; }
            first = false;
            __builtin_amdgcn_s_sleep(4);
          }
          MEMBAR();
        }
        const unsigned dst = L_RING + (seq & (NSLOT - 1)) * SLOT + lw * 8192;
        const int hs = i * 2 + lw, hr = (seg == 2 && hs < 16) ? (int)((DUAL >> (4 * hs)) & 15) : hs;
        if (seg == 1) {
          const char* sp = (const char*)wo + (size_t)hr * 65536 + cg * 128;
#pragma unroll
          for (int e = 0; e < 8; ++e) glds(sp + e * 8192, vo2, dst + e * 1024);
        } else {
          glds4(ws + src + hr * 8192, lv, dst);
          glds4(ws + src + hr * 8192 + 4096, lv, dst + 4096);
        }
        ++seq;
        if (seq - pub > 2) { VMWAIT(16); pub = seq - 2; *full = (unsigned)pub; }
      }
    };
    auto drain = [&]() INL { VMWAIT(0); if (pub < seq) { pub = seq; *full = (unsigned)seq; } };
    cache_load(wc, IC<6>{});
    wait_lds<2, 1>(c, F_JOB, 1u);
    VMWAIT(32);
    stream(1, 0, 4);
    drain();
    if (lw == 0) { poll_tags<32>(c, WS_F1 + m * 4096, *(volatile LAS v2u*)flg(c, F_TAG)); *flg(c, F_PTOK) = 1u; }
    else wait_lds<1, 1>(c, F_PTOK, 1u);
    stream(2, 0, 8);
    drain();
    wait_lds<4, 1>(c, F_XF, 0x100u);
    cache_conv(wc, IC<6>{});
    stream(2, 8, 4 * ng);
    drain();
    cache_put(wc, IC<6>{}, seq);
    seq += 11; pub = seq;
    stream(0, 0, 6);
    stream(1, 0, 4);
    stream(2, 0, 4 * ng);
    drain();
    cache_put(wc, IC<6>{}, seq);
  } else {
    const int hh = wave - 6;
    LANEVARS
    c.lane = lane;
    const int pts = L_RING + (6 + hh) * SLOT;
    __builtin_amdgcn_s_setprio(3);
    {
      const unsigned vo1 = (lane >> 2) * 3072 + (lane & 3) * 16;
      const char* j0 = (const char*)wqkv + (size_t)(cl * 32) * 3072 + m * 64;
      glds(j0 + (1 + hh) * 1024, vo1, pts); glds(j0 + (1 + hh) * 1024 + 16 * 3072, vo1, pts + 1024);
      glds(j0 + hh * 16 * 3072, vo1, pts + 2048);
    }
    const int srow = cl * 128 + m * 8 + hh * 4 + (lane >> 4);
    v4u xs[4];
#pragma unroll
    for (int i = 0; i < 4; ++i) xs[i] = *(const GAS v4u*)((const GAS char*)x + (size_t)srow * 1024 + i * 256 + (lane & 15) * 16);
    const v2u e0 = __builtin_amdgcn_raw_buffer_load_b64(c.rs, 0, WS_E, 16);
    VMWAIT(0);
    const u64 tag = mix64(((u64)(unsigned)__builtin_amdgcn_readfirstlane(e0.y) << 32) | (unsigned)__builtin_amdgcn_readfirstlane(e0.x));
    const v2u tag2 = {(unsigned)tag, (unsigned)(tag >> 32)};
    if (hh == 0) *(volatile LAS v2u*)flg(c, F_TAG) = tag2;
    {
      const unsigned tb = ((unsigned)(mix64(tag) >> 32) >> ((lane & 3) * 8)) & 0xffu;
      const v4u f0 = tr_frag(L + pts + (hh ? trS : trN)), f1 = tr_frag(L + pts + 1024 + (hh ? trS : trN)), f2 = tr_frag(L + pts + 2048 + trN);
      const float s0 = red_h(csum(f1, csum(f0, 0.f))), s2 = red_h(csum(f2, 0.f));
      __builtin_amdgcn_raw_buffer_store_b32((f2u(h ? s2 : s0) & 0xffffff00u) | tb, c.rs, l31 * 4 + h * 4096, WS_C1P + m * 8 * KB + cl * 256 + hh * 128, 16);
      float sa = 0.f, sb = 0.f, qa = 0.f, qb = 0.f;
#pragma unroll
      for (int i = 0; i < 4; ++i) {
        sa = dot2(xs[i].y, ONES2, dot2(xs[i].x, ONES2, sa)); sb = dot2(xs[i].w, ONES2, dot2(xs[i].z, ONES2, sb));
        qa = dot2(xs[i].y, xs[i].y, dot2(xs[i].x, xs[i].x, qa)); qb = dot2(xs[i].w, xs[i].w, dot2(xs[i].z, xs[i].z, qb));
      }
      sa += sb; qa += qb;
      sa += dpp<0x128>(sa); qa += dpp<0x128>(qa); sa += dpp<0x124>(sa); qa += dpp<0x124>(qa);
      sa += dpp<0x122>(sa); qa += dpp<0x122>(qa); sa += dpp<0x121>(sa); qa += dpp<0x121>(qa);
      if ((lane & 15) < 2) st_ws(c, v4u{f2u(sa), tag2.x, f2u(qa), tag2.y}, srow * 32 + (lane & 1) * 16, WS_S0P);
      {
        const unsigned nt = ~(hh ? tag2.y : tag2.x);
        const v4u W = {nt, nt >> 8, nt >> 16, nt >> 24};
        const int kvb = WS_KVP + ((hh * 16 + m) * 16 + cl) * KVR;
        st_ws(c, W, lv, kvb); st_ws(c, W, lv + KB, kvb);
        if (lane < 8) st_ws(c, W, lv + 2 * KB, kvb);
      }
      VMWAIT(0);
      __builtin_amdgcn_s_sleep(8);
      {
        float t;
#pragma nounroll
        for (unsigned sp = 0; sp < SPIN_CAP; ++sp) {
          unsigned p[16];
#pragma unroll
          for (int i = 0; i < 16; ++i) p[i] = __builtin_amdgcn_raw_buffer_load_b32(c.rs, lane * 4 + i * 256, WS_C1P + m * 8 * KB + hh * 4096, 16);
          SB();
          unsigned chk = 0;
          t = 0.f;
#pragma unroll
          for (int i = 0; i < 16; ++i) { p[i] ^= tb; chk |= p[i]; t += u2f(p[i]); }
          if (!__any((chk & 0xffu) != 0u)) break;
          MEMBAR();
        }
        if (hh) t = red_h(t);
        if (hh == 0 || h == 0) *(LAS v4u*)(L + L_CS + hh * 1024 + lv) = cs_operand(t);
        MEMBAR();
        *flg(c, F_CS1R + hh) = 1u;
      }
      __builtin_amdgcn_s_setprio(1);
      wait_lds<2, 1>(c, F_QGO, 1u);
      const int pt3 = pts + 4096;
      const unsigned vo3 = (lane >> 2) * 2752 + (lane & 3) * 16;
      const char* w3 = (const char*)(hh ? wu : wg) + (size_t)(cl * 32) * 2752;
#pragma unroll
      for (int i = 0; i < 3; ++i) {
        const int aa = g0 + min(i, ng - 1);
        glds(w3 + aa * 64, vo3, pt3 + i * 2048);
        glds(w3 + aa * 64 + 16 * 2752, vo3, pt3 + i * 2048 + 1024);
      }
      st_ws(c, f0, lv, WS_WT1 + m * 96 * KB + (cl * 4 + hh) * KB);
      st_ws(c, f1, lv, WS_WT1 + m * 96 * KB + (cl * 4 + 2 + hh) * KB);
      st_ws(c, f2, lv, WS_WT1 + m * 96 * KB + (64 + cl * 2 + hh) * KB);
      VMWAIT(0);
#pragma unroll
      for (int i = 0; i < 3; ++i) {
        const int aa = g0 + min(i, ng - 1);
        const v4u g0f = tr_frag(L + pt3 + i * 2048 + trS), g1f = tr_frag(L + pt3 + i * 2048 + 1024 + trS);
        st_ws(c, g0f, lv, WS_WT3 + aa * 64 * KB + (cl * 4 + hh) * KB);
        st_ws(c, g1f, lv, WS_WT3 + aa * 64 * KB + (cl * 4 + 2 + hh) * KB);
        const float s = red_h(csum(g1f, csum(g0f, 0.f)));
        if (h == 0) __builtin_amdgcn_raw_buffer_store_b32(f2u(s), c.rs, l31 * 4, WS_CSP + aa * 4 * KB + cl * 256 + hh * 128, 16);
      }
      MEMBAR();
      *flg(c, F_JOB + hh) = 1u;
    }
    VMWAIT(0);
    if (lane < 16) __builtin_amdgcn_raw_buffer_store_b64(tag2, c.rs, lane * 8, WS_F1 + ((m * 16 + cl) * 2 + hh) * 128, 16);
    v4u wc[40];
    cache_load(wc, IC<5>{});
    cache_conv(wc, IC<5>{});
    if (hh == 1) {
      wait_lds<1, 2>(c, F_PTOK, 1u);
#pragma unroll 1
      for (int i = 0; i < ng; ++i) {
        float t = 0.f;
#pragma unroll
        for (int q = 0; q < 16; ++q) t += u2f(__builtin_amdgcn_raw_buffer_load_b32(c.rs, lane * 4 + q * 256, WS_CSP + (g0 + i) * 4 * KB, 16));
        *(LAS v4u*)(L + L_CS + (3 + 2 * i) * 512 + lv) = cs_operand(t);
      }
      MEMBAR();
      *flg(c, F_CS3R) = 1u;
      MEMBAR();
    }
    __builtin_amdgcn_s_setprio(1);
    cache_put(wc, IC<5>{}, 10 + 4 * ng);
    if (b == 0 && wave == 6 && lane == 0) __hip_atomic_store((GAS u64*)(ws + WS_E), tag, RLX_AGENT);
    __builtin_amdgcn_s_setprio(1);
    cache_put(wc, IC<5>{}, 31 + 8 * ng);
  }
}

extern "C" void kernel_launch(void* const* d_in, const int* in_sizes, int n_in, void* d_out, int out_size, void* d_ws, size_t ws_size,
                              hipStream_t stream) {
  fused_fwd<<<256, 512, 0, stream>>>((const u16*)d_in[0], (const u16*)d_in[1], (const u16*)d_in[2], (const u16*)d_in[4], (const u16*)d_in[5],
                                     (const u16*)d_in[6], (u16*)d_out, (char*)d_ws);
}
```
